```python
import torch

def _softmax(x, axis=-1):
    return torch.softmax(x, dim=axis)
def _gelu(x):
    return torch.nn.functional.gelu(x, approximate="tanh")
def _silu(x):
    return torch.nn.functional.silu(x)
def _sigmoid(x):
    return torch.sigmoid(x)
def _layernorm(x, axis=-1, eps=1e-5):
    m = x.mean(dim=axis, keepdim=True)
    v = x.var(dim=axis, keepdim=True, unbiased=False)
    return (x - m) / torch.sqrt(v + eps)
def _scatter_add(tgt, idx, src, axis=0):
    return tgt.clone().index_add_(axis, idx, src)
def _segment_sum(src, seg, n):
    out = torch.zeros((n,) + src.shape[1:], dtype=src.dtype)
    return out.index_add_(0, seg, src)
def _pad(x, pw):
    flat = tuple(v for pair in reversed(pw) for v in pair)
    return torch.nn.functional.pad(x, flat)

kernel_name = 'interleave_layers__spectral_fnet__glu_gated_glu__prompt'

def setup_inputs():

    _w = (lambda a: (lambda t: t.to(torch.bfloat16) if t.is_floating_point() else t)(torch.as_tensor(a)))
    def _A_setup():
        B, S, D = 1, 2048, 512
        ns = torch.arange(S)[:, None] * torch.arange(S)[None, :]
        nd = torch.arange(D)[:, None] * torch.arange(D)[None, :]
        Ws_re = torch.cos(-2.0 * torch.pi * ns / S).to(float)
        Ws_im = torch.sin(-2.0 * torch.pi * ns / S).to(float)
        Wd_re = torch.cos(-2.0 * torch.pi * nd / D).to(float)
        Wd_im = torch.sin(-2.0 * torch.pi * nd / D).to(float)
        return {
            "x": _w((lambda *s: torch.randn(*s, dtype=torch.bfloat16) * 0.05)(B, S, D)),
            "dft_seq_re": _w(Ws_re),
            "dft_seq_im": _w(Ws_im),
            "dft_hid_re": _w(Wd_re),
            "dft_hid_im": _w(Wd_im),
        }
    _A_in = _A_setup()

    def _B_setup():
        B, S, D, F = 1, 2048, 512, 1792
        return {
            "x": _w((lambda *s: torch.randn(*s, dtype=torch.bfloat16) * 0.05)(B, S, D)),
            "w_gate": _w((lambda *s: torch.randn(*s, dtype=torch.bfloat16) * 1.6)(D, F)),
            "w_up": _w((lambda *s: torch.randn(*s, dtype=torch.bfloat16) * 0.8)(D, F)),
            "w_down": _w((lambda *s: torch.randn(*s, dtype=torch.bfloat16) * 0.8)(F, D)),
        }
    _B_in = _B_setup()

    return {
        "A_x": _A_in["x"],
        "A_dft_seq_re": _A_in["dft_seq_re"],
        "A_dft_seq_im": _A_in["dft_seq_im"],
        "A_dft_hid_re": _A_in["dft_hid_re"],
        "A_dft_hid_im": _A_in["dft_hid_im"],
        "B_x": _B_in["x"],
        "B_w_gate": _B_in["w_gate"],
        "B_w_up": _B_in["w_up"],
        "B_w_down": _B_in["w_down"],
    }

def reference(A_x, A_dft_seq_re, A_dft_seq_im, A_dft_hid_re, A_dft_hid_im, B_x, B_w_gate, B_w_up, B_w_down):
    def _A_ref(x, dft_seq_re, dft_seq_im, dft_hid_re, dft_hid_im):
        a = torch.einsum("bsd,ed->bse", x, dft_hid_re)
        b = torch.einsum("bsd,ed->bse", x, dft_hid_im)
        y = torch.einsum("ts,bse->bte", dft_seq_re, a) - torch.einsum("ts,bse->bte", dft_seq_im, b)
        return y

    def _B_ref(x, w_gate, w_up, w_down):
        g = torch.einsum("bsd,df->bsf", x, w_gate)
        u = torch.einsum("bsd,df->bsf", x, w_up)
        h = _sigmoid(g) * u
        return torch.einsum("bsf,fd->bsd", h, w_down)

    x = A_x
    x = x + _A_ref(_layernorm(x, axis=-1), A_dft_seq_re, A_dft_seq_im, A_dft_hid_re, A_dft_hid_im)
    x = x + _B_ref(_layernorm(x, axis=-1), B_w_gate, B_w_up, B_w_down)
    x = x + _A_ref(_layernorm(x, axis=-1), A_dft_seq_re, A_dft_seq_im, A_dft_hid_re, A_dft_hid_im)
    x = x + _B_ref(_layernorm(x, axis=-1), B_w_gate, B_w_up, B_w_down)
    return x
```

```cpp
#include <hip/hip_runtime.h>
#include <hip/hip_bf16.h>
#include <stdint.h>

typedef unsigned short u16;
typedef short bf16x8 __attribute__((ext_vector_type(8)));
typedef float f32x4 __attribute__((ext_vector_type(4)));

static constexpr int S = 2048, D = 512, F = 1792;

__device__ __forceinline__ float bf2f(u16 v) { return __uint_as_float(((unsigned)v) << 16); }
__device__ __forceinline__ u16 f2bf(float f) { __hip_bfloat16 b = __float2bfloat16(f); return *reinterpret_cast<u16*>(&b); }

__global__ void ln_kernel(const u16* __restrict__ x, u16* __restrict__ xn) {
    int row = blockIdx.x * 4 + (threadIdx.x >> 6), lane = threadIdx.x & 63;
    const u16* p = x + (size_t)row * D + lane * 8;
    float v[8]; float s = 0.f;
    for (int j = 0; j < 8; ++j) { v[j] = bf2f(p[j]); s += v[j]; }
    for (int o = 32; o; o >>= 1) s += __shfl_xor(s, o);
    float m = s * (1.0f / D); float q = 0.f;
    for (int j = 0; j < 8; ++j) { float d = v[j] - m; q += d * d; }
    for (int o = 32; o; o >>= 1) q += __shfl_xor(q, o);
    float r = 1.0f / sqrtf(q * (1.0f / D) + 1e-5f);
    for (int j = 0; j < 8; ++j) xn[(size_t)row * D + lane * 8 + j] = f2bf((v[j] - m) * r);
}

template <int MODE>
__global__ __launch_bounds__(256) void gemm_kernel(const u16* __restrict__ A, int sam, int sak,
        const u16* __restrict__ B, const u16* __restrict__ B2, int sbn, int sbk,
        const u16* __restrict__ A2, const u16* __restrict__ Bk2, int K, int K2,
        const u16* __restrict__ res, u16* __restrict__ C, int ldc, float alpha) {
    __shared__ __attribute__((aligned(16))) u16 sA[64][40], sB[64][40], sB2[64][40];
    int tid = threadIdx.x, lane = tid & 63, wid = tid >> 6;
    int m0 = blockIdx.y * 64, n0 = blockIdx.x * 64;
    int wr = wid >> 1, wc = wid & 1;
    f32x4 acc[2][2] = {}, acc2[2][2] = {};
    for (int pass = 0; pass < (K2 ? 2 : 1); ++pass) {
        const u16* Ap = pass ? A2 : A; const u16* Bp = pass ? Bk2 : B; int Kp = pass ? K2 : K;
        for (int k0 = 0; k0 < Kp; k0 += 32) {
            for (int i = tid; i < 64 * 32; i += 256) {
                int r = i >> 5, c = i & 31;
                sA[r][c] = Ap[(size_t)(m0 + r) * sam + (size_t)(k0 + c) * sak];
                sB[r][c] = Bp[(size_t)(n0 + r) * sbn + (size_t)(k0 + c) * sbk];
                if (MODE == 2) sB2[r][c] = B2[(size_t)(n0 + r) * sbn + (size_t)(k0 + c) * sbk];
            }
            __syncthreads();
            for (int m = 0; m < 2; ++m) for (int n = 0; n < 2; ++n) {
                bf16x8 a = *(const bf16x8*)&sA[wr * 32 + m * 16 + (lane & 15)][(lane >> 4) * 8];
                bf16x8 b = *(const bf16x8*)&sB[wc * 32 + n * 16 + (lane & 15)][(lane >> 4) * 8];
                acc[m][n] = __builtin_amdgcn_mfma_f32_16x16x32_bf16(a, b, acc[m][n], 0, 0, 0);
                if (MODE == 2) {
                    bf16x8 b2 = *(const bf16x8*)&sB2[wc * 32 + n * 16 + (lane & 15)][(lane >> 4) * 8];
                    acc2[m][n] = __builtin_amdgcn_mfma_f32_16x16x32_bf16(a, b2, acc2[m][n], 0, 0, 0);
                }
            }
            __syncthreads();
        }
    }
    for (int m = 0; m < 2; ++m) for (int n = 0; n < 2; ++n) for (int j = 0; j < 4; ++j) {
        int r = m0 + wr * 32 + m * 16 + (lane >> 4) * 4 + j, c = n0 + wc * 32 + n * 16 + (lane & 15);
        float v = acc[m][n][j];
        if (MODE == 0) v *= alpha;
        if (MODE == 1) v += bf2f(res[(size_t)r * ldc + c]);
        if (MODE == 2) { float g = v, u = acc2[m][n][j]; v = u / (1.0f + __expf(-g)); }
        C[(size_t)r * ldc + c] = f2bf(v);
    }
}

extern "C" void kernel_launch(void* const* d_in, const int* in_sizes, int n_in,
                              void* d_out, int out_size, void* d_ws, size_t ws_size,
                              hipStream_t stream) {
    const u16* x = (const u16*)d_in[0];
    const u16* ws_re = (const u16*)d_in[1];
    const u16* ws_im = (const u16*)d_in[2];
    const u16* wd_re = (const u16*)d_in[3];
    const u16* wd_im = (const u16*)d_in[4];
    const u16* wg = (const u16*)d_in[6];
    const u16* wu = (const u16*)d_in[7];
    const u16* wdn = (const u16*)d_in[8];
    u16* w = (u16*)d_ws;
    u16* xn = w; w += S * D;
    u16* a = w; w += S * D;
    u16* bneg = w; w += S * D;
    u16* x1 = w; w += S * D;
    u16* x2 = w; w += S * D;
    u16* h = w; w += S * F;
    const u16* xin = x;
    for (int layer = 0; layer < 2; ++layer) {
        u16* xo = layer ? (u16*)d_out : x2;
        ln_kernel<<<S / 4, 256, 0, stream>>>(xin, xn);
        gemm_kernel<0><<<dim3(D / 64, S / 64), 256, 0, stream>>>(xn, D, 1, wd_re, nullptr, D, 1, nullptr, nullptr, D, 0, nullptr, a, D, 1.0f);
        gemm_kernel<0><<<dim3(D / 64, S / 64), 256, 0, stream>>>(xn, D, 1, wd_im, nullptr, D, 1, nullptr, nullptr, D, 0, nullptr, bneg, D, -1.0f);
        gemm_kernel<1><<<dim3(D / 64, S / 64), 256, 0, stream>>>(ws_re, S, 1, a, nullptr, 1, D, ws_im, bneg, S, S, xin, x1, D, 1.0f);
        ln_kernel<<<S / 4, 256, 0, stream>>>(x1, xn);
        gemm_kernel<2><<<dim3(F / 64, S / 64), 256, 0, stream>>>(xn, D, 1, wg, wu, 1, F, nullptr, nullptr, D, 0, nullptr, h, F, 1.0f);
        gemm_kernel<1><<<dim3(D / 64, S / 64), 256, 0, stream>>>(h, F, 1, wdn, nullptr, 1, D, nullptr, nullptr, F, 0, x1, xo, D, 1.0f);
        xin = xo;
    }
}
```

```cpp
#include <hip/hip_runtime.h>
#include <hip/hip_bf16.h>
#include <stdint.h>
#include <type_traits>
#include <utility>

#define DI __device__ __forceinline__
#define LAS __attribute__((address_space(3)))
#define GAS __attribute__((address_space(1)))
typedef short bf16x8 __attribute__((ext_vector_type(8)));
typedef short s16x4 __attribute__((ext_vector_type(4)));
typedef float f32x4 __attribute__((ext_vector_type(4)));
typedef float f32x2 __attribute__((ext_vector_type(2)));
typedef float f32x16 __attribute__((ext_vector_type(16)));
typedef unsigned u32x4 __attribute__((ext_vector_type(4)));
typedef unsigned u32x2 __attribute__((ext_vector_type(2)));
typedef __bf16 bf16x2 __attribute__((ext_vector_type(2)));
typedef unsigned long long u64;
typedef __amdgpu_buffer_rsrc_t rsrc_t;

#define WARM 2
namespace fn {

constexpr int S = 2048, D = 512, F = 1792;
constexpr unsigned MB = 1u << 20;
constexpr unsigned WS_FLAGS = 4096;
constexpr unsigned WS_ABT = 1 * MB;
constexpr unsigned WS_X1 = 9 * MB;
constexpr unsigned WS_X2 = 13 * MB;
constexpr unsigned WS_H = 15 * MB;
constexpr unsigned WS_ST = 29 * MB;
constexpr unsigned WS_STAMP = 30 * MB;
constexpr unsigned WS_PART = 32 * MB;
constexpr unsigned ST_BYTES = 128 * 1024;

constexpr unsigned REG = 32768;
constexpr unsigned LDS_M = 4 * REG;
constexpr unsigned T_RES = 4096;
constexpr unsigned T_STAT = 6144;
constexpr unsigned T_CS1 = LDS_M + 6400;
constexpr unsigned T_CS3 = LDS_M + 8192 + 6400;
constexpr unsigned T_CS3P = LDS_M + 16384 + 6400;
constexpr unsigned T_DUMMY = LDS_M + 24576 + 6400;
constexpr unsigned LDS_BYTES = 163840;
constexpr int CODE_PREFETCH_BYTES = 0;
constexpr unsigned SPIN_LIMIT = 200000;
constexpr float EPS = 1e-5f;

#define WDEPTH 2
#define WDELAY 0
DI rsrc_t mk(const void* p, unsigned bytes) { return __builtin_amdgcn_make_buffer_rsrc((void*)p, 0, bytes, 0x00020000); }
DI void dma(unsigned lds, int LI, unsigned voff, rsrc_t r, unsigned sb, int SI) {
    unsigned t;
    asm volatile("s_add_u32 m0, %1, %5\n\ts_add_u32 %0, %4, %6\n\tbuffer_load_dwordx4 %2, %3, %0 offen lds"
                 : "=&s"(t) : "s"(lds), "v"(voff), "s"(r), "s"(sb), "i"(LI), "i"(SI) : "m0", "scc", "memory");
}
DI void aload(bf16x8& d, unsigned voff, rsrc_t r, unsigned sb, int SI) {
    asm volatile("buffer_load_dwordx4 %0, %1, %2, %3 offen offset:%4 sc1"
                 : "=v"(d) : "v"(voff), "s"(r), "s"(sb + (SI & ~4095)), "i"(SI & 4095) : "memory");
}
DI void aload2(u32x2& d, unsigned voff, rsrc_t r, unsigned sb) {
    asm volatile("buffer_load_dwordx2 %0, %1, %2, %3 offen sc1" : "=v"(d) : "v"(voff), "s"(r), "s"(sb) : "memory");
}
template <int... Is, class Fn> DI void sfor_impl(std::integer_sequence<int, Is...>, Fn&& f) { (f(std::integral_constant<int, Is>{}), ...); }
template <int N, class Fn> DI void sfor(Fn&& f) { sfor_impl(std::make_integer_sequence<int, N>{}, f); }
#define CI(x) decltype(x)::value
DI int lane_id() { int l; asm volatile("v_mbcnt_lo_u32_b32 %0, -1, 0\n\tv_mbcnt_hi_u32_b32 %0, -1, %0" : "=v"(l)); return l; }
DI int opq_s(int v) { asm volatile("" : "+s"(v)); return v; }
template <int N> DI void wait_vm() { asm volatile("s_waitcnt vmcnt(%0)" :: "i"(N) : "memory"); }
template <int N> DI void wait2(bf16x8& a, bf16x8& b) { asm volatile("s_waitcnt vmcnt(%2)" : "+v"(a), "+v"(b) : "i"(N) : "memory"); }
template <int N> DI void wait4(bf16x8& a, bf16x8& b, bf16x8& c, bf16x8& d) {
    asm volatile("s_waitcnt vmcnt(%4)" : "+v"(a), "+v"(b), "+v"(c), "+v"(d) : "i"(N) : "memory");
}
template <int N> DI void wait8(bf16x8 (&a)[8]) {
    asm volatile("s_waitcnt vmcnt(%8)" : "+v"(a[0]), "+v"(a[1]), "+v"(a[2]), "+v"(a[3]), "+v"(a[4]), "+v"(a[5]), "+v"(a[6]), "+v"(a[7]) : "i"(N) : "memory");
}
DI void wait_lds() { asm volatile("s_waitcnt lgkmcnt(0)" ::: "memory"); }
DI void bar() { asm volatile("" ::: "memory"); __builtin_amdgcn_s_barrier(); asm volatile("" ::: "memory"); }
#define SCHED() __builtin_amdgcn_sched_barrier(0)
DI f32x4 mfma(bf16x8 a, bf16x8 b, f32x4 c) { return __builtin_amdgcn_mfma_f32_16x16x32_bf16(a, b, c, 0, 0, 0); }
DI f32x16 mfma32(bf16x8 a, bf16x8 b, f32x16 c) { return __builtin_amdgcn_mfma_f32_32x32x16_bf16(a, b, c, 0, 0, 0); }
DI unsigned pk(float lo, float hi) { f32x2 v = {lo, hi}; return __builtin_bit_cast(unsigned, __builtin_convertvector(v, bf16x2)); }
DI float lo16(unsigned u) { return __uint_as_float(u << 16); }
DI float hi16(unsigned u) { return __uint_as_float(u & 0xffff0000u); }
DI float sum16(float v) { auto r = __builtin_amdgcn_permlane16_swap(__float_as_uint(v), __float_as_uint(v), false, false); return __uint_as_float(r[0]) + __uint_as_float(r[1]); }
DI float sum32(float v) { auto r = __builtin_amdgcn_permlane32_swap(__float_as_uint(v), __float_as_uint(v), false, false); return __uint_as_float(r[0]) + __uint_as_float(r[1]); }
DI float sumq(float v) { return sum32(sum16(v)); }
DI u32x4 gather8(unsigned x0, unsigned x1, unsigned y0, unsigned y1) {
    auto a = __builtin_amdgcn_permlane32_swap(x0, y0, false, false);
    auto b = __builtin_amdgcn_permlane32_swap(x1, y1, false, false);
    auto c = __builtin_amdgcn_permlane16_swap(a[0], a[1], false, false);
    auto d = __builtin_amdgcn_permlane16_swap(b[0], b[1], false, false);
    return (u32x4){c[0], d[0], c[1], d[1]};
}
DI void store16(u32x4 v, rsrc_t r, unsigned voff) { __builtin_amdgcn_raw_buffer_store_b128(v, r, voff, 0, 16); }
DI bf16x8 ldsA(LAS char* L, unsigned off) { return *(LAS bf16x8*)(L + off); }
DI bf16x8 ldsT(LAS char* L, unsigned off) {
    s16x4 lo = __builtin_amdgcn_ds_read_tr16_b64_v4i16((LAS s16x4*)(L + off));
    s16x4 hi = __builtin_amdgcn_ds_read_tr16_b64_v4i16((LAS s16x4*)(L + off + 512));
    return __builtin_shufflevector(lo, hi, 0, 1, 2, 3, 4, 5, 6, 7);
}
DI int blk_p24(int rt, int ct) { return 8 * (4 * ((rt >> 1) & 7) + (ct >> 1)) + 4 * (rt >> 4) + 2 * (rt & 1) + (ct & 1); }
DI int blk_p1(int rt1, int et) { return 8 * (8 * (rt1 & 3) + (et & 7)) + 2 * (rt1 >> 2) + (et >> 3); }
DI int blk_p3(int RT, int CT) { return 8 * (8 * (RT & 3) + (CT & 7)) + 2 * (CT >> 3) + (RT >> 2); }

DI void poll8(rsrc_t rw, int phase, int prod, u64 tag, bool& dead) {
    const unsigned voff = (unsigned)prod * 256u, sb = WS_FLAGS + (unsigned)phase * 65536u;
    if (!dead) {
        unsigned spins = 0;
        for (;;) {
            u64 v;
            asm volatile("buffer_load_dwordx2 %0, %1, %2, %3 offen sc1\n\ts_waitcnt vmcnt(0)" : "=v"(v) : "v"(voff), "s"(rw), "s"(sb) : "memory");
            if (__all(v == tag)) break;
            if (++spins > SPIN_LIMIT) { dead = true; break; }
        }
    }
    asm volatile("" ::: "memory");
}
DI void publish(rsrc_t rw, int phase, int blk, u64 tag, int w) {
    if (w == 0)
        __builtin_amdgcn_raw_buffer_store_b64((u32x2){(unsigned)tag, (unsigned)(tag >> 32)}, rw, WS_FLAGS + (unsigned)(phase * 256 + blk) * 256u, 0, 16);
}

#define STAMP(n) do {} while (0)

constexpr int P1_LA = 12;
constexpr int p1_issued(int ks) { return 2 * P1_LA + 2 * (ks < 16 - P1_LA ? ks : 16 - P1_LA); }
constexpr int p1_wait(int ks) { return p1_issued(ks) - 1 - (ks < P1_LA ? 2 * ks + 1 : p1_issued(ks - P1_LA + 1) - 1); }
constexpr int P2_LA = 22;
constexpr int p2_dma(int v) { return (v >= 8 && v <= 39) || (v >= 43 && v <= 58) ? 1 : 0; }
constexpr int p2_ops(int v) { return p2_dma(v) + (v + P2_LA < 64 ? 1 : 0); }
constexpr int p2_issued(int v) { int n = P2_LA; for (int j = 0; j < v; ++j) n += p2_ops(j); return n; }
constexpr int p2_wait(int v) {
    const int pa = v < P2_LA ? v : p2_issued(v - P2_LA + 1) - 1;
    const int c = (v >> 2) + 1;
    const int pd = (v & 3) == 2 && c >= 8 && c < 16 ? p2_issued(4 * (c - 8 + 2) + 3) : -1;
    return p2_issued(v) - 1 - (pa > pd ? pa : pd);
}
constexpr int P3_LA = 6;
constexpr int p3_ops(int g) { return (g + P3_LA < 32 ? 4 : 0) + (g == 8 ? 8 : 0) + (g == 15 ? 4 : 0) + (g == 30 ? 1 : 0); }
constexpr int p3_issued(int g) { int n = 4 * P3_LA; for (int j = 0; j < g; ++j) n += p3_ops(j); return n; }
constexpr int p3_xlast(int j) { return p3_issued(j) + 3; }
constexpr int p3_wait(int g) { return p3_issued(g) - 1 - (g < P3_LA ? 4 * g + 3 : p3_xlast(g - P3_LA)); }
constexpr int P3_PUB = 19;
constexpr int p3_wait_pub() { return p3_issued(P3_PUB) - p3_issued(16); }
constexpr int p4_ops(int u) { return (u == 1 ? 1 : 0) + (u >= 1 && u <= 6 ? 4 : 0) + (u <= 8 ? 4 : 0); }
constexpr int p4_issued(int u) { int n = 40; for (int j = 0; j < u; ++j) n += p4_ops(j); return n; }
constexpr int p4_wait(int u) {
    int p = u < 5 ? 4 * u + 3 : p4_issued(u - 4) - 1;
    if (u == 1 && p < 23) p = 23;
    if (u == 3 && p < p4_issued(1)) p = p4_issued(1);
    if ((u & 1) && u < 13) {
        const int c = (u + 1) >> 1;
        const int pd = c == 2 ? 31 : c == 3 ? 39 : c >= 4 ? p4_issued(2 * c - 6) + (2 * c - 6 == 1 ? 1 : 0) + 6 : -1;
        if (pd > p) p = pd;
    }
    return p4_issued(u) - 1 - p;
}
constexpr int p4_row(int u, int qq) { return u < 8 ? 56 * u + 8 * qq : 56 * ((4 * (u - 8) + qq) / 3) + 32 + 8 * ((4 * (u - 8) + qq) % 3); }

template <int N> DI void combine_stats(const u32x2 (&a)[N], const u32x2 (&b)[N], float (&mean)[N], float (&var)[N]) {
    float s[N], t[N];
#pragma unroll
    for (int i = 0; i < N; ++i) { s[i] = __uint_as_float(a[i][0]) + __uint_as_float(b[i][0]); t[i] = __uint_as_float(a[i][1]) + __uint_as_float(b[i][1]); }
#pragma unroll
    for (int i = 0; i < N; ++i) { s[i] = sum16(s[i]); t[i] = sum16(t[i]); }
#pragma unroll
    for (int i = 0; i < N; ++i) { s[i] = sum32(s[i]); t[i] = sum32(t[i]); }
#pragma unroll
    for (int i = 0; i < N; ++i) { mean[i] = s[i] * (1.0f / 512.0f); var[i] = fmaxf(t[i] * (1.0f / 512.0f) - mean[i] * mean[i], 0.f); }
}

__global__ __launch_bounds__(256, 2) void fnet_glu_kernel(const char* __restrict__ x, const char* __restrict__ wsre, const char* __restrict__ wsim,
        const char* __restrict__ wdre, const char* __restrict__ wdim_, const char* __restrict__ wg, const char* __restrict__ wu,
        const char* __restrict__ wdn, char* __restrict__ out, char* __restrict__ ws) {
    extern __shared__ __attribute__((aligned(16))) char smem[];
    LAS char* L = (LAS char*)smem;
    int wid;
    asm volatile("s_nop 1\n\tv_readfirstlane_b32 %0, %1\n\ts_nop 4" : "=s"(wid) : "v"(threadIdx.x >> 6));
    const int blk = blockIdx.x, xcd = blk & 7, idx = blk >> 3;

    const unsigned ldsb = (unsigned)(size_t)L;
    const rsrc_t rw = mk(ws, 48 * MB);
    const int T2 = 8 * (xcd >> 2) + (idx >> 2), E2 = idx & 3, j2 = xcd & 3;
    const int rt = 2 * T2 + (j2 >> 1), ct = 2 * E2 + (j2 & 1);
    const int rt1 = 4 * (xcd >> 1) + (idx >> 3), et = 8 * (xcd & 1) + (idx & 7);
    const int RT = 4 * (xcd & 1) + (idx >> 3), CT = 8 * (xcd >> 1) + (idx & 7);
    STAMP(0);

    auto p1_rsrc = [&](unsigned bytes) { return mk(((et >> 3) ? wdim_ : wdre) + (size_t)(et & 7) * 64 * 1024, bytes); };
    auto dma_p1 = [&](int j, int p, rsrc_t r, unsigned v0, unsigned lw, unsigned sb) {
        dma(lw, (32 * j + p) * 1024, v0 ^ (16 * p), r, sb, (32 * j + p) * 1024);
    };
    auto p3_voff = [&](int ln, int up, int odd) -> unsigned {
        const unsigned c = (ln & 7) ^ ((2 * ((ln >> 4) & 1) + 4 * odd) ^ up);
        return (ln >> 3) * 3584 + 16 * (c > 6 ? 6 : c);
    };
    auto dma_p3 = [&](int j, int p, unsigned v, unsigned lw, unsigned sb) {
        dma(lw, j * REG + p * 1024, v, mk((j >> 1) ? wu : wg, D * F * 2), sb, (256 * (j & 1) + 8 * p) * 3584);
    };
    auto dma_p2n = [&](int c0, int c1) {
        const int w = opq_s(wid), ln = lane_id();
        const rsrc_t r = mk(((j2 >> 1) ? wsim : wsre) + ((size_t)128 * T2 * S + 1024 * (j2 & 1)) * 2, (128 * S - 1024 * (j2 & 1)) * 2);
        const unsigned vE = (ln >> 3) * 4096 + (((ln & 7) ^ (ln >> 4)) * 16), vO = vE ^ 64;
#pragma unroll
        for (int c = c0; c < c1; ++c)
#pragma unroll
            for (int p = 0; p < 4; ++p) dma(ldsb + w * 4096, (c & 7) * 16384 + p * 1024, (p & 1) ? vO : vE, r, 32 * w * 4096, p * 32768 + c * 128);
    };
    auto dma_p1_all = [&]() {
        const int w = opq_s(wid), ln = lane_id();
        const rsrc_t r = p1_rsrc(65536);
        const unsigned v0 = (unsigned)(ln * 16) ^ (128 * (w & 1)), lw = ldsb + w * 8192;
#pragma unroll
        for (int j = 0; j < 2; ++j)
#pragma unroll
            for (int p = 0; p < 8; ++p) dma_p1(j, p, r, v0, lw, w * 8192);
    };
    auto warm_ffn = [&]() {
        const int w = opq_s(wid), ln = lane_id();
        const unsigned o = blk * 7168u + (ln < 56 ? ln : 55) * 128u, l = ldsb + T_DUMMY + w * 256;
        auto touch = [&](rsrc_t r) {
            asm volatile("s_mov_b32 m0, %0\n\ts_nop 0\n\tbuffer_load_dword %1, %2, 0 offen lds" :: "s"(l), "v"(o), "s"(r) : "m0", "memory");
        };
        if (w == 0) touch(mk(wg, D * F * 2));
        if (w == 1) touch(mk(wu, D * F * 2));
        if (w == 2) touch(mk(wdn, D * F * 2));
    };
    dma_p1_all();

    u32x2 ev;
    aload2(ev, 0u, rw, 0u);
    u64 epoch = 0;
    bool dead = false;
    const bf16x8 ones = {0x3F80, 0x3F80, 0x3F80, 0x3F80, 0x3F80, 0x3F80, 0x3F80, 0x3F80};

        auto phase1 = [&](auto first_tag) {
            constexpr bool FIRST = decltype(first_tag)::value;
            constexpr int layer = FIRST ? 0 : 1;
            const int w = opq_s(wid), lane = lane_id(), i16 = lane & 15, q = lane >> 4;
            const int sbg = 8 * rt1 + 2 * w;
            bf16x8 xa[2][16];
            u32x2 sa[2], sb_[2];
            f32x4 acc[2][4];
#pragma unroll
            for (int s = 0; s < 2; ++s)
#pragma unroll
                for (int e = 0; e < 4; ++e) acc[s][e] = (f32x4){0.f, 0.f, 0.f, 0.f};
            unsigned wb[4];
#pragma unroll
            for (int k = 0; k < 4; ++k) wb[k] = i16 * 1024 + (((4 * k + q) ^ i16) * 16);
            LAS f32x2* tst = (LAS f32x2*)(L + LDS_M + w * 8192 + T_STAT);
            const float sgn = (et >> 3) ? -1.0f : 1.0f;
            f32x4 aS[2], aQ[2], aC = {0.f, 0.f, 0.f, 0.f};
#pragma unroll
            for (int s = 0; s < 2; ++s) { aS[s] = (f32x4){0.f, 0.f, 0.f, 0.f}; aQ[s] = aS[s]; }
            const unsigned wown = w * 16384;
            const unsigned vo = lane * 16, sbase = WS_X2 + sbg * 16384;
            if constexpr (FIRST) {
                const rsrc_t rx = mk(x, S * D * 2);
                bf16x8 xr[32];
#pragma unroll
                for (int r = 0; r < 32; ++r) aload(xr[r], vo, rx, sbg * 16384, r * 1024);
                LAS char* T0 = L + 2 * REG + w * 8192;
                sfor<2>([&](auto SB) {
                    constexpr int s = CI(SB);
                    sfor<4>([&](auto G) {
                        constexpr int g = CI(G), r0 = 16 * s + 4 * g;
                        wait4<28 - r0>(xr[r0], xr[r0 + 1], xr[r0 + 2], xr[r0 + 3]);
#pragma unroll
                        for (int r = 0; r < 4; ++r)
                            *(LAS bf16x8*)(T0 + ((4 * g + r) >> 3) * REG + ((4 * g + r) & 7) * 1024 + ((lane * 16) ^ ((4 * g + r) * 16))) = xr[r0 + r];
                    });
#pragma unroll
                    for (int ks = 0; ks < 16; ++ks)
                        xa[s][ks] = *(LAS bf16x8*)(T0 + (i16 >> 3) * REG + (i16 & 7) * 1024 + (((4 * ks + q) ^ i16) * 16));
                    wait_lds();
                });
                asm volatile("" : "+v"(ev));
                epoch = ((u64)(unsigned)__builtin_amdgcn_readfirstlane((int)ev[1]) << 32) | (unsigned)__builtin_amdgcn_readfirstlane((int)ev[0]);
                wait_lds();
                bar();
                STAMP(1);
                dma_p2n(4, 8);
                warm_ffn();
            } else {
                poll8(rw, 5, blk_p24(2 * rt1 + (w >> 1), lane & 7), epoch + 6, dead);
                STAMP(17);
#pragma unroll
                for (int s = 0; s < 2; ++s) {
                    const unsigned so = WS_ST + 2 * ST_BYTES + (sbg + s) * 1024;
                    aload2(sa[s], (2 * q * 16 + i16) * 8, rw, so);
                    aload2(sb_[s], ((2 * q + 1) * 16 + i16) * 8, rw, so);
                }
#pragma unroll
                for (int ks = 0; ks < P1_LA; ++ks)
#pragma unroll
                    for (int s = 0; s < 2; ++s) aload(xa[s][ks], vo, rw, sbase, s * 16384 + ks * 1024);
                wait2<p1_wait(0)>(xa[0][0], xa[1][0]);
                asm volatile("" : "+v"(sa[0]), "+v"(sa[1]), "+v"(sb_[0]), "+v"(sb_[1]));
            }
            bf16x8 wf[2][4], wo;
#pragma unroll
            for (int e = 0; e < 4; ++e) wf[0][e] = ldsA(L, wb[0] + e * 16384);
            sfor<16>([&](auto KS) {
                constexpr int ks = CI(KS);
                if constexpr (!FIRST && ks > 0) wait2<p1_wait(ks)>(xa[0][ks], xa[1][ks]);
                if constexpr (FIRST) wo = ldsA(L, wb[ks & 3] + wown + 256 * (ks >> 2));
                sfor<4>([&](auto E) {
                    constexpr int e = CI(E);
                    acc[0][e] = mfma(xa[0][ks], wf[ks & 1][e], acc[0][e]);
                    acc[1][e] = mfma(xa[1][ks], wf[ks & 1][e], acc[1][e]);
                    SCHED();
                    if constexpr (ks < 15) wf[(ks + 1) & 1][e] = ldsA(L, wb[(ks + 1) & 3] + e * 16384 + 256 * ((ks + 1) >> 2));
                    if constexpr (!FIRST && e >= 2 && ks + P1_LA < 16) aload(xa[e - 2][ks + P1_LA], vo, rw, sbase, (e - 2) * 16384 + (ks + P1_LA) * 1024);
                    SCHED();
                });
                if constexpr (FIRST) {
                    aC = mfma(ones, wo, aC);
#pragma unroll
                    for (int s = 0; s < 2; ++s) { aS[s] = mfma(ones, xa[s][ks], aS[s]); aQ[s] = mfma(xa[s][ks], xa[s][ks], aQ[s]); }
                    SCHED();
                }
            });
            if constexpr (FIRST) {
                ((LAS float*)(L + T_CS1))[16 * w + i16] = aC[0];
#pragma unroll
                for (int s = 0; s < 2; ++s) {
                    const int r3 = i16 & 3;
                    float dq = r3 == 0 ? aQ[s][0] : r3 == 1 ? aQ[s][1] : r3 == 2 ? aQ[s][2] : aQ[s][3];
                    dq = (q == (i16 >> 2)) ? dq : 0.f;
                    const float mean = aS[s][0] * (1.0f / 512.0f);
                    const float var = fmaxf(sumq(dq) * (1.0f / 512.0f) - mean * mean, 0.f);
                    tst[16 * s + i16] = (f32x2){mean, sgn * __builtin_amdgcn_rsqf(var + EPS)};
                }
            } else {
                float mean[2], var[2];
                combine_stats<2>(sa, sb_, mean, var);
#pragma unroll
                for (int s = 0; s < 2; ++s) tst[16 * s + i16] = (f32x2){mean[s], sgn * __builtin_amdgcn_rsqf(var[s] + EPS)};
            }
            wait_lds();
            bar();
            STAMP(2 + 16 * layer);
            f32x4 st0[2], st1[2];
#pragma unroll
            for (int s = 0; s < 2; ++s) {
                const f32x4 a = *(LAS f32x4*)(tst + 16 * s + 4 * q), b = *(LAS f32x4*)(tst + 16 * s + 4 * q + 2);
                st0[s] = (f32x4){a[0], a[2], b[0], b[2]}; st1[s] = (f32x4){a[1], a[3], b[1], b[3]};
            }
#pragma unroll
            for (int E = 0; E < 2; ++E) {
                const float cs0 = ((LAS float*)(L + T_CS1))[32 * E + i16], cs1 = ((LAS float*)(L + T_CS1))[32 * E + 16 + i16];
#pragma unroll
                for (int s = 0; s < 2; ++s) {
                    f32x4 v0, v1;
#pragma unroll
                    for (int r = 0; r < 4; ++r) {
                        v0[r] = (acc[s][2 * E][r] - st0[s][r] * cs0) * st1[s][r];
                        v1[r] = (acc[s][2 * E + 1][r] - st0[s][r] * cs1) * st1[s][r];
                    }
                    auto a = __builtin_amdgcn_permlane16_swap(pk(v0[0], v0[1]), pk(v1[0], v1[1]), false, false);
                    auto b = __builtin_amdgcn_permlane16_swap(pk(v0[2], v0[3]), pk(v1[2], v1[3]), false, false);
                    store16((u32x4){a[0], b[0], a[1], b[1]}, rw,
                            WS_ABT + layer * 4 * MB + (et >> 3) * 2 * MB + ((2 * (et & 7) + E) * 128 + sbg + s) * 1024 + lane * 16);
                }
            }
        };

    phase1(std::true_type{});
    for (int layer = 0; layer < 2; ++layer) {
        const u64 tag = epoch + 1 + 6 * layer;
        auto finish_strip = [&](f32x4 (&v)[4], bool last, unsigned xo, unsigned sto) {
            const int w = opq_s(wid), lane = lane_id(), i16 = lane & 15, q = lane >> 4;
            float sum = 0.f, sq = 0.f;
            u32x2 res[4];
            {
                const u32x4 r0 = *(LAS u32x4*)(L + LDS_M + w * 8192 + T_RES + lane * 16), r1 = *(LAS u32x4*)(L + LDS_M + w * 8192 + T_RES + 1024 + lane * 16);
                res[0] = (u32x2){r0[0], r0[1]}; res[1] = (u32x2){r0[2], r0[3]}; res[2] = (u32x2){r1[0], r1[1]}; res[3] = (u32x2){r1[2], r1[3]};
            }
#pragma unroll
            for (int b = 0; b < 4; ++b) {
                res[b][0] = pk(lo16(res[b][0]) + v[b][0], hi16(res[b][0]) + v[b][1]);
                res[b][1] = pk(lo16(res[b][1]) + v[b][2], hi16(res[b][1]) + v[b][3]);
                const float x0 = lo16(res[b][0]), x1 = hi16(res[b][0]), x2 = lo16(res[b][1]), x3 = hi16(res[b][1]);
                sum += (x0 + x1) + (x2 + x3);
                sq += (x0 * x0 + x1 * x1) + (x2 * x2 + x3 * x3);
            }
            const u32x4 o0 = gather8(res[0][0], res[0][1], res[1][0], res[1][1]);
            const u32x4 o1 = gather8(res[2][0], res[2][1], res[3][0], res[3][1]);
            if (last) {
                const rsrc_t rxo = mk(out, S * D * 2);
                const unsigned a = (64 * rt + 16 * w + i16) * 1024 + (64 * ct + 8 * q) * 2;
                __builtin_amdgcn_raw_buffer_store_b128(o0, rxo, a, 0, 0);
                __builtin_amdgcn_raw_buffer_store_b128(o1, rxo, a + 64, 0, 0);
            } else {
                const unsigned a = xo + ((4 * rt + w) * 16 + 2 * ct) * 1024 + lane * 16;
                store16(o0, rw, a); store16(o1, rw, a + 1024);
                *(LAS u32x4*)(L + LDS_M + w * 8192 + T_RES + lane * 16) = (u32x4){res[0][0], res[0][1], res[1][0], res[1][1]};
                *(LAS u32x4*)(L + LDS_M + w * 8192 + T_RES + 1024 + lane * 16) = (u32x4){res[2][0], res[2][1], res[3][0], res[3][1]};
                sum = sum16(sum); sq = sum16(sq);
                sum = sum32(sum); sq = sum32(sq);
                __builtin_amdgcn_raw_buffer_store_b64((u32x2){__float_as_uint(sum), __float_as_uint(sq)}, rw, sto + (((4 * rt + w) * 8 + ct) * 16 + i16) * 8, 0, 16);
            }
        };

        {
            const int w = opq_s(wid), lane = lane_id(), i16 = lane & 15, q = lane >> 4;
            const int part = j2 >> 1, half = j2 & 1, r32 = lane & 31, hh = lane >> 5;
            const rsrc_t rws = mk((part ? wsim : wsre) + ((size_t)128 * T2 * S + 1024 * half) * 2, (128 * S - 1024 * half) * 2);
            const unsigned vsE = (lane >> 3) * 4096 + (((lane & 7) ^ (lane >> 4)) * 16), vsO = vsE ^ 64;
            const int w4_ = opq_s(wid);
            const unsigned lw4 = ldsb + w4_ * 4096, wsb = 32 * w4_ * 4096;
            auto dma_p2 = [&](int c, int p) { dma(lw4, (c & 7) * 16384 + p * 1024, (p & 1) ? vsO : vsE, rws, wsb, p * 32768 + c * 128); };
            dma_p2n(0, 4);
            wait_vm<16>();
            bar();
            publish(rw, 6 * layer + 0, blk, tag, w);
            STAMP(3 + 16 * layer);
            if (layer == 0) {
                u32x2 res0[4];
                const rsrc_t rx = mk(x, S * D * 2);
                const unsigned vr = (64 * rt + 16 * w + i16) * 1024 + (64 * ct + 4 * q) * 2;
#pragma unroll
                for (int b = 0; b < 4; ++b) aload2(res0[b], vr + b * 32, rx, 0u);
                asm volatile("s_waitcnt vmcnt(0)" : "+v"(res0[0]), "+v"(res0[1]), "+v"(res0[2]), "+v"(res0[3]) :: "memory");
                *(LAS u32x4*)(L + LDS_M + w * 8192 + T_RES + lane * 16) = (u32x4){res0[0][0], res0[0][1], res0[1][0], res0[1][1]};
                *(LAS u32x4*)(L + LDS_M + w * 8192 + T_RES + 1024 + lane * 16) = (u32x4){res0[2][0], res0[2][1], res0[3][0], res0[3][1]};
            }
            poll8(rw, 6 * layer + 0, blk_p1(8 * half + (lane & 7), 8 * part + 2 * E2 + (w >> 1)), tag, dead);
            bar();
            STAMP(4 + 16 * layer);
            const unsigned so0 = WS_ABT + layer * 4 * MB + part * 2 * MB + ((4 * E2 + w) * 128 + 64 * half) * 1024, vo = lane * 16;
            bf16x8 act[P2_LA + 2];
            auto load_act = [&](int v) { aload(act[v % (P2_LA + 2)], vo, rw, so0, v * 1024); };
#pragma unroll
            for (int v = 0; v < P2_LA; ++v) load_act(v);
            f32x16 acc[4];
#pragma unroll
            for (int t = 0; t < 4; ++t)
#pragma unroll
                for (int r = 0; r < 16; ++r) acc[t][r] = 0.f;
            unsigned rb[2][4];
#pragma unroll
            for (int k = 0; k < 4; ++k) { rb[0][k] = r32 * 128 + (((2 * k + hh) ^ ((r32 >> 1) & 7)) * 16); rb[1][k] = rb[0][k] + 65536; }
            auto ws_frag = [&](int n) {
                const int v = n >> 2, t = n & 3, c = v >> 2;
                return ldsA(L, rb[(c & 7) >> 2][v & 3] + (c & 3) * 16384 + t * 4096);
            };
            const int ws_ = opq_s(wid);
            const unsigned lw = ldsb + ws_ * 8192, g_sb = 64 * ws_ * 3584 + 112 * CT;
            unsigned gg0 = 0, gg1 = 0;
            bf16x8 wf[8];
#pragma unroll
            for (int n = 0; n < 6; ++n) wf[n] = ws_frag(n);
            sfor<64>([&](auto V) {
                constexpr int v = CI(V);
                asm volatile("s_waitcnt vmcnt(%1)" : "+v"(act[v % (P2_LA + 2)]) : "i"(p2_wait(v)) : "memory");
                if constexpr (v == 43) { const int l2 = lane_id(); gg0 = p3_voff(l2, 0, 0); gg1 = p3_voff(l2, 0, 1); }
                sfor<4>([&](auto TB) {
                    constexpr int t = CI(TB), n = 4 * v + t;
                    acc[t] = mfma32(act[v % (P2_LA + 2)], wf[n & 7], acc[t]);
                    SCHED();
                    if constexpr ((v & 3) == 2 && t == 2 && v < 60) bar();
                    if constexpr (n + 6 < 256) wf[(n + 6) & 7] = ws_frag(n + 6);
                    if constexpr (t == 1 && p2_dma(v)) {
                        if constexpr (v <= 39) dma_p2(((v - 8) >> 2) + 8, v & 3);
                        else dma_p3((v - 43) >> 3, (v - 43) & 7, ((v - 43) & 1) ? gg1 : gg0, lw, g_sb);
                    }
                    if constexpr (t == 3 && v + P2_LA < 64) load_act(v + P2_LA);
                    SCHED();
                });
            });
            STAMP(5 + 16 * layer);
            const unsigned pgrp = WS_PART + layer * 8 * MB + (T2 * 4 + E2) * 131072;
            const int lx = lane_id(), i16x = lx & 15, tup = (lx >> 4) & 1, hx = lx >> 5;
#pragma unroll
            for (int a = 0; a < 2; ++a) {
                const int owner = 2 * a + (w >> 1);
                if (owner == j2) {
#pragma unroll
                    for (int u = 0; u < 2; ++u)
#pragma unroll
                        for (int g = 0; g < 4; ++g) {
                            const f32x16& c = acc[2 * a + u];
                            *(LAS f32x4*)(L + LDS_M + (2 * u + tup) * 8192 + (2 * (w & 1) + (g >> 1)) * 1024 + (i16x + 16 * (2 * (g & 1) + hx)) * 16) =
                                (f32x4){c[4 * g], c[4 * g + 1], c[4 * g + 2], c[4 * g + 3]};
                        }
                } else {
                    const unsigned po = pgrp + owner * 32768 + j2 * 8192 + (w & 1) * 1024 + tup * 2048 + (i16x + 16 * hx) * 16;
#pragma unroll
                    for (int u = 0; u < 2; ++u)
#pragma unroll
                        for (int qh = 0; qh < 2; ++qh) {
                            const f32x16& c = acc[2 * a + u];
                            store16((u32x4){pk(c[4 * qh], c[4 * qh + 1]), pk(c[4 * qh + 2], c[4 * qh + 3]),
                                            pk(c[8 + 4 * qh], c[8 + 4 * qh + 1]), pk(c[8 + 4 * qh + 2], c[8 + 4 * qh + 3])}, rw, po + u * 4096 + qh * 512);
                        }
                }
            }
            wait_vm<0>();
            wait_lds();
            bar();
            publish(rw, 6 * layer + 1, blk, tag + 1, w);
            STAMP(13 + 16 * layer);
            const unsigned gu0 = p3_voff(lx, 1, 0), gu1 = p3_voff(lx, 1, 1), vx = lx * 16;
#pragma unroll
            for (int n = 0; n < 8; ++n) dma_p3(2, n, (n & 1) ? gu1 : gu0, lw, g_sb);
            poll8(rw, 6 * layer + 1, (blk & ~3) | (lx & 3), tag + 1, dead);
            STAMP(14 + 16 * layer);
            bf16x8 pv[3][2];
#pragma unroll
            for (int d = 1; d < 4; ++d)
#pragma unroll
                for (int h = 0; h < 2; ++h) aload(pv[d - 1][h], vx, rw, pgrp + j2 * 32768 + ((j2 + d) & 3) * 8192 + w * 2048, h * 1024);
            f32x4 v[4];
#pragma unroll
            for (int b = 0; b < 4; ++b) v[b] = *(LAS f32x4*)(L + LDS_M + w * 8192 + b * 1024 + vx);
            asm volatile("s_waitcnt vmcnt(0)" : "+v"(pv[0][0]), "+v"(pv[0][1]), "+v"(pv[1][0]), "+v"(pv[1][1]), "+v"(pv[2][0]), "+v"(pv[2][1]) :: "memory");
#pragma unroll
            for (int d = 0; d < 3; ++d)
#pragma unroll
                for (int h = 0; h < 2; ++h) {
                    const u32x4 p = __builtin_bit_cast(u32x4, pv[d][h]);
                    v[2 * h] += (f32x4){lo16(p[0]), hi16(p[0]), lo16(p[1]), hi16(p[1])};
                    v[2 * h + 1] += (f32x4){lo16(p[2]), hi16(p[2]), lo16(p[3]), hi16(p[3])};
                }
            finish_strip(v, false, WS_X1 + layer * 2 * MB, WS_ST + layer * ST_BYTES);
            wait_vm<0>();
            bar();
            publish(rw, 6 * layer + 2, blk, tag + 2, w);
            STAMP(6 + 16 * layer);
#pragma unroll
            for (int n = 0; n < 8; ++n) dma_p3(3, n, (n & 1) ? gu1 : gu0, lw, g_sb);
        }

        {
            const int w = opq_s(wid), lane = lane_id(), i16 = lane & 15, q = lane >> 4;
            poll8(rw, 6 * layer + 2, blk_p24(4 * RT + w, lane & 7), tag + 2, dead);
            if (layer == 0 && blk == 0 && w == 0) {
                u64 e2 = epoch; asm volatile("" : "+s"(e2)); e2 += 64;
                __builtin_amdgcn_raw_buffer_store_b64((u32x2){(unsigned)e2, (unsigned)(e2 >> 32)}, rw, 0, 0, 16);
                wait_vm<0>();
            }
            STAMP(7 + 16 * layer);
            const int sb0 = 16 * RT + 4 * w;
            u32x2 sa[4], sb_[4];
            bf16x8 act[P3_LA + 1][4];
            const unsigned vo = lane * 16, xo = WS_X1 + layer * 2 * MB + sb0 * 16384;
            auto load_x = [&](int g, int t) { aload(act[g % (P3_LA + 1)][t], vo, rw, xo, t * 16384 + (g & 15) * 1024); };
#pragma unroll
            for (int g = 0; g < P3_LA; ++g)
#pragma unroll
                for (int t = 0; t < 4; ++t) load_x(g, t);
            bar();
            const int qq = (lane >> 2) & 3, p4 = lane & 3;
            unsigned tb_[7];
            {
                const unsigned f = 2 * (qq >> 1) + 4 * (q & 1), rowb = (8 * q + qq) * 128 + 8 * (p4 & 1);
#pragma unroll
                for (int b = 0; b < 6; ++b) tb_[b] = (b & 1) * 65536 + rowb + (((2 * (b >> 1) + (p4 >> 1)) ^ f ^ (b & 1)) * 16);
                tb_[6] = (p4 >> 1) * 65536 + rowb + ((6 ^ f ^ (p4 >> 1)) * 16);
            }
            if (layer == 0) {
                f32x4 ac[7];
#pragma unroll
                for (int b = 0; b < 7; ++b) ac[b] = (f32x4){0.f, 0.f, 0.f, 0.f};
#pragma unroll
                for (int k = 0; k < 4; ++k)
#pragma unroll
                    for (int b = 0; b < 7; ++b) ac[b] = mfma(ldsT(L, tb_[b] + w * 16384 + k * 4096), ones, ac[b]);
#pragma unroll
                for (int b = 0; b < 7; ++b) *(LAS f32x4*)(L + T_CS3P + ((w * 7 + b) * 4 + q) * 16) = ac[b];
                wait_lds();
                bar();
                if (w == 0) {
                    const int l = lane < 28 ? lane : 27;
                    f32x4 s = *(LAS f32x4*)(L + T_CS3P + l * 16);
#pragma unroll
                    for (int k = 1; k < 4; ++k) s += *(LAS f32x4*)(L + T_CS3P + (k * 28 + l) * 16);
                    *(LAS f32x4*)(L + T_CS3 + l * 16) = s;
                }
                wait_lds();
                bar();
            }
            auto w_lo = [&](int ks, int b) { return __builtin_amdgcn_ds_read_tr16_b64_v4i16((LAS s16x4*)(L + tb_[b] + ks * 4096)); };
            auto w_hi = [&](int ks, int b) { return __builtin_amdgcn_ds_read_tr16_b64_v4i16((LAS s16x4*)(L + tb_[b] + ks * 4096 + 512)); };
            const unsigned sto = WS_ST + layer * ST_BYTES + sb0 * 1024, sv0 = (2 * q * 16 + i16) * 8;
            const unsigned ho = WS_H + layer * 7 * MB, hw = 14 * (CT >> 3);
            float mean[4], sig[4], kk_[4];
            auto glu = [&](float G, float U, float cg, float cu, int t) {
                const float e = __builtin_amdgcn_exp2f(kk_[t] * (mean[t] * cg - G));
                return (U - mean[t] * cu) * __builtin_amdgcn_rcpf(sig[t] * e + sig[t]);
            };
            u32x2 fl;
            {
                f32x4 a3[4][4];
#pragma unroll
                for (int b = 0; b < 4; ++b)
#pragma unroll
                    for (int t = 0; t < 4; ++t) a3[b][t] = (f32x4){0.f, 0.f, 0.f, 0.f};
                bf16x8 wf[4];
#pragma unroll
                for (int b = 0; b < 3; ++b) wf[b] = ldsT(L, tb_[b]);
                sfor<16>([&](auto KS) {
                    constexpr int ks = CI(KS), g = ks, sl = g % (P3_LA + 1);
                    wait4<p3_wait(g)>(act[sl][0], act[sl][1], act[sl][2], act[sl][3]);
                    if constexpr (ks == 0) STAMP(32 + 8 * layer);
                    sfor<4>([&](auto B) {
                        constexpr int b = CI(B), pb = b == 0 ? 3 : b - 1, pks = b == 0 ? ks : ks + 1;
                        s16x4 lo, hi;
                        a3[b][0] = mfma(wf[b], act[sl][0], a3[b][0]);
                        SCHED();
                        if constexpr (pks < 16) lo = w_lo(pks, pb);
                        SCHED();
                        a3[b][1] = mfma(wf[b], act[sl][1], a3[b][1]);
                        SCHED();
                        if constexpr (pks < 16) { hi = w_hi(pks, pb); wf[pb] = __builtin_shufflevector(lo, hi, 0, 1, 2, 3, 4, 5, 6, 7); }
                        SCHED();
                        a3[b][2] = mfma(wf[b], act[sl][2], a3[b][2]);
                        SCHED();
                        load_x(g + P3_LA, b);
                        SCHED();
                        a3[b][3] = mfma(wf[b], act[sl][3], a3[b][3]);
                        SCHED();
                        if constexpr (g == 8 && b == 3) {
#pragma unroll
                            for (int t = 0; t < 4; ++t) { aload2(sa[t], sv0, rw, sto + t * 1024); aload2(sb_[t], sv0 + 128, rw, sto + t * 1024); }
                            SCHED();
                        }
                    });
                });
                STAMP(33 + 8 * layer);
                asm volatile("" : "+v"(sa[0]), "+v"(sa[1]), "+v"(sa[2]), "+v"(sa[3]), "+v"(sb_[0]), "+v"(sb_[1]), "+v"(sb_[2]), "+v"(sb_[3]));
                {
                    float v[4];
                    combine_stats<4>(sa, sb_, mean, v);
#pragma unroll
                    for (int t = 0; t < 4; ++t) {
                        const float vv = v[t] + EPS, r = __builtin_amdgcn_rsqf(vv);
                        sig[t] = vv * r; kk_[t] = 1.44269504088896f * r;
                    }
                }
                unsigned H[2][4][2];
#pragma unroll
                for (int j = 0; j < 2; ++j) {
                    const f32x4 cg = *(LAS f32x4*)(L + T_CS3 + ((2 * j) * 4 + q) * 16), cu = *(LAS f32x4*)(L + T_CS3 + ((2 * j + 1) * 4 + q) * 16);
#pragma unroll
                    for (int t = 0; t < 4; ++t) {
                        float hv[4];
#pragma unroll
                        for (int r = 0; r < 4; ++r) hv[r] = glu(a3[2 * j][t][r], a3[2 * j + 1][t][r], cg[r], cu[r], t);
                        H[j][t][0] = pk(hv[0], hv[1]); H[j][t][1] = pk(hv[2], hv[3]);
                    }
                }
#pragma unroll
                for (int t = 0; t < 4; ++t)
                    store16(gather8(H[0][t][0], H[0][t][1], H[1][t][0], H[1][t][1]), rw, ho + ((sb0 + t) * 56 + hw + (CT & 7)) * 1024 + vo);
                SCHED();
            }
            STAMP(34 + 8 * layer);
            f32x4 a3[3][4];
            {
#pragma unroll
                for (int b = 0; b < 3; ++b)
#pragma unroll
                    for (int t = 0; t < 4; ++t) a3[b][t] = (f32x4){0.f, 0.f, 0.f, 0.f};
                bf16x8 wf[3];
#pragma unroll
                for (int b = 0; b < 2; ++b) wf[b] = ldsT(L, tb_[4 + b]);
                sfor<16>([&](auto KS) {
                    constexpr int ks = CI(KS), g = 16 + ks, sl = g % (P3_LA + 1);
                    wait4<p3_wait(g)>(act[sl][0], act[sl][1], act[sl][2], act[sl][3]);
                    if constexpr (g == P3_PUB) {
                        wait_vm<p3_wait_pub()>();
                        bar();
                        publish(rw, 6 * layer + 3, blk, tag + 3, w);
                        SCHED();
                    }
                    sfor<3>([&](auto B) {
                        constexpr int b = CI(B), pb = b == 0 ? 2 : b - 1, pks = b == 0 ? ks : ks + 1;
                        s16x4 lo, hi;
                        a3[b][0] = mfma(wf[b], act[sl][0], a3[b][0]);
                        SCHED();
                        if constexpr (pks < 16) lo = w_lo(pks, 4 + pb);
                        SCHED();
                        a3[b][1] = mfma(wf[b], act[sl][1], a3[b][1]);
                        SCHED();
                        if constexpr (pks < 16) { hi = w_hi(pks, 4 + pb); wf[pb] = __builtin_shufflevector(lo, hi, 0, 1, 2, 3, 4, 5, 6, 7); }
                        SCHED();
                        a3[b][2] = mfma(wf[b], act[sl][2], a3[b][2]);
                        SCHED();
                        if constexpr (g + P3_LA < 32) load_x(g + P3_LA, b);
                        SCHED();
                        a3[b][3] = mfma(wf[b], act[sl][3], a3[b][3]);
                        SCHED();
                        if constexpr (b == 2 && g + P3_LA < 32) { load_x(g + P3_LA, 3); SCHED(); }
                        if constexpr (b == 2 && g == 30) {
                            aload2(fl, (unsigned)blk_p3(rt >> 2, 8 * w + (lane & 7)) * 256u, rw, WS_FLAGS + (6 * layer + 3) * 65536u);
                            SCHED();
                        }
                    });
                });
            }
            wait_lds();
            bar();
            STAMP(8 + 16 * layer);

            const unsigned vE = (lane >> 3) * 1024 + (((lane & 7) ^ ((lane >> 3) & 2)) * 16), vO = vE ^ 64;
            const int ws_ = opq_s(wid);
            const unsigned lw = ldsb + ws_ * 8192, dsb = 448 * ws_ * 1024;
            const rsrc_t rwd = mk(wdn + 128 * ct, F * D * 2 - 128 * ct);
            auto dma_p4 = [&](int c, int p) {
                dma(lw, ((c + 1) & 3) * REG + p * 1024, (p & 1) ? vO : vE, rwd, dsb, p4_row(2 * c + (p >> 2), p & 3) * 1024);
            };
#pragma unroll
            for (int n = 0; n < 16; ++n) dma_p4(n >> 3, n & 7);
            asm volatile("s_waitcnt vmcnt(16)" : "+v"(fl) :: "memory");
            if (!__all((((u64)fl[1] << 32) | fl[0]) == tag + 3)) poll8(rw, 6 * layer + 3, blk_p3(rt >> 2, 8 * w + (lane & 7)), tag + 3, dead);
            STAMP(10 + 16 * layer);
            unsigned so[4];
#pragma unroll
            for (int d = 0; d < 4; ++d) so[d] = ho + ((4 * rt + ((w + d) & 3)) * 56 + 14 * w) * 1024;
            bf16x8 hact[6][4];
            auto load_h = [&](int u, int d) { aload(hact[u % 6][d], vo, rw, so[d], u * 1024); };
#pragma unroll
            for (int u = 0; u < 5; ++u)
#pragma unroll
                for (int d = 0; d < 4; ++d) load_h(u, d);
            SCHED();
            {
                const f32x4 cg = *(LAS f32x4*)(L + T_CS3 + (4 * 4 + q) * 16), cu = *(LAS f32x4*)(L + T_CS3 + (5 * 4 + q) * 16);
                const f32x4 mg = *(LAS f32x4*)(L + T_CS3 + (6 * 4 + (q & 1)) * 16), mu = *(LAS f32x4*)(L + T_CS3 + (6 * 4 + 2 + (q & 1)) * 16);
                const unsigned beta = 3 * (CT & 7) + q;
                const unsigned hb = q < 3 ? ho + (sb0 * 56 + hw + 8 + (beta >> 2)) * 1024 + ((beta & 3) * 16 + i16) * 16 : 0x7ff00000u;
#pragma unroll
                for (int t = 0; t < 4; ++t) {
                    float hv[4], hm[4];
#pragma unroll
                    for (int r = 0; r < 4; ++r) {
                        hv[r] = glu(a3[0][t][r], a3[1][t][r], cg[r], cu[r], t);
                        auto sw = __builtin_amdgcn_permlane32_swap(__float_as_uint(a3[2][t][r]), __float_as_uint(a3[2][t][r]), false, false);
                        hm[r] = glu(__uint_as_float(sw[0]), __uint_as_float(sw[1]), mg[r], mu[r], t);
                    }
                    store16(gather8(pk(hv[0], hv[1]), pk(hv[2], hv[3]), pk(hm[0], hm[1]), pk(hm[2], hm[3])), rw, hb + t * 56 * 1024);
                }
                SCHED();
            }
            STAMP(35 + 8 * layer);
#pragma unroll
            for (int n = 16; n < 32; ++n) dma_p4(n >> 3, n & 7);
            f32x4 acc[4][4];
#pragma unroll
            for (int b = 0; b < 4; ++b)
#pragma unroll
                for (int d = 0; d < 4; ++d) acc[b][d] = (f32x4){0.f, 0.f, 0.f, 0.f};
            unsigned wb_[2][4];
#pragma unroll
            for (int b = 0; b < 4; ++b) {
                wb_[0][b] = w * 8192 + (8 * q + qq) * 128 + (((2 * b + (p4 >> 1)) ^ ((qq & 2) + 4 * (q & 1))) * 16) + 8 * (p4 & 1);
                wb_[1][b] = wb_[0][b] + 2 * REG;
            }
            auto wd_off = [&](int u, int b) { return wb_[(((u >> 1) + 1) & 3) >> 1][b] + (((u >> 1) + 1) & 1) * REG + (u & 1) * 4096; };
            bf16x8 wf[2][4];
            u32x2 flb;
            sfor<14>([&](auto U) {
                constexpr int u = CI(U);
                wait4<p4_wait(u)>(hact[u % 6][0], hact[u % 6][1], hact[u % 6][2], hact[u % 6][3]);
                if constexpr (u == 0) {
#pragma unroll
                    for (int b = 0; b < 4; ++b) wf[0][b] = ldsT(L, wd_off(0, b));
                }
                if constexpr (u == 1) {
                    bar();
                    publish(rw, 6 * layer + 4, blk, tag + 4, w);
                    STAMP(9 + 16 * layer);
                    aload2(flb, (unsigned)blk_p3(rt >> 2, 8 * w + (lane & 7)) * 256u, rw, WS_FLAGS + (6 * layer + 4) * 65536u);
                    SCHED();
                }
                if constexpr (u == 3) {
                    asm volatile("" : "+v"(flb));
                    if (!__all((((u64)flb[1] << 32) | flb[0]) == tag + 4)) poll8(rw, 6 * layer + 4, blk_p3(rt >> 2, 8 * w + (lane & 7)), tag + 4, dead);
                    SCHED();
                }
                sfor<8>([&](auto J) {
                    constexpr int j = CI(J), b = j >> 1, d0 = 2 * (j & 1);
                    acc[b][d0] = mfma(wf[u & 1][b], hact[u % 6][d0], acc[b][d0]);
                    acc[b][d0 + 1] = mfma(wf[u & 1][b], hact[u % 6][d0 + 1], acc[b][d0 + 1]);
                    SCHED();
                    if constexpr (j < 4 && u < 13) wf[(u + 1) & 1][j] = ldsT(L, wd_off(u + 1, j));
                    if constexpr ((j & 1) == 0) {
                        constexpr int p = 4 * ((u + 1) & 1) + (j >> 1);
                        if constexpr (u >= 1 && u <= 6) dma_p4(((u - 1) >> 1) + 4, p);
                    } else if constexpr (u <= 8) load_h(u + 5, j >> 1);
                    SCHED();
                });
            });
            STAMP(11 + 16 * layer);
            if (layer == 0) dma_p1_all();
#pragma unroll
            for (int d = 1; d < 4; ++d)
#pragma unroll
                for (int b = 0; b < 4; ++b)
                    *(LAS f32x4*)(L + w * 8192 + lane * 16 + (d < 3 ? 3 * REG + (d - 1) * 4096 : LDS_M) + b * 1024) = acc[b][d];
            wait_lds();
            bar();
            if (layer == 0) dma_p2n(4, 6);
            f32x4 v[4];
#pragma unroll
            for (int b = 0; b < 4; ++b) v[b] = acc[b][0];
#pragma unroll
            for (int d = 1; d < 4; ++d) {
                const int src = (w - d) & 3;
#pragma unroll
                for (int b = 0; b < 4; ++b)
                    v[b] += *(LAS f32x4*)(L + src * 8192 + lane * 16 + (d < 3 ? 3 * REG + (d - 1) * 4096 : LDS_M) + b * 1024);
            }
            if (layer == 0) {
                finish_strip(v, false, WS_X2, WS_ST + 2 * ST_BYTES);
                wait_vm<0>();
                bar();
                publish(rw, 5, blk, tag + 5, w);
                STAMP(12);
                dma_p2n(6, 8);
            } else {
                finish_strip(v, true, 0, 0);
                STAMP(28);
            }
        }
        if (layer == 1) break;
        phase1(std::false_type{});
    }
}

}

extern "C" void kernel_launch(void* const* d_in, const int* in_sizes, int n_in,
                              void* d_out, int out_size, void* d_ws, size_t ws_size,
                              hipStream_t stream) {
    (void)in_sizes; (void)n_in; (void)out_size; (void)ws_size;
    (void)hipFuncSetAttribute((const void*)fn::fnet_glu_kernel, hipFuncAttributeMaxDynamicSharedMemorySize, fn::LDS_BYTES);
    fn::fnet_glu_kernel<<<256, 256, fn::LDS_BYTES, stream>>>((const char*)d_in[0], (const char*)d_in[1], (const char*)d_in[2],
        (const char*)d_in[3], (const char*)d_in[4], (const char*)d_in[6], (const char*)d_in[7], (const char*)d_in[8],
        (char*)d_out, (char*)d_ws);
}
```

```cpp
#include <hip/hip_runtime.h>
#include <hip/hip_bf16.h>
#include <stdint.h>
#include <type_traits>
#include <utility>

#define DI __device__ __forceinline__
#define LAS __attribute__((address_space(3)))
#define GAS __attribute__((address_space(1)))
typedef short bf16x8 __attribute__((ext_vector_type(8)));
typedef short s16x4 __attribute__((ext_vector_type(4)));
typedef float f32x4 __attribute__((ext_vector_type(4)));
typedef float f32x2 __attribute__((ext_vector_type(2)));
typedef float f32x16 __attribute__((ext_vector_type(16)));
typedef unsigned u32x4 __attribute__((ext_vector_type(4)));
typedef unsigned u32x2 __attribute__((ext_vector_type(2)));
typedef __bf16 bf16x2 __attribute__((ext_vector_type(2)));
typedef unsigned long long u64;
typedef __amdgpu_buffer_rsrc_t rsrc_t;

#define WARM 2
namespace fn {

constexpr int S = 2048, D = 512, F = 1792;
constexpr unsigned MB = 1u << 20;
constexpr unsigned WS_FLAGS = 4096;
constexpr unsigned WS_ABT = 1 * MB;
constexpr unsigned WS_X1 = 9 * MB;
constexpr unsigned WS_X2 = 13 * MB;
constexpr unsigned WS_H = 15 * MB;
constexpr unsigned WS_ST = 29 * MB;
constexpr unsigned WS_STAMP = 30 * MB;
constexpr unsigned WS_PART = 32 * MB;
constexpr unsigned ST_BYTES = 128 * 1024;

constexpr unsigned REG = 32768;
constexpr unsigned LDS_M = 4 * REG;
constexpr unsigned T_RES = 4096;
constexpr unsigned T_STAT = 6144;
constexpr unsigned T_CS1 = LDS_M + 6400;
constexpr unsigned T_CS3 = LDS_M + 8192 + 6400;
constexpr unsigned T_CS3P = LDS_M + 16384 + 6400;
constexpr unsigned T_DUMMY = LDS_M + 24576 + 6400;
constexpr unsigned LDS_BYTES = 163840;
#define CODE_KB 52
constexpr unsigned CODE_PREFETCH_BYTES = CODE_KB * 1024;
constexpr unsigned SPIN_LIMIT = 200000;
constexpr float EPS = 1e-5f;

#define TLBW 0
#define WDEPTH 2
#define WDELAY 0
DI rsrc_t mk(const void* p, unsigned bytes) { return __builtin_amdgcn_make_buffer_rsrc((void*)p, 0, bytes, 0x00020000); }
DI void dma(unsigned lds, int LI, unsigned voff, rsrc_t r, unsigned sb, int SI) {
    unsigned t;
    asm volatile("s_add_u32 m0, %1, %5\n\ts_add_u32 %0, %4, %6\n\tbuffer_load_dwordx4 %2, %3, %0 offen lds"
                 : "=&s"(t) : "s"(lds), "v"(voff), "s"(r), "s"(sb), "i"(LI), "i"(SI) : "m0", "scc", "memory");
}
DI void aload(bf16x8& d, unsigned voff, rsrc_t r, unsigned sb, int SI) {
    asm volatile("buffer_load_dwordx4 %0, %1, %2, %3 offen offset:%4 sc1"
                 : "=v"(d) : "v"(voff), "s"(r), "s"(sb + (SI & ~4095)), "i"(SI & 4095) : "memory");
}
DI void aload2(u32x2& d, unsigned voff, rsrc_t r, unsigned sb) {
    asm volatile("buffer_load_dwordx2 %0, %1, %2, %3 offen sc1" : "=v"(d) : "v"(voff), "s"(r), "s"(sb) : "memory");
}
template <int... Is, class Fn> DI void sfor_impl(std::integer_sequence<int, Is...>, Fn&& f) { (f(std::integral_constant<int, Is>{}), ...); }
template <int N, class Fn> DI void sfor(Fn&& f) { sfor_impl(std::make_integer_sequence<int, N>{}, f); }
#define CI(x) decltype(x)::value
DI int lane_id() { int l; asm volatile("v_mbcnt_lo_u32_b32 %0, -1, 0\n\tv_mbcnt_hi_u32_b32 %0, -1, %0" : "=v"(l)); return l; }
DI int opq_s(int v) { asm volatile("" : "+s"(v)); return v; }
template <int N> DI void wait_vm() { asm volatile("s_waitcnt vmcnt(%0)" :: "i"(N) : "memory"); }
template <int N> DI void wait2(bf16x8& a, bf16x8& b) { asm volatile("s_waitcnt vmcnt(%2)" : "+v"(a), "+v"(b) : "i"(N) : "memory"); }
template <int N> DI void wait4(bf16x8& a, bf16x8& b, bf16x8& c, bf16x8& d) {
    asm volatile("s_waitcnt vmcnt(%4)" : "+v"(a), "+v"(b), "+v"(c), "+v"(d) : "i"(N) : "memory");
}
template <int N> DI void wait8(bf16x8 (&a)[8]) {
    asm volatile("s_waitcnt vmcnt(%8)" : "+v"(a[0]), "+v"(a[1]), "+v"(a[2]), "+v"(a[3]), "+v"(a[4]), "+v"(a[5]), "+v"(a[6]), "+v"(a[7]) : "i"(N) : "memory");
}
DI void wait_lds() { asm volatile("s_waitcnt lgkmcnt(0)" ::: "memory"); }
DI void bar() { asm volatile("" ::: "memory"); __builtin_amdgcn_s_barrier(); asm volatile("" ::: "memory"); }
#define SCHED() __builtin_amdgcn_sched_barrier(0)
DI f32x4 mfma(bf16x8 a, bf16x8 b, f32x4 c) { return __builtin_amdgcn_mfma_f32_16x16x32_bf16(a, b, c, 0, 0, 0); }
DI f32x16 mfma32(bf16x8 a, bf16x8 b, f32x16 c) { return __builtin_amdgcn_mfma_f32_32x32x16_bf16(a, b, c, 0, 0, 0); }
DI unsigned pk(float lo, float hi) { f32x2 v = {lo, hi}; return __builtin_bit_cast(unsigned, __builtin_convertvector(v, bf16x2)); }
DI float lo16(unsigned u) { return __uint_as_float(u << 16); }
DI float hi16(unsigned u) { return __uint_as_float(u & 0xffff0000u); }
DI float sum16(float v) { auto r = __builtin_amdgcn_permlane16_swap(__float_as_uint(v), __float_as_uint(v), false, false); return __uint_as_float(r[0]) + __uint_as_float(r[1]); }
DI float sum32(float v) { auto r = __builtin_amdgcn_permlane32_swap(__float_as_uint(v), __float_as_uint(v), false, false); return __uint_as_float(r[0]) + __uint_as_float(r[1]); }
DI float sumq(float v) { return sum32(sum16(v)); }
DI u32x4 gather8(unsigned x0, unsigned x1, unsigned y0, unsigned y1) {
    auto a = __builtin_amdgcn_permlane32_swap(x0, y0, false, false);
    auto b = __builtin_amdgcn_permlane32_swap(x1, y1, false, false);
    auto c = __builtin_amdgcn_permlane16_swap(a[0], a[1], false, false);
    auto d = __builtin_amdgcn_permlane16_swap(b[0], b[1], false, false);
    return (u32x4){c[0], d[0], c[1], d[1]};
}
DI void store16(u32x4 v, rsrc_t r, unsigned voff) { __builtin_amdgcn_raw_buffer_store_b128(v, r, voff, 0, 16); }
DI bf16x8 ldsA(LAS char* L, unsigned off) { return *(LAS bf16x8*)(L + off); }
DI bf16x8 ldsT(LAS char* L, unsigned off) {
    s16x4 lo = __builtin_amdgcn_ds_read_tr16_b64_v4i16((LAS s16x4*)(L + off));
    s16x4 hi = __builtin_amdgcn_ds_read_tr16_b64_v4i16((LAS s16x4*)(L + off + 512));
    return __builtin_shufflevector(lo, hi, 0, 1, 2, 3, 4, 5, 6, 7);
}
DI int blk_p24(int rt, int ct) { return 8 * (4 * ((rt >> 1) & 7) + (ct >> 1)) + 4 * (rt >> 4) + 2 * (rt & 1) + (ct & 1); }
DI int blk_p1(int rt1, int et) { return 8 * (8 * (rt1 & 3) + (et & 7)) + 2 * (rt1 >> 2) + (et >> 3); }
DI int blk_p3(int RT, int CT) { return 8 * (8 * (RT & 3) + (CT & 7)) + 2 * (CT >> 3) + (RT >> 2); }

DI void poll8(rsrc_t rw, int phase, int prod, u64 tag, bool& dead) {
    const unsigned voff = (unsigned)prod * 256u, sb = WS_FLAGS + (unsigned)phase * 65536u;
    if (!dead) {
        unsigned spins = 0;
        for (;;) {
            u64 v;
            asm volatile("buffer_load_dwordx2 %0, %1, %2, %3 offen sc1\n\ts_waitcnt vmcnt(0)" : "=v"(v) : "v"(voff), "s"(rw), "s"(sb) : "memory");
            if (__all(v == tag)) break;
            if (++spins > SPIN_LIMIT) { dead = true; break; }
        }
    }
    asm volatile("" ::: "memory");
}
DI void publish(rsrc_t rw, int phase, int blk, u64 tag, int w) {
    if (w == 0)
        __builtin_amdgcn_raw_buffer_store_b64((u32x2){(unsigned)tag, (unsigned)(tag >> 32)}, rw, WS_FLAGS + (unsigned)(phase * 256 + blk) * 256u, 0, 16);
}

#define STAMP(n) do {} while (0)

constexpr int P1_LA = 12;
constexpr int p1_issued(int ks) { return 2 * P1_LA + 2 * (ks < 16 - P1_LA ? ks : 16 - P1_LA); }
constexpr int p1_wait(int ks) { return p1_issued(ks) - 1 - (ks < P1_LA ? 2 * ks + 1 : p1_issued(ks - P1_LA + 1) - 1); }
constexpr int P2_LA = 22;
constexpr int p2_dma(int v) { return (v >= 8 && v <= 39) || (v >= 43 && v <= 58) ? 1 : 0; }
constexpr int p2_ops(int v) { return p2_dma(v) + (v + P2_LA < 64 ? 1 : 0); }
constexpr int p2_issued(int v) { int n = P2_LA; for (int j = 0; j < v; ++j) n += p2_ops(j); return n; }
constexpr int p2_wait(int v) {
    const int pa = v < P2_LA ? v : p2_issued(v - P2_LA + 1) - 1;
    const int c = (v >> 2) + 1;
    const int pd = (v & 3) == 2 && c >= 8 && c < 16 ? p2_issued(4 * (c - 8 + 2) + 3) : -1;
    return p2_issued(v) - 1 - (pa > pd ? pa : pd);
}
constexpr int P3_LA = 6;
constexpr int p3_ops(int g) { return (g + P3_LA < 32 ? 4 : 0) + (g == 8 ? 8 : 0) + (g == 15 ? 4 : 0) + (g == 30 ? 1 : 0); }
constexpr int p3_issued(int g) { int n = 4 * P3_LA; for (int j = 0; j < g; ++j) n += p3_ops(j); return n; }
constexpr int p3_xlast(int j) { return p3_issued(j) + 3; }
constexpr int p3_wait(int g) { return p3_issued(g) - 1 - (g < P3_LA ? 4 * g + 3 : p3_xlast(g - P3_LA)); }
constexpr int P3_PUB = 19;
constexpr int p3_wait_pub() { return p3_issued(P3_PUB) - p3_issued(16); }
constexpr int p4_ops(int u) { return (u == 1 ? 1 : 0) + (u >= 1 && u <= 6 ? 4 : 0) + (u <= 8 ? 4 : 0); }
constexpr int p4_issued(int u) { int n = 40; for (int j = 0; j < u; ++j) n += p4_ops(j); return n; }
constexpr int p4_wait(int u) {
    int p = u < 5 ? 4 * u + 3 : p4_issued(u - 4) - 1;
    if (u == 1 && p < 23) p = 23;
    if (u == 3 && p < p4_issued(1)) p = p4_issued(1);
    if ((u & 1) && u < 13) {
        const int c = (u + 1) >> 1;
        const int pd = c == 2 ? 31 : c == 3 ? 39 : c >= 4 ? p4_issued(2 * c - 6) + (2 * c - 6 == 1 ? 1 : 0) + 6 : -1;
        if (pd > p) p = pd;
    }
    return p4_issued(u) - 1 - p;
}
constexpr int p4_row(int u, int qq) { return u < 8 ? 56 * u + 8 * qq : 56 * ((4 * (u - 8) + qq) / 3) + 32 + 8 * ((4 * (u - 8) + qq) % 3); }

template <int N> DI void combine_stats(const u32x2 (&a)[N], const u32x2 (&b)[N], float (&mean)[N], float (&var)[N]) {
    float s[N], t[N];
#pragma unroll
    for (int i = 0; i < N; ++i) { s[i] = __uint_as_float(a[i][0]) + __uint_as_float(b[i][0]); t[i] = __uint_as_float(a[i][1]) + __uint_as_float(b[i][1]); }
#pragma unroll
    for (int i = 0; i < N; ++i) { s[i] = sum16(s[i]); t[i] = sum16(t[i]); }
#pragma unroll
    for (int i = 0; i < N; ++i) { s[i] = sum32(s[i]); t[i] = sum32(t[i]); }
#pragma unroll
    for (int i = 0; i < N; ++i) { mean[i] = s[i] * (1.0f / 512.0f); var[i] = fmaxf(t[i] * (1.0f / 512.0f) - mean[i] * mean[i], 0.f); }
}

__global__ __launch_bounds__(256, 2) void fnet_glu_kernel(const char* __restrict__ x, const char* __restrict__ wsre, const char* __restrict__ wsim,
        const char* __restrict__ wdre, const char* __restrict__ wdim_, const char* __restrict__ wg, const char* __restrict__ wu,
        const char* __restrict__ wdn, char* __restrict__ out, char* __restrict__ ws) {
    extern __shared__ __attribute__((aligned(16))) char smem[];
    LAS char* L = (LAS char*)smem;
    int wid;
    asm volatile("s_nop 1\n\tv_readfirstlane_b32 %0, %1\n\ts_nop 4" : "=s"(wid) : "v"(threadIdx.x >> 6));
    const int blk = blockIdx.x, xcd = blk & 7, idx = blk >> 3;

    const unsigned ldsb = (unsigned)(size_t)L;
    const rsrc_t rw = mk(ws, 48 * MB);
    const int T2 = 8 * (xcd >> 2) + (idx >> 2), E2 = idx & 3, j2 = xcd & 3;
    const int rt = 2 * T2 + (j2 >> 1), ct = 2 * E2 + (j2 & 1);
    const int rt1 = 4 * (xcd >> 1) + (idx >> 3), et = 8 * (xcd & 1) + (idx & 7);
    const int RT = 4 * (xcd & 1) + (idx >> 3), CT = 8 * (xcd >> 1) + (idx & 7);
    STAMP(0);
    if (CODE_PREFETCH_BYTES) {
        u64 pc;
        asm volatile("s_getpc_b64 %0" : "=s"(pc));
        const rsrc_t rc = mk((const void*)(pc & ~(u64)127), CODE_PREFETCH_BYTES);
        const unsigned seg = (unsigned)(idx * 4 + wid) % ((CODE_PREFETCH_BYTES + 8191) / 8192);
        const unsigned o = seg * 8192u + lane_id() * 128u, l = ldsb + T_DUMMY + 768;
        asm volatile("s_mov_b32 m0, %0\n\ts_nop 0\n\tbuffer_load_dword %1, %2, 0 offen lds" :: "s"(l), "v"(o), "s"(rc) : "m0", "memory");
    }

    auto p1_rsrc = [&](unsigned bytes) { return mk(((et >> 3) ? wdim_ : wdre) + (size_t)(et & 7) * 64 * 1024, bytes); };
    auto dma_p1 = [&](int j, int p, rsrc_t r, unsigned v0, unsigned lw, unsigned sb) {
        dma(lw, (32 * j + p) * 1024, v0 ^ (16 * p), r, sb, (32 * j + p) * 1024);
    };
    auto p3_voff = [&](int ln, int up, int odd) -> unsigned {
        const unsigned c = (ln & 7) ^ ((2 * ((ln >> 4) & 1) + 4 * odd) ^ up);
        return (ln >> 3) * 3584 + 16 * (c > 6 ? 6 : c);
    };
    auto dma_p3 = [&](int j, int p, unsigned v, unsigned lw, unsigned sb) {
        dma(lw, j * REG + p * 1024, v, mk((j >> 1) ? wu : wg, D * F * 2), sb, (256 * (j & 1) + 8 * p) * 3584);
    };
    auto dma_p2n = [&](int c0, int c1) {
        const int w = opq_s(wid), ln = lane_id();
        const rsrc_t r = mk(((j2 >> 1) ? wsim : wsre) + ((size_t)128 * T2 * S + 1024 * (j2 & 1)) * 2, (128 * S - 1024 * (j2 & 1)) * 2);
        const unsigned vE = (ln >> 3) * 4096 + (((ln & 7) ^ (ln >> 4)) * 16), vO = vE ^ 64;
#pragma unroll
        for (int c = c0; c < c1; ++c)
#pragma unroll
            for (int p = 0; p < 4; ++p) dma(ldsb + w * 4096, (c & 7) * 16384 + p * 1024, (p & 1) ? vO : vE, r, 32 * w * 4096, p * 32768 + c * 128);
    };
    auto dma_p1_all = [&]() {
        const int w = opq_s(wid), ln = lane_id();
        const rsrc_t r = p1_rsrc(65536);
        const unsigned v0 = (unsigned)(ln * 16) ^ (128 * (w & 1)), lw = ldsb + w * 8192;
#pragma unroll
        for (int j = 0; j < 2; ++j)
#pragma unroll
            for (int p = 0; p < 8; ++p) dma_p1(j, p, r, v0, lw, w * 8192);
    };
    auto warm_ffn = [&]() {
        const int w = opq_s(wid), ln = lane_id();
        const unsigned o = blk * 7168u + (ln < 56 ? ln : 55) * 128u, l = ldsb + T_DUMMY + w * 256;
        auto touch = [&](rsrc_t r) {
            asm volatile("s_mov_b32 m0, %0\n\ts_nop 0\n\tbuffer_load_dword %1, %2, 0 offen lds" :: "s"(l), "v"(o), "s"(r) : "m0", "memory");
        };
        if (w == 0) touch(mk(wg, D * F * 2));
        if (w == 1) touch(mk(wu, D * F * 2));
        if (w == 2) touch(mk(wdn, D * F * 2));
    };
    dma_p1_all();

    u32x2 ev;
    aload2(ev, 0u, rw, 0u);
    u64 epoch = 0;
    bool dead = false;
    const bf16x8 ones = {0x3F80, 0x3F80, 0x3F80, 0x3F80, 0x3F80, 0x3F80, 0x3F80, 0x3F80};

        auto phase1 = [&](auto first_tag) {
            constexpr bool FIRST = decltype(first_tag)::value;
            constexpr int layer = FIRST ? 0 : 1;
            const int w = opq_s(wid), lane = lane_id(), i16 = lane & 15, q = lane >> 4;
            const int sbg = 8 * rt1 + 2 * w;
            bf16x8 xa[2][16];
            u32x2 sa[2], sb_[2];
            f32x4 acc[2][4];
#pragma unroll
            for (int s = 0; s < 2; ++s)
#pragma unroll
                for (int e = 0; e < 4; ++e) acc[s][e] = (f32x4){0.f, 0.f, 0.f, 0.f};
            unsigned wb[4];
#pragma unroll
            for (int k = 0; k < 4; ++k) wb[k] = i16 * 1024 + (((4 * k + q) ^ i16) * 16);
            LAS f32x2* tst = (LAS f32x2*)(L + LDS_M + w * 8192 + T_STAT);
            const float sgn = (et >> 3) ? -1.0f : 1.0f;
            f32x4 aS[2], aQ[2], aC = {0.f, 0.f, 0.f, 0.f};
#pragma unroll
            for (int s = 0; s < 2; ++s) { aS[s] = (f32x4){0.f, 0.f, 0.f, 0.f}; aQ[s] = aS[s]; }
            const unsigned wown = w * 16384;
            const unsigned vo = lane * 16, sbase = WS_X2 + sbg * 16384;
            if constexpr (FIRST) {
                const rsrc_t rx = mk(x, S * D * 2);
                bf16x8 xr[32];
#pragma unroll
                for (int r = 0; r < 32; ++r) aload(xr[r], vo, rx, sbg * 16384, r * 1024);
                LAS char* T0 = L + 2 * REG + w * 8192;
                sfor<2>([&](auto SB) {
                    constexpr int s = CI(SB);
                    sfor<4>([&](auto G) {
                        constexpr int g = CI(G), r0 = 16 * s + 4 * g;
                        wait4<28 - r0>(xr[r0], xr[r0 + 1], xr[r0 + 2], xr[r0 + 3]);
#pragma unroll
                        for (int r = 0; r < 4; ++r)
                            *(LAS bf16x8*)(T0 + ((4 * g + r) >> 3) * REG + ((4 * g + r) & 7) * 1024 + ((lane * 16) ^ ((4 * g + r) * 16))) = xr[r0 + r];
                    });
#pragma unroll
                    for (int ks = 0; ks < 16; ++ks)
                        xa[s][ks] = *(LAS bf16x8*)(T0 + (i16 >> 3) * REG + (i16 & 7) * 1024 + (((4 * ks + q) ^ i16) * 16));
                    wait_lds();
                });
                asm volatile("" : "+v"(ev));
                epoch = ((u64)(unsigned)__builtin_amdgcn_readfirstlane((int)ev[1]) << 32) | (unsigned)__builtin_amdgcn_readfirstlane((int)ev[0]);
                wait_lds();
                bar();
                STAMP(1);
                dma_p2n(4, 8);
                warm_ffn();
            } else {
                poll8(rw, 5, blk_p24(2 * rt1 + (w >> 1), lane & 7), epoch + 6, dead);
                STAMP(17);
#pragma unroll
                for (int s = 0; s < 2; ++s) {
                    const unsigned so = WS_ST + 2 * ST_BYTES + (sbg + s) * 1024;
                    aload2(sa[s], (2 * q * 16 + i16) * 8, rw, so);
                    aload2(sb_[s], ((2 * q + 1) * 16 + i16) * 8, rw, so);
                }
#pragma unroll
                for (int ks = 0; ks < P1_LA; ++ks)
#pragma unroll
                    for (int s = 0; s < 2; ++s) aload(xa[s][ks], vo, rw, sbase, s * 16384 + ks * 1024);
                wait2<p1_wait(0)>(xa[0][0], xa[1][0]);
                asm volatile("" : "+v"(sa[0]), "+v"(sa[1]), "+v"(sb_[0]), "+v"(sb_[1]));
            }
            bf16x8 wf[2][4], wo;
#pragma unroll
            for (int e = 0; e < 4; ++e) wf[0][e] = ldsA(L, wb[0] + e * 16384);
            sfor<16>([&](auto KS) {
                constexpr int ks = CI(KS);
                if constexpr (!FIRST && ks > 0) wait2<p1_wait(ks)>(xa[0][ks], xa[1][ks]);
                if constexpr (FIRST) wo = ldsA(L, wb[ks & 3] + wown + 256 * (ks >> 2));
                sfor<4>([&](auto E) {
                    constexpr int e = CI(E);
                    acc[0][e] = mfma(xa[0][ks], wf[ks & 1][e], acc[0][e]);
                    acc[1][e] = mfma(xa[1][ks], wf[ks & 1][e], acc[1][e]);
                    SCHED();
                    if constexpr (ks < 15) wf[(ks + 1) & 1][e] = ldsA(L, wb[(ks + 1) & 3] + e * 16384 + 256 * ((ks + 1) >> 2));
                    if constexpr (!FIRST && e >= 2 && ks + P1_LA < 16) aload(xa[e - 2][ks + P1_LA], vo, rw, sbase, (e - 2) * 16384 + (ks + P1_LA) * 1024);
                    SCHED();
                });
                if constexpr (FIRST) {
                    aC = mfma(ones, wo, aC);
#pragma unroll
                    for (int s = 0; s < 2; ++s) { aS[s] = mfma(ones, xa[s][ks], aS[s]); aQ[s] = mfma(xa[s][ks], xa[s][ks], aQ[s]); }
                    SCHED();
                }
            });
            if constexpr (FIRST) {
                ((LAS float*)(L + T_CS1))[16 * w + i16] = aC[0];
#pragma unroll
                for (int s = 0; s < 2; ++s) {
                    const int r3 = i16 & 3;
                    float dq = r3 == 0 ? aQ[s][0] : r3 == 1 ? aQ[s][1] : r3 == 2 ? aQ[s][2] : aQ[s][3];
                    dq = (q == (i16 >> 2)) ? dq : 0.f;
                    const float mean = aS[s][0] * (1.0f / 512.0f);
                    const float var = fmaxf(sumq(dq) * (1.0f / 512.0f) - mean * mean, 0.f);
                    tst[16 * s + i16] = (f32x2){mean, sgn * __builtin_amdgcn_rsqf(var + EPS)};
                }
            } else {
                float mean[2], var[2];
                combine_stats<2>(sa, sb_, mean, var);
#pragma unroll
                for (int s = 0; s < 2; ++s) tst[16 * s + i16] = (f32x2){mean[s], sgn * __builtin_amdgcn_rsqf(var[s] + EPS)};
            }
            wait_lds();
            bar();
            STAMP(2 + 16 * layer);
            f32x4 st0[2], st1[2];
#pragma unroll
            for (int s = 0; s < 2; ++s) {
                const f32x4 a = *(LAS f32x4*)(tst + 16 * s + 4 * q), b = *(LAS f32x4*)(tst + 16 * s + 4 * q + 2);
                st0[s] = (f32x4){a[0], a[2], b[0], b[2]}; st1[s] = (f32x4){a[1], a[3], b[1], b[3]};
            }
#pragma unroll
            for (int E = 0; E < 2; ++E) {
                const float cs0 = ((LAS float*)(L + T_CS1))[32 * E + i16], cs1 = ((LAS float*)(L + T_CS1))[32 * E + 16 + i16];
#pragma unroll
                for (int s = 0; s < 2; ++s) {
                    f32x4 v0, v1;
#pragma unroll
                    for (int r = 0; r < 4; ++r) {
                        v0[r] = (acc[s][2 * E][r] - st0[s][r] * cs0) * st1[s][r];
                        v1[r] = (acc[s][2 * E + 1][r] - st0[s][r] * cs1) * st1[s][r];
                    }
                    auto a = __builtin_amdgcn_permlane16_swap(pk(v0[0], v0[1]), pk(v1[0], v1[1]), false, false);
                    auto b = __builtin_amdgcn_permlane16_swap(pk(v0[2], v0[3]), pk(v1[2], v1[3]), false, false);
                    store16((u32x4){a[0], b[0], a[1], b[1]}, rw,
                            WS_ABT + layer * 4 * MB + (et >> 3) * 2 * MB + ((2 * (et & 7) + E) * 128 + sbg + s) * 1024 + lane * 16);
                }
            }
        };

    phase1(std::true_type{});
    for (int layer = 0; layer < 2; ++layer) {
        const u64 tag = epoch + 1 + 6 * layer;
        auto finish_strip = [&](f32x4 (&v)[4], bool last, unsigned xo, unsigned sto) {
            const int w = opq_s(wid), lane = lane_id(), i16 = lane & 15, q = lane >> 4;
            float sum = 0.f, sq = 0.f;
            u32x2 res[4];
            {
                const u32x4 r0 = *(LAS u32x4*)(L + LDS_M + w * 8192 + T_RES + lane * 16), r1 = *(LAS u32x4*)(L + LDS_M + w * 8192 + T_RES + 1024 + lane * 16);
                res[0] = (u32x2){r0[0], r0[1]}; res[1] = (u32x2){r0[2], r0[3]}; res[2] = (u32x2){r1[0], r1[1]}; res[3] = (u32x2){r1[2], r1[3]};
            }
#pragma unroll
            for (int b = 0; b < 4; ++b) {
                res[b][0] = pk(lo16(res[b][0]) + v[b][0], hi16(res[b][0]) + v[b][1]);
                res[b][1] = pk(lo16(res[b][1]) + v[b][2], hi16(res[b][1]) + v[b][3]);
                const float x0 = lo16(res[b][0]), x1 = hi16(res[b][0]), x2 = lo16(res[b][1]), x3 = hi16(res[b][1]);
                sum += (x0 + x1) + (x2 + x3);
                sq += (x0 * x0 + x1 * x1) + (x2 * x2 + x3 * x3);
            }
            const u32x4 o0 = gather8(res[0][0], res[0][1], res[1][0], res[1][1]);
            const u32x4 o1 = gather8(res[2][0], res[2][1], res[3][0], res[3][1]);
            if (last) {
                const rsrc_t rxo = mk(out, S * D * 2);
                const unsigned a = (64 * rt + 16 * w + i16) * 1024 + (64 * ct + 8 * q) * 2;
                __builtin_amdgcn_raw_buffer_store_b128(o0, rxo, a, 0, 0);
                __builtin_amdgcn_raw_buffer_store_b128(o1, rxo, a + 64, 0, 0);
            } else {
                const unsigned a = xo + ((4 * rt + w) * 16 + 2 * ct) * 1024 + lane * 16;
                store16(o0, rw, a); store16(o1, rw, a + 1024);
                *(LAS u32x4*)(L + LDS_M + w * 8192 + T_RES + lane * 16) = (u32x4){res[0][0], res[0][1], res[1][0], res[1][1]};
                *(LAS u32x4*)(L + LDS_M + w * 8192 + T_RES + 1024 + lane * 16) = (u32x4){res[2][0], res[2][1], res[3][0], res[3][1]};
                sum = sum16(sum); sq = sum16(sq);
                sum = sum32(sum); sq = sum32(sq);
                __builtin_amdgcn_raw_buffer_store_b64((u32x2){__float_as_uint(sum), __float_as_uint(sq)}, rw, sto + (((4 * rt + w) * 8 + ct) * 16 + i16) * 8, 0, 16);
            }
        };

        {
            const int w = opq_s(wid), lane = lane_id(), i16 = lane & 15, q = lane >> 4;
            const int part = j2 >> 1, half = j2 & 1, r32 = lane & 31, hh = lane >> 5;
            const rsrc_t rws = mk((part ? wsim : wsre) + ((size_t)128 * T2 * S + 1024 * half) * 2, (128 * S - 1024 * half) * 2);
            const unsigned vsE = (lane >> 3) * 4096 + (((lane & 7) ^ (lane >> 4)) * 16), vsO = vsE ^ 64;
            const int w4_ = opq_s(wid);
            const unsigned lw4 = ldsb + w4_ * 4096, wsb = 32 * w4_ * 4096;
            auto dma_p2 = [&](int c, int p) { dma(lw4, (c & 7) * 16384 + p * 1024, (p & 1) ? vsO : vsE, rws, wsb, p * 32768 + c * 128); };
            dma_p2n(0, 4);
            wait_vm<16>();
            bar();
            publish(rw, 6 * layer + 0, blk, tag, w);
            STAMP(3 + 16 * layer);
            if (layer == 0) {
                u32x2 res0[4];
                const rsrc_t rx = mk(x, S * D * 2);
                const unsigned vr = (64 * rt + 16 * w + i16) * 1024 + (64 * ct + 4 * q) * 2;
#pragma unroll
                for (int b = 0; b < 4; ++b) aload2(res0[b], vr + b * 32, rx, 0u);
                asm volatile("s_waitcnt vmcnt(0)" : "+v"(res0[0]), "+v"(res0[1]), "+v"(res0[2]), "+v"(res0[3]) :: "memory");
                *(LAS u32x4*)(L + LDS_M + w * 8192 + T_RES + lane * 16) = (u32x4){res0[0][0], res0[0][1], res0[1][0], res0[1][1]};
                *(LAS u32x4*)(L + LDS_M + w * 8192 + T_RES + 1024 + lane * 16) = (u32x4){res0[2][0], res0[2][1], res0[3][0], res0[3][1]};
            }
            poll8(rw, 6 * layer + 0, blk_p1(8 * half + (lane & 7), 8 * part + 2 * E2 + (w >> 1)), tag, dead);
            bar();
            STAMP(4 + 16 * layer);
            const unsigned so0 = WS_ABT + layer * 4 * MB + part * 2 * MB + ((4 * E2 + w) * 128 + 64 * half) * 1024, vo = lane * 16;
            bf16x8 act[P2_LA + 2];
            auto load_act = [&](int v) { aload(act[v % (P2_LA + 2)], vo, rw, so0, v * 1024); };
#pragma unroll
            for (int v = 0; v < P2_LA; ++v) load_act(v);
            f32x16 acc[4];
#pragma unroll
            for (int t = 0; t < 4; ++t)
#pragma unroll
                for (int r = 0; r < 16; ++r) acc[t][r] = 0.f;
            unsigned rb[2][4];
#pragma unroll
            for (int k = 0; k < 4; ++k) { rb[0][k] = r32 * 128 + (((2 * k + hh) ^ ((r32 >> 1) & 7)) * 16); rb[1][k] = rb[0][k] + 65536; }
            auto ws_frag = [&](int n) {
                const int v = n >> 2, t = n & 3, c = v >> 2;
                return ldsA(L, rb[(c & 7) >> 2][v & 3] + (c & 3) * 16384 + t * 4096);
            };
            const int ws_ = opq_s(wid);
            const unsigned lw = ldsb + ws_ * 8192, g_sb = 64 * ws_ * 3584 + 112 * CT;
            unsigned gg0 = 0, gg1 = 0;
            bf16x8 wf[8];
#pragma unroll
            for (int n = 0; n < 6; ++n) wf[n] = ws_frag(n);
            sfor<64>([&](auto V) {
                constexpr int v = CI(V);
                asm volatile("s_waitcnt vmcnt(%1)" : "+v"(act[v % (P2_LA + 2)]) : "i"(p2_wait(v)) : "memory");
                if constexpr (v == 43) { const int l2 = lane_id(); gg0 = p3_voff(l2, 0, 0); gg1 = p3_voff(l2, 0, 1); }
                sfor<4>([&](auto TB) {
                    constexpr int t = CI(TB), n = 4 * v + t;
                    acc[t] = mfma32(act[v % (P2_LA + 2)], wf[n & 7], acc[t]);
                    SCHED();
                    if constexpr ((v & 3) == 2 && t == 2 && v < 60) bar();
                    if constexpr (n + 6 < 256) wf[(n + 6) & 7] = ws_frag(n + 6);
                    if constexpr (t == 1 && p2_dma(v)) {
                        if constexpr (v <= 39) dma_p2(((v - 8) >> 2) + 8, v & 3);
                        else dma_p3((v - 43) >> 3, (v - 43) & 7, ((v - 43) & 1) ? gg1 : gg0, lw, g_sb);
                    }
                    if constexpr (t == 3 && v + P2_LA < 64) load_act(v + P2_LA);
                    SCHED();
                });
            });
            STAMP(5 + 16 * layer);
            const unsigned pgrp = WS_PART + layer * 8 * MB + (T2 * 4 + E2) * 131072;
            const int lx = lane_id(), i16x = lx & 15, tup = (lx >> 4) & 1, hx = lx >> 5;
#pragma unroll
            for (int a = 0; a < 2; ++a) {
                const int owner = 2 * a + (w >> 1);
                if (owner == j2) {
#pragma unroll
                    for (int u = 0; u < 2; ++u)
#pragma unroll
                        for (int g = 0; g < 4; ++g) {
                            const f32x16& c = acc[2 * a + u];
                            *(LAS f32x4*)(L + LDS_M + (2 * u + tup) * 8192 + (2 * (w & 1) + (g >> 1)) * 1024 + (i16x + 16 * (2 * (g & 1) + hx)) * 16) =
                                (f32x4){c[4 * g], c[4 * g + 1], c[4 * g + 2], c[4 * g + 3]};
                        }
                } else {
                    const unsigned po = pgrp + owner * 32768 + j2 * 8192 + (w & 1) * 1024 + tup * 2048 + (i16x + 16 * hx) * 16;
#pragma unroll
                    for (int u = 0; u < 2; ++u)
#pragma unroll
                        for (int qh = 0; qh < 2; ++qh) {
                            const f32x16& c = acc[2 * a + u];
                            store16((u32x4){pk(c[4 * qh], c[4 * qh + 1]), pk(c[4 * qh + 2], c[4 * qh + 3]),
                                            pk(c[8 + 4 * qh], c[8 + 4 * qh + 1]), pk(c[8 + 4 * qh + 2], c[8 + 4 * qh + 3])}, rw, po + u * 4096 + qh * 512);
                        }
                }
            }
            wait_vm<0>();
            wait_lds();
            bar();
            publish(rw, 6 * layer + 1, blk, tag + 1, w);
            STAMP(13 + 16 * layer);
            const unsigned gu0 = p3_voff(lx, 1, 0), gu1 = p3_voff(lx, 1, 1), vx = lx * 16;
#pragma unroll
            for (int n = 0; n < 8; ++n) dma_p3(2, n, (n & 1) ? gu1 : gu0, lw, g_sb);
            poll8(rw, 6 * layer + 1, (blk & ~3) | (lx & 3), tag + 1, dead);
            STAMP(14 + 16 * layer);
            bf16x8 pv[3][2];
#pragma unroll
            for (int d = 1; d < 4; ++d)
#pragma unroll
                for (int h = 0; h < 2; ++h) aload(pv[d - 1][h], vx, rw, pgrp + j2 * 32768 + ((j2 + d) & 3) * 8192 + w * 2048, h * 1024);
            f32x4 v[4];
#pragma unroll
            for (int b = 0; b < 4; ++b) v[b] = *(LAS f32x4*)(L + LDS_M + w * 8192 + b * 1024 + vx);
            asm volatile("s_waitcnt vmcnt(0)" : "+v"(pv[0][0]), "+v"(pv[0][1]), "+v"(pv[1][0]), "+v"(pv[1][1]), "+v"(pv[2][0]), "+v"(pv[2][1]) :: "memory");
#pragma unroll
            for (int d = 0; d < 3; ++d)
#pragma unroll
                for (int h = 0; h < 2; ++h) {
                    const u32x4 p = __builtin_bit_cast(u32x4, pv[d][h]);
                    v[2 * h] += (f32x4){lo16(p[0]), hi16(p[0]), lo16(p[1]), hi16(p[1])};
                    v[2 * h + 1] += (f32x4){lo16(p[2]), hi16(p[2]), lo16(p[3]), hi16(p[3])};
                }
            finish_strip(v, false, WS_X1 + layer * 2 * MB, WS_ST + layer * ST_BYTES);
            wait_vm<0>();
            bar();
            publish(rw, 6 * layer + 2, blk, tag + 2, w);
            STAMP(6 + 16 * layer);
#pragma unroll
            for (int n = 0; n < 8; ++n) dma_p3(3, n, (n & 1) ? gu1 : gu0, lw, g_sb);
        }

        {
            const int w = opq_s(wid), lane = lane_id(), i16 = lane & 15, q = lane >> 4;
            poll8(rw, 6 * layer + 2, blk_p24(4 * RT + w, lane & 7), tag + 2, dead);
            if (layer == 0 && blk == 0 && w == 0) {
                u64 e2 = epoch; asm volatile("" : "+s"(e2)); e2 += 64;
                __builtin_amdgcn_raw_buffer_store_b64((u32x2){(unsigned)e2, (unsigned)(e2 >> 32)}, rw, 0, 0, 16);
                wait_vm<0>();
            }
            STAMP(7 + 16 * layer);
            const int sb0 = 16 * RT + 4 * w;
            u32x2 sa[4], sb_[4];
            bf16x8 act[P3_LA + 1][4];
            const unsigned vo = lane * 16, xo = WS_X1 + layer * 2 * MB + sb0 * 16384;
            auto load_x = [&](int g, int t) { aload(act[g % (P3_LA + 1)][t], vo, rw, xo, t * 16384 + (g & 15) * 1024); };
#pragma unroll
            for (int g = 0; g < P3_LA; ++g)
#pragma unroll
                for (int t = 0; t < 4; ++t) load_x(g, t);
            bar();
            const int qq = (lane >> 2) & 3, p4 = lane & 3;
            unsigned tb_[7];
            {
                const unsigned f = 2 * (qq >> 1) + 4 * (q & 1), rowb = (8 * q + qq) * 128 + 8 * (p4 & 1);
#pragma unroll
                for (int b = 0; b < 6; ++b) tb_[b] = (b & 1) * 65536 + rowb + (((2 * (b >> 1) + (p4 >> 1)) ^ f ^ (b & 1)) * 16);
                tb_[6] = (p4 >> 1) * 65536 + rowb + ((6 ^ f ^ (p4 >> 1)) * 16);
            }
            if (layer == 0) {
                f32x4 ac[7];
#pragma unroll
                for (int b = 0; b < 7; ++b) ac[b] = (f32x4){0.f, 0.f, 0.f, 0.f};
#pragma unroll
                for (int k = 0; k < 4; ++k)
#pragma unroll
                    for (int b = 0; b < 7; ++b) ac[b] = mfma(ldsT(L, tb_[b] + w * 16384 + k * 4096), ones, ac[b]);
#pragma unroll
                for (int b = 0; b < 7; ++b) *(LAS f32x4*)(L + T_CS3P + ((w * 7 + b) * 4 + q) * 16) = ac[b];
                wait_lds();
                bar();
                if (w == 0) {
                    const int l = lane < 28 ? lane : 27;
                    f32x4 s = *(LAS f32x4*)(L + T_CS3P + l * 16);
#pragma unroll
                    for (int k = 1; k < 4; ++k) s += *(LAS f32x4*)(L + T_CS3P + (k * 28 + l) * 16);
                    *(LAS f32x4*)(L + T_CS3 + l * 16) = s;
                }
                wait_lds();
                bar();
            }
            auto w_lo = [&](int ks, int b) { return __builtin_amdgcn_ds_read_tr16_b64_v4i16((LAS s16x4*)(L + tb_[b] + ks * 4096)); };
            auto w_hi = [&](int ks, int b) { return __builtin_amdgcn_ds_read_tr16_b64_v4i16((LAS s16x4*)(L + tb_[b] + ks * 4096 + 512)); };
            const unsigned sto = WS_ST + layer * ST_BYTES + sb0 * 1024, sv0 = (2 * q * 16 + i16) * 8;
            const unsigned ho = WS_H + layer * 7 * MB, hw = 14 * (CT >> 3);
            float mean[4], sig[4], kk_[4];
            auto glu = [&](float G, float U, float cg, float cu, int t) {
                const float e = __builtin_amdgcn_exp2f(kk_[t] * (mean[t] * cg - G));
                return (U - mean[t] * cu) * __builtin_amdgcn_rcpf(sig[t] * e + sig[t]);
            };
            u32x2 fl;
            {
                f32x4 a3[4][4];
#pragma unroll
                for (int b = 0; b < 4; ++b)
#pragma unroll
                    for (int t = 0; t < 4; ++t) a3[b][t] = (f32x4){0.f, 0.f, 0.f, 0.f};
                bf16x8 wf[4];
#pragma unroll
                for (int b = 0; b < 3; ++b) wf[b] = ldsT(L, tb_[b]);
                sfor<16>([&](auto KS) {
                    constexpr int ks = CI(KS), g = ks, sl = g % (P3_LA + 1);
                    wait4<p3_wait(g)>(act[sl][0], act[sl][1], act[sl][2], act[sl][3]);
                    if constexpr (ks == 0) STAMP(32 + 8 * layer);
                    sfor<4>([&](auto B) {
                        constexpr int b = CI(B), pb = b == 0 ? 3 : b - 1, pks = b == 0 ? ks : ks + 1;
                        s16x4 lo, hi;
                        a3[b][0] = mfma(wf[b], act[sl][0], a3[b][0]);
                        SCHED();
                        if constexpr (pks < 16) lo = w_lo(pks, pb);
                        SCHED();
                        a3[b][1] = mfma(wf[b], act[sl][1], a3[b][1]);
                        SCHED();
                        if constexpr (pks < 16) { hi = w_hi(pks, pb); wf[pb] = __builtin_shufflevector(lo, hi, 0, 1, 2, 3, 4, 5, 6, 7); }
                        SCHED();
                        a3[b][2] = mfma(wf[b], act[sl][2], a3[b][2]);
                        SCHED();
                        load_x(g + P3_LA, b);
                        SCHED();
                        a3[b][3] = mfma(wf[b], act[sl][3], a3[b][3]);
                        SCHED();
                        if constexpr (g == 8 && b == 3) {
#pragma unroll
                            for (int t = 0; t < 4; ++t) { aload2(sa[t], sv0, rw, sto + t * 1024); aload2(sb_[t], sv0 + 128, rw, sto + t * 1024); }
                            SCHED();
                        }
                    });
                });
                STAMP(33 + 8 * layer);
                asm volatile("" : "+v"(sa[0]), "+v"(sa[1]), "+v"(sa[2]), "+v"(sa[3]), "+v"(sb_[0]), "+v"(sb_[1]), "+v"(sb_[2]), "+v"(sb_[3]));
                {
                    float v[4];
                    combine_stats<4>(sa, sb_, mean, v);
#pragma unroll
                    for (int t = 0; t < 4; ++t) {
                        const float vv = v[t] + EPS, r = __builtin_amdgcn_rsqf(vv);
                        sig[t] = vv * r; kk_[t] = 1.44269504088896f * r;
                    }
                }
                unsigned H[2][4][2];
#pragma unroll
                for (int j = 0; j < 2; ++j) {
                    const f32x4 cg = *(LAS f32x4*)(L + T_CS3 + ((2 * j) * 4 + q) * 16), cu = *(LAS f32x4*)(L + T_CS3 + ((2 * j + 1) * 4 + q) * 16);
#pragma unroll
                    for (int t = 0; t < 4; ++t) {
                        float hv[4];
#pragma unroll
                        for (int r = 0; r < 4; ++r) hv[r] = glu(a3[2 * j][t][r], a3[2 * j + 1][t][r], cg[r], cu[r], t);
                        H[j][t][0] = pk(hv[0], hv[1]); H[j][t][1] = pk(hv[2], hv[3]);
                    }
                }
#pragma unroll
                for (int t = 0; t < 4; ++t)
                    store16(gather8(H[0][t][0], H[0][t][1], H[1][t][0], H[1][t][1]), rw, ho + ((sb0 + t) * 56 + hw + (CT & 7)) * 1024 + vo);
                SCHED();
            }
            STAMP(34 + 8 * layer);
            f32x4 a3[3][4];
            {
#pragma unroll
                for (int b = 0; b < 3; ++b)
#pragma unroll
                    for (int t = 0; t < 4; ++t) a3[b][t] = (f32x4){0.f, 0.f, 0.f, 0.f};
                bf16x8 wf[3];
#pragma unroll
                for (int b = 0; b < 2; ++b) wf[b] = ldsT(L, tb_[4 + b]);
                sfor<16>([&](auto KS) {
                    constexpr int ks = CI(KS), g = 16 + ks, sl = g % (P3_LA + 1);
                    wait4<p3_wait(g)>(act[sl][0], act[sl][1], act[sl][2], act[sl][3]);
                    if constexpr (g == P3_PUB) {
                        wait_vm<p3_wait_pub()>();
                        bar();
                        publish(rw, 6 * layer + 3, blk, tag + 3, w);
                        SCHED();
                    }
                    sfor<3>([&](auto B) {
                        constexpr int b = CI(B), pb = b == 0 ? 2 : b - 1, pks = b == 0 ? ks : ks + 1;
                        s16x4 lo, hi;
                        a3[b][0] = mfma(wf[b], act[sl][0], a3[b][0]);
                        SCHED();
                        if constexpr (pks < 16) lo = w_lo(pks, 4 + pb);
                        SCHED();
                        a3[b][1] = mfma(wf[b], act[sl][1], a3[b][1]);
                        SCHED();
                        if constexpr (pks < 16) { hi = w_hi(pks, 4 + pb); wf[pb] = __builtin_shufflevector(lo, hi, 0, 1, 2, 3, 4, 5, 6, 7); }
                        SCHED();
                        a3[b][2] = mfma(wf[b], act[sl][2], a3[b][2]);
                        SCHED();
                        if constexpr (g + P3_LA < 32) load_x(g + P3_LA, b);
                        SCHED();
                        a3[b][3] = mfma(wf[b], act[sl][3], a3[b][3]);
                        SCHED();
                        if constexpr (b == 2 && g + P3_LA < 32) { load_x(g + P3_LA, 3); SCHED(); }
                        if constexpr (b == 2 && g == 30) {
                            aload2(fl, (unsigned)blk_p3(rt >> 2, 8 * w + (lane & 7)) * 256u, rw, WS_FLAGS + (6 * layer + 3) * 65536u);
                            SCHED();
                        }
                    });
                });
            }
            wait_lds();
            bar();
            STAMP(8 + 16 * layer);

            const unsigned vE = (lane >> 3) * 1024 + (((lane & 7) ^ ((lane >> 3) & 2)) * 16), vO = vE ^ 64;
            const int ws_ = opq_s(wid);
            const unsigned lw = ldsb + ws_ * 8192, dsb = 448 * ws_ * 1024;
            const rsrc_t rwd = mk(wdn + 128 * ct, F * D * 2 - 128 * ct);
            auto dma_p4 = [&](int c, int p) {
                dma(lw, ((c + 1) & 3) * REG + p * 1024, (p & 1) ? vO : vE, rwd, dsb, p4_row(2 * c + (p >> 2), p & 3) * 1024);
            };
#pragma unroll
            for (int n = 0; n < 16; ++n) dma_p4(n >> 3, n & 7);
            asm volatile("s_waitcnt vmcnt(16)" : "+v"(fl) :: "memory");
            if (!__all((((u64)fl[1] << 32) | fl[0]) == tag + 3)) poll8(rw, 6 * layer + 3, blk_p3(rt >> 2, 8 * w + (lane & 7)), tag + 3, dead);
            STAMP(10 + 16 * layer);
            unsigned so[4];
#pragma unroll
            for (int d = 0; d < 4; ++d) so[d] = ho + ((4 * rt + ((w + d) & 3)) * 56 + 14 * w) * 1024;
            bf16x8 hact[6][4];
            auto load_h = [&](int u, int d) { aload(hact[u % 6][d], vo, rw, so[d], u * 1024); };
#pragma unroll
            for (int u = 0; u < 5; ++u)
#pragma unroll
                for (int d = 0; d < 4; ++d) load_h(u, d);
            SCHED();
            {
                const f32x4 cg = *(LAS f32x4*)(L + T_CS3 + (4 * 4 + q) * 16), cu = *(LAS f32x4*)(L + T_CS3 + (5 * 4 + q) * 16);
                const f32x4 mg = *(LAS f32x4*)(L + T_CS3 + (6 * 4 + (q & 1)) * 16), mu = *(LAS f32x4*)(L + T_CS3 + (6 * 4 + 2 + (q & 1)) * 16);
                const unsigned beta = 3 * (CT & 7) + q;
                const unsigned hb = q < 3 ? ho + (sb0 * 56 + hw + 8 + (beta >> 2)) * 1024 + ((beta & 3) * 16 + i16) * 16 : 0x7ff00000u;
#pragma unroll
                for (int t = 0; t < 4; ++t) {
                    float hv[4], hm[4];
#pragma unroll
                    for (int r = 0; r < 4; ++r) {
                        hv[r] = glu(a3[0][t][r], a3[1][t][r], cg[r], cu[r], t);
                        auto sw = __builtin_amdgcn_permlane32_swap(__float_as_uint(a3[2][t][r]), __float_as_uint(a3[2][t][r]), false, false);
                        hm[r] = glu(__uint_as_float(sw[0]), __uint_as_float(sw[1]), mg[r], mu[r], t);
                    }
                    store16(gather8(pk(hv[0], hv[1]), pk(hv[2], hv[3]), pk(hm[0], hm[1]), pk(hm[2], hm[3])), rw, hb + t * 56 * 1024);
                }
                SCHED();
            }
            STAMP(35 + 8 * layer);
#pragma unroll
            for (int n = 16; n < 32; ++n) dma_p4(n >> 3, n & 7);
            f32x4 acc[4][4];
#pragma unroll
            for (int b = 0; b < 4; ++b)
#pragma unroll
                for (int d = 0; d < 4; ++d) acc[b][d] = (f32x4){0.f, 0.f, 0.f, 0.f};
            unsigned wb_[2][4];
#pragma unroll
            for (int b = 0; b < 4; ++b) {
                wb_[0][b] = w * 8192 + (8 * q + qq) * 128 + (((2 * b + (p4 >> 1)) ^ ((qq & 2) + 4 * (q & 1))) * 16) + 8 * (p4 & 1);
                wb_[1][b] = wb_[0][b] + 2 * REG;
            }
            auto wd_off = [&](int u, int b) { return wb_[(((u >> 1) + 1) & 3) >> 1][b] + (((u >> 1) + 1) & 1) * REG + (u & 1) * 4096; };
            bf16x8 wf[2][4];
            u32x2 flb;
            sfor<14>([&](auto U) {
                constexpr int u = CI(U);
                wait4<p4_wait(u)>(hact[u % 6][0], hact[u % 6][1], hact[u % 6][2], hact[u % 6][3]);
                if constexpr (u == 0) {
#pragma unroll
                    for (int b = 0; b < 4; ++b) wf[0][b] = ldsT(L, wd_off(0, b));
                }
                if constexpr (u == 1) {
                    bar();
                    publish(rw, 6 * layer + 4, blk, tag + 4, w);
                    STAMP(9 + 16 * layer);
                    aload2(flb, (unsigned)blk_p3(rt >> 2, 8 * w + (lane & 7)) * 256u, rw, WS_FLAGS + (6 * layer + 4) * 65536u);
                    SCHED();
                }
                if constexpr (u == 3) {
                    asm volatile("" : "+v"(flb));
                    if (!__all((((u64)flb[1] << 32) | flb[0]) == tag + 4)) poll8(rw, 6 * layer + 4, blk_p3(rt >> 2, 8 * w + (lane & 7)), tag + 4, dead);
                    SCHED();
                }
                sfor<8>([&](auto J) {
                    constexpr int j = CI(J), b = j >> 1, d0 = 2 * (j & 1);
                    acc[b][d0] = mfma(wf[u & 1][b], hact[u % 6][d0], acc[b][d0]);
                    acc[b][d0 + 1] = mfma(wf[u & 1][b], hact[u % 6][d0 + 1], acc[b][d0 + 1]);
                    SCHED();
                    if constexpr (j < 4 && u < 13) wf[(u + 1) & 1][j] = ldsT(L, wd_off(u + 1, j));
                    if constexpr ((j & 1) == 0) {
                        constexpr int p = 4 * ((u + 1) & 1) + (j >> 1);
                        if constexpr (u >= 1 && u <= 6) dma_p4(((u - 1) >> 1) + 4, p);
                    } else if constexpr (u <= 8) load_h(u + 5, j >> 1);
                    SCHED();
                });
            });
            STAMP(11 + 16 * layer);
            if (layer == 0) dma_p1_all();
#pragma unroll
            for (int d = 1; d < 4; ++d)
#pragma unroll
                for (int b = 0; b < 4; ++b)
                    *(LAS f32x4*)(L + w * 8192 + lane * 16 + (d < 3 ? 3 * REG + (d - 1) * 4096 : LDS_M) + b * 1024) = acc[b][d];
            wait_lds();
            bar();
            if (layer == 0) dma_p2n(4, 6);
            f32x4 v[4];
#pragma unroll
            for (int b = 0; b < 4; ++b) v[b] = acc[b][0];
#pragma unroll
            for (int d = 1; d < 4; ++d) {
                const int src = (w - d) & 3;
#pragma unroll
                for (int b = 0; b < 4; ++b)
                    v[b] += *(LAS f32x4*)(L + src * 8192 + lane * 16 + (d < 3 ? 3 * REG + (d - 1) * 4096 : LDS_M) + b * 1024);
            }
            if (layer == 0) {
                finish_strip(v, false, WS_X2, WS_ST + 2 * ST_BYTES);
                wait_vm<0>();
                bar();
                publish(rw, 5, blk, tag + 5, w);
                STAMP(12);
                dma_p2n(6, 8);
            } else {
                finish_strip(v, true, 0, 0);
                STAMP(28);
            }
        }
        if (layer == 1) break;
        phase1(std::false_type{});
    }
}

}

extern "C" void kernel_launch(void* const* d_in, const int* in_sizes, int n_in,
                              void* d_out, int out_size, void* d_ws, size_t ws_size,
                              hipStream_t stream) {
    (void)in_sizes; (void)n_in; (void)out_size; (void)ws_size;
    (void)hipFuncSetAttribute((const void*)fn::fnet_glu_kernel, hipFuncAttributeMaxDynamicSharedMemorySize, fn::LDS_BYTES);
    fn::fnet_glu_kernel<<<256, 256, fn::LDS_BYTES, stream>>>((const char*)d_in[0], (const char*)d_in[1], (const char*)d_in[2],
        (const char*)d_in[3], (const char*)d_in[4], (const char*)d_in[6], (const char*)d_in[7], (const char*)d_in[8],
        (char*)d_out, (char*)d_ws);
}
```

```cpp
#include <hip/hip_runtime.h>
#include <hip/hip_bf16.h>
#include <stdint.h>
#include <type_traits>
#include <utility>

#define DI __device__ __forceinline__
#define LAS __attribute__((address_space(3)))
#define GAS __attribute__((address_space(1)))
typedef short bf16x8 __attribute__((ext_vector_type(8)));
typedef short s16x4 __attribute__((ext_vector_type(4)));
typedef float f32x4 __attribute__((ext_vector_type(4)));
typedef float f32x2 __attribute__((ext_vector_type(2)));
typedef float f32x16 __attribute__((ext_vector_type(16)));
typedef unsigned u32x4 __attribute__((ext_vector_type(4)));
typedef unsigned u32x2 __attribute__((ext_vector_type(2)));
typedef __bf16 bf16x2 __attribute__((ext_vector_type(2)));
typedef unsigned long long u64;
typedef __amdgpu_buffer_rsrc_t rsrc_t;

#define WARM 2
namespace fn {

constexpr int S = 2048, D = 512, F = 1792;
constexpr unsigned MB = 1u << 20;
constexpr unsigned WS_FLAGS = 4096;
constexpr unsigned WS_ABT = 1 * MB;
constexpr unsigned WS_X1 = 9 * MB;
constexpr unsigned WS_X2 = 13 * MB;
constexpr unsigned WS_H = 15 * MB;
constexpr unsigned WS_ST = 29 * MB;
constexpr unsigned WS_STAMP = 30 * MB;
constexpr unsigned WS_PART = 32 * MB;
constexpr unsigned ST_BYTES = 128 * 1024;

constexpr unsigned REG = 32768;
constexpr unsigned LDS_M = 4 * REG;
constexpr unsigned T_RES = 4096;
constexpr unsigned T_STAT = 6144;
constexpr unsigned T_CS1 = LDS_M + 6400;
constexpr unsigned T_CS3 = LDS_M + 8192 + 6400;
constexpr unsigned T_CS3P = LDS_M + 16384 + 6400;
constexpr unsigned T_DUMMY = LDS_M + 24576 + 6400;
constexpr unsigned LDS_BYTES = 163840;
#define CODE_KB 52
constexpr unsigned CODE_PREFETCH_BYTES = CODE_KB * 1024;
constexpr unsigned SPIN_LIMIT = 200000;
constexpr float EPS = 1e-5f;

#define TLBW 0
#define WDEPTH 2
#define WDELAY 0
DI rsrc_t mk(const void* p, unsigned bytes) { return __builtin_amdgcn_make_buffer_rsrc((void*)p, 0, bytes, 0x00020000); }
DI void dma(unsigned lds, int LI, unsigned voff, rsrc_t r, unsigned sb, int SI) {
    unsigned t;
    asm volatile("s_add_u32 m0, %1, %5\n\ts_add_u32 %0, %4, %6\n\tbuffer_load_dwordx4 %2, %3, %0 offen lds"
                 : "=&s"(t) : "s"(lds), "v"(voff), "s"(r), "s"(sb), "i"(LI), "i"(SI) : "m0", "scc", "memory");
}
DI void aload(bf16x8& d, unsigned voff, rsrc_t r, unsigned sb, int SI) {
    asm volatile("buffer_load_dwordx4 %0, %1, %2, %3 offen offset:%4 sc1"
                 : "=v"(d) : "v"(voff), "s"(r), "s"(sb + (SI & ~4095)), "i"(SI & 4095) : "memory");
}
DI void aload2(u32x2& d, unsigned voff, rsrc_t r, unsigned sb) {
    asm volatile("buffer_load_dwordx2 %0, %1, %2, %3 offen sc1" : "=v"(d) : "v"(voff), "s"(r), "s"(sb) : "memory");
}
template <int... Is, class Fn> DI void sfor_impl(std::integer_sequence<int, Is...>, Fn&& f) { (f(std::integral_constant<int, Is>{}), ...); }
template <int N, class Fn> DI void sfor(Fn&& f) { sfor_impl(std::make_integer_sequence<int, N>{}, f); }
#define CI(x) decltype(x)::value
DI int lane_id() { int l; asm volatile("v_mbcnt_lo_u32_b32 %0, -1, 0\n\tv_mbcnt_hi_u32_b32 %0, -1, %0" : "=v"(l)); return l; }
DI int opq_s(int v) { asm volatile("" : "+s"(v)); return v; }
template <int N> DI void wait_vm() { asm volatile("s_waitcnt vmcnt(%0)" :: "i"(N) : "memory"); }
template <int N> DI void wait2(bf16x8& a, bf16x8& b) { asm volatile("s_waitcnt vmcnt(%2)" : "+v"(a), "+v"(b) : "i"(N) : "memory"); }
template <int N> DI void wait4(bf16x8& a, bf16x8& b, bf16x8& c, bf16x8& d) {
    asm volatile("s_waitcnt vmcnt(%4)" : "+v"(a), "+v"(b), "+v"(c), "+v"(d) : "i"(N) : "memory");
}
template <int N> DI void wait8(bf16x8 (&a)[8]) {
    asm volatile("s_waitcnt vmcnt(%8)" : "+v"(a[0]), "+v"(a[1]), "+v"(a[2]), "+v"(a[3]), "+v"(a[4]), "+v"(a[5]), "+v"(a[6]), "+v"(a[7]) : "i"(N) : "memory");
}
DI void wait_lds() { asm volatile("s_waitcnt lgkmcnt(0)" ::: "memory"); }
DI void bar() { asm volatile("" ::: "memory"); __builtin_amdgcn_s_barrier(); asm volatile("" ::: "memory"); }
#define SCHED() __builtin_amdgcn_sched_barrier(0)
DI f32x4 mfma(bf16x8 a, bf16x8 b, f32x4 c) { return __builtin_amdgcn_mfma_f32_16x16x32_bf16(a, b, c, 0, 0, 0); }
DI f32x16 mfma32(bf16x8 a, bf16x8 b, f32x16 c) { return __builtin_amdgcn_mfma_f32_32x32x16_bf16(a, b, c, 0, 0, 0); }
DI unsigned pk(float lo, float hi) { f32x2 v = {lo, hi}; return __builtin_bit_cast(unsigned, __builtin_convertvector(v, bf16x2)); }
DI float lo16(unsigned u) { return __uint_as_float(u << 16); }
DI float hi16(unsigned u) { return __uint_as_float(u & 0xffff0000u); }
DI float sum16(float v) { auto r = __builtin_amdgcn_permlane16_swap(__float_as_uint(v), __float_as_uint(v), false, false); return __uint_as_float(r[0]) + __uint_as_float(r[1]); }
DI float sum32(float v) { auto r = __builtin_amdgcn_permlane32_swap(__float_as_uint(v), __float_as_uint(v), false, false); return __uint_as_float(r[0]) + __uint_as_float(r[1]); }
DI float sumq(float v) { return sum32(sum16(v)); }
DI u32x4 gather8(unsigned x0, unsigned x1, unsigned y0, unsigned y1) {
    auto a = __builtin_amdgcn_permlane32_swap(x0, y0, false, false);
    auto b = __builtin_amdgcn_permlane32_swap(x1, y1, false, false);
    auto c = __builtin_amdgcn_permlane16_swap(a[0], a[1], false, false);
    auto d = __builtin_amdgcn_permlane16_swap(b[0], b[1], false, false);
    return (u32x4){c[0], d[0], c[1], d[1]};
}
DI void store16(u32x4 v, rsrc_t r, unsigned voff) { __builtin_amdgcn_raw_buffer_store_b128(v, r, voff, 0, 16); }
DI bf16x8 ldsA(LAS char* L, unsigned off) { return *(LAS bf16x8*)(L + off); }
DI bf16x8 ldsT(LAS char* L, unsigned off) {
    s16x4 lo = __builtin_amdgcn_ds_read_tr16_b64_v4i16((LAS s16x4*)(L + off));
    s16x4 hi = __builtin_amdgcn_ds_read_tr16_b64_v4i16((LAS s16x4*)(L + off + 512));
    return __builtin_shufflevector(lo, hi, 0, 1, 2, 3, 4, 5, 6, 7);
}
DI int blk_p24(int rt, int ct) { return 8 * (4 * ((rt >> 1) & 7) + (ct >> 1)) + 4 * (rt >> 4) + 2 * (rt & 1) + (ct & 1); }
DI int blk_p1(int rt1, int et) { return 8 * (8 * (rt1 & 3) + (et & 7)) + 2 * (rt1 >> 2) + (et >> 3); }
DI int blk_p3(int RT, int CT) { return 8 * (8 * (RT & 3) + (CT & 7)) + 2 * (CT >> 3) + (RT >> 2); }

DI void poll8(rsrc_t rw, int phase, int prod, u64 tag, bool& dead) {
    const unsigned voff = (unsigned)prod * 256u, sb = WS_FLAGS + (unsigned)phase * 65536u;
    if (!dead) {
        unsigned spins = 0;
        for (;;) {
            u64 v;
            asm volatile("buffer_load_dwordx2 %0, %1, %2, %3 offen sc1\n\ts_waitcnt vmcnt(0)" : "=v"(v) : "v"(voff), "s"(rw), "s"(sb) : "memory");
            if (__all(v == tag)) break;
            if (++spins > SPIN_LIMIT) { dead = true; break; }
        }
    }
    asm volatile("" ::: "memory");
}
DI void publish(rsrc_t rw, int phase, int blk, u64 tag, int w) {
    if (w == 0)
        __builtin_amdgcn_raw_buffer_store_b64((u32x2){(unsigned)tag, (unsigned)(tag >> 32)}, rw, WS_FLAGS + (unsigned)(phase * 256 + blk) * 256u, 0, 16);
}

#define STAMP(n) do {} while (0)

constexpr int P1_LA = 12;
constexpr int p1_issued(int ks) { return 2 * P1_LA + 2 * (ks < 16 - P1_LA ? ks : 16 - P1_LA); }
constexpr int p1_wait(int ks) { return p1_issued(ks) - 1 - (ks < P1_LA ? 2 * ks + 1 : p1_issued(ks - P1_LA + 1) - 1); }
constexpr int P2_LA = 22;
constexpr int p2_dma(int v) { return (v >= 8 && v <= 39) || (v >= 43 && v <= 50) ? 1 : 0; }
constexpr int p2_ops(int v) { return p2_dma(v) + (v + P2_LA < 64 ? 1 : 0); }
constexpr int p2_issued(int v) { int n = P2_LA; for (int j = 0; j < v; ++j) n += p2_ops(j); return n; }
constexpr int p2_wait(int v) {
    const int pa = v < P2_LA ? v : p2_issued(v - P2_LA + 1) - 1;
    const int c = (v >> 2) + 1;
    const int pd = (v & 3) == 2 && c >= 8 && c < 16 ? p2_issued(4 * (c - 6) + 3) : -1;
    return p2_issued(v) - 1 - (pa > pd ? pa : pd);
}
constexpr int P3_LA = 6;
constexpr int p3_ops(int g) { return (g + P3_LA < 32 ? 4 : 0) + (g == 8 ? 8 : 0) + (g == 15 ? 4 : 0) + (g == 30 ? 1 : 0); }
constexpr int p3_issued(int g) { int n = 4 * P3_LA; for (int j = 0; j < g; ++j) n += p3_ops(j); return n; }
constexpr int p3_xlast(int j) { return p3_issued(j) + 3; }
constexpr int p3_wait(int g) { return p3_issued(g) - 1 - (g < P3_LA ? 4 * g + 3 : p3_xlast(g - P3_LA)); }
constexpr int P3_PUB = 19;
constexpr int p3_wait_pub() { return p3_issued(P3_PUB) - p3_issued(16); }
constexpr int p4_ndma(int u) { return u <= 1 ? 2 : u <= 7 ? 1 : 0; }
constexpr int p4_nact(int u) { return u <= 2 ? 1 : u >= 5 && u <= 7 ? 2 : 0; }
constexpr int p4_ops(int u) { return (u == 3 ? 1 : 0) + 4 * p4_ndma(u) + 4 * p4_nact(u); }
constexpr int p4_issued(int u) { int n = 40; for (int j = 0; j < u; ++j) n += p4_ops(j); return n; }
constexpr int p4_slot_end(int u, int j) {
    int n = p4_issued(u) + (u == 3 ? 1 : 0);
    for (int k = 0; k <= j; ++k) n += (k & 1) ? p4_nact(u) : p4_ndma(u);
    return n - 1;
}
constexpr int p4_wait(int u) {
    int p = u == 0 ? 11 : u == 1 ? 15 : u == 2 ? 27 : u == 3 ? 31 : u == 4 ? 35 : u < 8 ? p4_slot_end(u - 5, 7)
          : p4_slot_end(5 + ((u - 8) >> 1), (u & 1) ? 7 : 3);
    if (u == 1 && p < 39) p = 39;
    if (u == 5 && p < p4_issued(3)) p = p4_issued(3);
    if ((u & 1) && u < 13) {
        const int c = (u + 1) >> 1;
        const int pd = c == 1 ? 23 : c <= 3 ? p4_slot_end(c - 2, 6) : p4_slot_end(2 * c - 5, 6);
        if (pd > p) p = pd;
    }
    return p4_issued(u) - 1 - p;
}
constexpr int p4_row(int u, int qq) { return u < 8 ? 56 * u + 8 * qq : 56 * ((4 * (u - 8) + qq) / 3) + 32 + 8 * ((4 * (u - 8) + qq) % 3); }

template <int N> DI void combine_stats(const u32x2 (&a)[N], const u32x2 (&b)[N], float (&mean)[N], float (&var)[N]) {
    float s[N], t[N];
#pragma unroll
    for (int i = 0; i < N; ++i) { s[i] = __uint_as_float(a[i][0]) + __uint_as_float(b[i][0]); t[i] = __uint_as_float(a[i][1]) + __uint_as_float(b[i][1]); }
#pragma unroll
    for (int i = 0; i < N; ++i) { s[i] = sum16(s[i]); t[i] = sum16(t[i]); }
#pragma unroll
    for (int i = 0; i < N; ++i) { s[i] = sum32(s[i]); t[i] = sum32(t[i]); }
#pragma unroll
    for (int i = 0; i < N; ++i) { mean[i] = s[i] * (1.0f / 512.0f); var[i] = fmaxf(t[i] * (1.0f / 512.0f) - mean[i] * mean[i], 0.f); }
}

__global__ __launch_bounds__(256, 2) void fnet_glu_kernel(const char* __restrict__ x, const char* __restrict__ wsre, const char* __restrict__ wsim,
        const char* __restrict__ wdre, const char* __restrict__ wdim_, const char* __restrict__ wg, const char* __restrict__ wu,
        const char* __restrict__ wdn, char* __restrict__ out, char* __restrict__ ws) {
    extern __shared__ __attribute__((aligned(16))) char smem[];
    LAS char* L = (LAS char*)smem;
    int wid;
    asm volatile("s_nop 1\n\tv_readfirstlane_b32 %0, %1\n\ts_nop 4" : "=s"(wid) : "v"(threadIdx.x >> 6));
    const int blk = blockIdx.x, xcd = blk & 7, idx = blk >> 3;

    const unsigned ldsb = (unsigned)(size_t)L;
    const rsrc_t rw = mk(ws, 48 * MB);
    const int T2 = 8 * (xcd >> 2) + (idx >> 2), E2 = idx & 3, j2 = xcd & 3;
    const int rt = 2 * T2 + (j2 >> 1), ct = 2 * E2 + (j2 & 1);
    const int rt1 = 4 * (xcd >> 1) + (idx >> 3), et = 8 * (xcd & 1) + (idx & 7);
    const int RT = 4 * (xcd & 1) + (idx >> 3), CT = 8 * (xcd >> 1) + (idx & 7);
    STAMP(0);
    if (CODE_PREFETCH_BYTES) {
        u64 pc;
        asm volatile("s_getpc_b64 %0" : "=s"(pc));
        const rsrc_t rc = mk((const void*)(pc & ~(u64)127), CODE_PREFETCH_BYTES);
        const unsigned seg = (unsigned)(idx * 4 + wid) % ((CODE_PREFETCH_BYTES + 8191) / 8192);
        const unsigned o = seg * 8192u + lane_id() * 128u, l = ldsb + T_DUMMY + 768;
        asm volatile("s_mov_b32 m0, %0\n\ts_nop 0\n\tbuffer_load_dword %1, %2, 0 offen lds" :: "s"(l), "v"(o), "s"(rc) : "m0", "memory");
    }

    auto p1_rsrc = [&](unsigned bytes) { return mk(((et >> 3) ? wdim_ : wdre) + (size_t)(et & 7) * 64 * 1024, bytes); };
    auto dma_p1 = [&](int j, int p, rsrc_t r, unsigned v0, unsigned lw, unsigned sb) {
        dma(lw, (32 * j + p) * 1024, v0 ^ (16 * p), r, sb, (32 * j + p) * 1024);
    };
    auto p3_voff = [&](int ln, int up, int odd) -> unsigned {
        const unsigned c = (ln & 7) ^ ((2 * ((ln >> 4) & 1) + 4 * odd) ^ up);
        return (ln >> 3) * 3584 + 16 * (c > 6 ? 6 : c);
    };
    auto dma_p3 = [&](int j, int p, unsigned v, unsigned lw, unsigned sb) {
        dma(lw, j * REG + p * 1024, v, mk((j >> 1) ? wu : wg, D * F * 2), sb, (256 * (j & 1) + 8 * p) * 3584);
    };
    auto dma_p2n = [&](int c0, int c1) {
        const int w = opq_s(wid), ln = lane_id();
        const rsrc_t r = mk(((j2 >> 1) ? wsim : wsre) + ((size_t)128 * T2 * S + 1024 * (j2 & 1)) * 2, (128 * S - 1024 * (j2 & 1)) * 2);
        const unsigned vE = (ln >> 3) * 4096 + (((ln & 7) ^ (ln >> 4)) * 16), vO = vE ^ 64;
#pragma unroll
        for (int c = c0; c < c1; ++c)
#pragma unroll
            for (int p = 0; p < 4; ++p) dma(ldsb + w * 4096, (c & 7) * 16384 + p * 1024, (p & 1) ? vO : vE, r, 32 * w * 4096, p * 32768 + c * 128);
    };
    auto dma_p1_all = [&]() {
        const int w = opq_s(wid), ln = lane_id();
        const rsrc_t r = p1_rsrc(65536);
        const unsigned v0 = (unsigned)(ln * 16) ^ (128 * (w & 1)), lw = ldsb + w * 8192;
#pragma unroll
        for (int j = 0; j < 2; ++j)
#pragma unroll
            for (int p = 0; p < 8; ++p) dma_p1(j, p, r, v0, lw, w * 8192);
    };
    auto warm_ffn = [&]() {
        const int w = opq_s(wid), ln = lane_id();
        const unsigned o = blk * 7168u + (ln < 56 ? ln : 55) * 128u, l = ldsb + T_DUMMY + w * 256;
        auto touch = [&](rsrc_t r) {
            asm volatile("s_mov_b32 m0, %0\n\ts_nop 0\n\tbuffer_load_dword %1, %2, 0 offen lds" :: "s"(l), "v"(o), "s"(r) : "m0", "memory");
        };
        if (w == 0) touch(mk(wg, D * F * 2));
        if (w == 1) touch(mk(wu, D * F * 2));
        if (w == 2) touch(mk(wdn, D * F * 2));
    };
    dma_p1_all();

    u32x2 ev;
    aload2(ev, 0u, rw, 0u);
    u64 epoch = 0;
    bool dead = false;
    const bf16x8 ones = {0x3F80, 0x3F80, 0x3F80, 0x3F80, 0x3F80, 0x3F80, 0x3F80, 0x3F80};

        auto phase1 = [&](auto first_tag) {
            constexpr bool FIRST = decltype(first_tag)::value;
            constexpr int layer = FIRST ? 0 : 1;
            const int w = opq_s(wid), lane = lane_id(), i16 = lane & 15, q = lane >> 4;
            const int sbg = 8 * rt1 + 2 * w;
            bf16x8 xa[2][16];
            u32x2 sa[2], sb_[2];
            f32x4 acc[2][4];
#pragma unroll
            for (int s = 0; s < 2; ++s)
#pragma unroll
                for (int e = 0; e < 4; ++e) acc[s][e] = (f32x4){0.f, 0.f, 0.f, 0.f};
            unsigned wb[4];
#pragma unroll
            for (int k = 0; k < 4; ++k) wb[k] = i16 * 1024 + (((4 * k + q) ^ i16) * 16);
            LAS f32x2* tst = (LAS f32x2*)(L + LDS_M + w * 8192 + T_STAT);
            const float sgn = (et >> 3) ? -1.0f : 1.0f;
            f32x4 aS[2], aQ[2], aC = {0.f, 0.f, 0.f, 0.f};
#pragma unroll
            for (int s = 0; s < 2; ++s) { aS[s] = (f32x4){0.f, 0.f, 0.f, 0.f}; aQ[s] = aS[s]; }
            const unsigned wown = w * 16384;
            const unsigned vo = lane * 16, sbase = WS_X2 + sbg * 16384;
            if constexpr (FIRST) {
                const rsrc_t rx = mk(x, S * D * 2);
                bf16x8 xr[32];
#pragma unroll
                for (int r = 0; r < 32; ++r) aload(xr[r], vo, rx, sbg * 16384, r * 1024);
                LAS char* T0 = L + 2 * REG + w * 8192;
                sfor<2>([&](auto SB) {
                    constexpr int s = CI(SB);
                    sfor<4>([&](auto G) {
                        constexpr int g = CI(G), r0 = 16 * s + 4 * g;
                        wait4<28 - r0>(xr[r0], xr[r0 + 1], xr[r0 + 2], xr[r0 + 3]);
#pragma unroll
                        for (int r = 0; r < 4; ++r)
                            *(LAS bf16x8*)(T0 + ((4 * g + r) >> 3) * REG + ((4 * g + r) & 7) * 1024 + ((lane * 16) ^ ((4 * g + r) * 16))) = xr[r0 + r];
                    });
#pragma unroll
                    for (int ks = 0; ks < 16; ++ks)
                        xa[s][ks] = *(LAS bf16x8*)(T0 + (i16 >> 3) * REG + (i16 & 7) * 1024 + (((4 * ks + q) ^ i16) * 16));
                    wait_lds();
                });
                asm volatile("" : "+v"(ev));
                epoch = ((u64)(unsigned)__builtin_amdgcn_readfirstlane((int)ev[1]) << 32) | (unsigned)__builtin_amdgcn_readfirstlane((int)ev[0]);
                wait_lds();
                bar();
                STAMP(1);
                dma_p2n(4, 8);
                warm_ffn();
            } else {
                poll8(rw, 5, blk_p24(2 * rt1 + (w >> 1), lane & 7), epoch + 6, dead);
                STAMP(17);
#pragma unroll
                for (int s = 0; s < 2; ++s) {
                    const unsigned so = WS_ST + 2 * ST_BYTES + (sbg + s) * 1024;
                    aload2(sa[s], (2 * q * 16 + i16) * 8, rw, so);
                    aload2(sb_[s], ((2 * q + 1) * 16 + i16) * 8, rw, so);
                }
#pragma unroll
                for (int ks = 0; ks < P1_LA; ++ks)
#pragma unroll
                    for (int s = 0; s < 2; ++s) aload(xa[s][ks], vo, rw, sbase, s * 16384 + ks * 1024);
                wait2<p1_wait(0)>(xa[0][0], xa[1][0]);
                asm volatile("" : "+v"(sa[0]), "+v"(sa[1]), "+v"(sb_[0]), "+v"(sb_[1]));
            }
            bf16x8 wf[2][4], wo;
#pragma unroll
            for (int e = 0; e < 4; ++e) wf[0][e] = ldsA(L, wb[0] + e * 16384);
            sfor<16>([&](auto KS) {
                constexpr int ks = CI(KS);
                if constexpr (!FIRST && ks > 0) wait2<p1_wait(ks)>(xa[0][ks], xa[1][ks]);
                if constexpr (FIRST) wo = ldsA(L, wb[ks & 3] + wown + 256 * (ks >> 2));
                sfor<4>([&](auto E) {
                    constexpr int e = CI(E);
                    acc[0][e] = mfma(xa[0][ks], wf[ks & 1][e], acc[0][e]);
                    acc[1][e] = mfma(xa[1][ks], wf[ks & 1][e], acc[1][e]);
                    SCHED();
                    if constexpr (ks < 15) wf[(ks + 1) & 1][e] = ldsA(L, wb[(ks + 1) & 3] + e * 16384 + 256 * ((ks + 1) >> 2));
                    if constexpr (!FIRST && e >= 2 && ks + P1_LA < 16) aload(xa[e - 2][ks + P1_LA], vo, rw, sbase, (e - 2) * 16384 + (ks + P1_LA) * 1024);
                    SCHED();
                });
                if constexpr (FIRST) {
                    aC = mfma(ones, wo, aC);
#pragma unroll
                    for (int s = 0; s < 2; ++s) { aS[s] = mfma(ones, xa[s][ks], aS[s]); aQ[s] = mfma(xa[s][ks], xa[s][ks], aQ[s]); }
                    SCHED();
                }
            });
            if constexpr (FIRST) {
                ((LAS float*)(L + T_CS1))[16 * w + i16] = aC[0];
#pragma unroll
                for (int s = 0; s < 2; ++s) {
                    const int r3 = i16 & 3;
                    float dq = r3 == 0 ? aQ[s][0] : r3 == 1 ? aQ[s][1] : r3 == 2 ? aQ[s][2] : aQ[s][3];
                    dq = (q == (i16 >> 2)) ? dq : 0.f;
                    const float mean = aS[s][0] * (1.0f / 512.0f);
                    const float var = fmaxf(sumq(dq) * (1.0f / 512.0f) - mean * mean, 0.f);
                    tst[16 * s + i16] = (f32x2){mean, sgn * __builtin_amdgcn_rsqf(var + EPS)};
                }
            } else {
                float mean[2], var[2];
                combine_stats<2>(sa, sb_, mean, var);
#pragma unroll
                for (int s = 0; s < 2; ++s) tst[16 * s + i16] = (f32x2){mean[s], sgn * __builtin_amdgcn_rsqf(var[s] + EPS)};
            }
            wait_lds();
            bar();
            STAMP(2 + 16 * layer);
            dma_p2n(0, 2);
            f32x4 st0[2], st1[2];
#pragma unroll
            for (int s = 0; s < 2; ++s) {
                const f32x4 a = *(LAS f32x4*)(tst + 16 * s + 4 * q), b = *(LAS f32x4*)(tst + 16 * s + 4 * q + 2);
                st0[s] = (f32x4){a[0], a[2], b[0], b[2]}; st1[s] = (f32x4){a[1], a[3], b[1], b[3]};
            }
#pragma unroll
            for (int E = 0; E < 2; ++E) {
                const float cs0 = ((LAS float*)(L + T_CS1))[32 * E + i16], cs1 = ((LAS float*)(L + T_CS1))[32 * E + 16 + i16];
#pragma unroll
                for (int s = 0; s < 2; ++s) {
                    f32x4 v0, v1;
#pragma unroll
                    for (int r = 0; r < 4; ++r) {
                        v0[r] = (acc[s][2 * E][r] - st0[s][r] * cs0) * st1[s][r];
                        v1[r] = (acc[s][2 * E + 1][r] - st0[s][r] * cs1) * st1[s][r];
                    }
                    auto a = __builtin_amdgcn_permlane16_swap(pk(v0[0], v0[1]), pk(v1[0], v1[1]), false, false);
                    auto b = __builtin_amdgcn_permlane16_swap(pk(v0[2], v0[3]), pk(v1[2], v1[3]), false, false);
                    store16((u32x4){a[0], b[0], a[1], b[1]}, rw,
                            WS_ABT + layer * 4 * MB + (et >> 3) * 2 * MB + ((2 * (et & 7) + E) * 128 + sbg + s) * 1024 + lane * 16);
                }
            }
        };

    phase1(std::true_type{});
    for (int layer = 0; layer < 2; ++layer) {
        const u64 tag = epoch + 1 + 6 * layer;
        auto finish_strip = [&](f32x4 (&v)[4], bool last, unsigned xo, unsigned sto) {
            const int w = opq_s(wid), lane = lane_id(), i16 = lane & 15, q = lane >> 4;
            float sum = 0.f, sq = 0.f;
            u32x2 res[4];
            {
                const u32x4 r0 = *(LAS u32x4*)(L + LDS_M + w * 8192 + T_RES + lane * 16), r1 = *(LAS u32x4*)(L + LDS_M + w * 8192 + T_RES + 1024 + lane * 16);
                res[0] = (u32x2){r0[0], r0[1]}; res[1] = (u32x2){r0[2], r0[3]}; res[2] = (u32x2){r1[0], r1[1]}; res[3] = (u32x2){r1[2], r1[3]};
            }
#pragma unroll
            for (int b = 0; b < 4; ++b) {
                res[b][0] = pk(lo16(res[b][0]) + v[b][0], hi16(res[b][0]) + v[b][1]);
                res[b][1] = pk(lo16(res[b][1]) + v[b][2], hi16(res[b][1]) + v[b][3]);
                const float x0 = lo16(res[b][0]), x1 = hi16(res[b][0]), x2 = lo16(res[b][1]), x3 = hi16(res[b][1]);
                sum += (x0 + x1) + (x2 + x3);
                sq += (x0 * x0 + x1 * x1) + (x2 * x2 + x3 * x3);
            }
            const u32x4 o0 = gather8(res[0][0], res[0][1], res[1][0], res[1][1]);
            const u32x4 o1 = gather8(res[2][0], res[2][1], res[3][0], res[3][1]);
            if (last) {
                const rsrc_t rxo = mk(out, S * D * 2);
                const unsigned a = (64 * rt + 16 * w + i16) * 1024 + (64 * ct + 8 * q) * 2;
                __builtin_amdgcn_raw_buffer_store_b128(o0, rxo, a, 0, 0);
                __builtin_amdgcn_raw_buffer_store_b128(o1, rxo, a + 64, 0, 0);
            } else {
                const unsigned a = xo + ((4 * rt + w) * 16 + 2 * ct) * 1024 + lane * 16;
                store16(o0, rw, a); store16(o1, rw, a + 1024);
                *(LAS u32x4*)(L + LDS_M + w * 8192 + T_RES + lane * 16) = (u32x4){res[0][0], res[0][1], res[1][0], res[1][1]};
                *(LAS u32x4*)(L + LDS_M + w * 8192 + T_RES + 1024 + lane * 16) = (u32x4){res[2][0], res[2][1], res[3][0], res[3][1]};
                sum = sum16(sum); sq = sum16(sq);
                sum = sum32(sum); sq = sum32(sq);
                __builtin_amdgcn_raw_buffer_store_b64((u32x2){__float_as_uint(sum), __float_as_uint(sq)}, rw, sto + (((4 * rt + w) * 8 + ct) * 16 + i16) * 8, 0, 16);
            }
        };

        {
            const int w = opq_s(wid), lane = lane_id(), i16 = lane & 15, q = lane >> 4;
            const int part = j2 >> 1, half = j2 & 1, r32 = lane & 31, hh = lane >> 5;
            const rsrc_t rws = mk((part ? wsim : wsre) + ((size_t)128 * T2 * S + 1024 * half) * 2, (128 * S - 1024 * half) * 2);
            const unsigned vsE = (lane >> 3) * 4096 + (((lane & 7) ^ (lane >> 4)) * 16), vsO = vsE ^ 64;
            const int w4_ = opq_s(wid);
            const unsigned lw4 = ldsb + w4_ * 4096, wsb = 32 * w4_ * 4096;
            auto dma_p2 = [&](int c, int p) { dma(lw4, (c & 7) * 16384 + p * 1024, (p & 1) ? vsO : vsE, rws, wsb, p * 32768 + c * 128); };
            wait_vm<0>();
            bar();
            publish(rw, 6 * layer + 0, blk, tag, w);
            STAMP(3 + 16 * layer);
            dma_p2n(2, 4);
            if (layer == 0) {
                u32x2 res0[4];
                const rsrc_t rx = mk(x, S * D * 2);
                const unsigned vr = (64 * rt + 16 * w + i16) * 1024 + (64 * ct + 4 * q) * 2;
#pragma unroll
                for (int b = 0; b < 4; ++b) aload2(res0[b], vr + b * 32, rx, 0u);
                asm volatile("s_waitcnt vmcnt(0)" : "+v"(res0[0]), "+v"(res0[1]), "+v"(res0[2]), "+v"(res0[3]) :: "memory");
                *(LAS u32x4*)(L + LDS_M + w * 8192 + T_RES + lane * 16) = (u32x4){res0[0][0], res0[0][1], res0[1][0], res0[1][1]};
                *(LAS u32x4*)(L + LDS_M + w * 8192 + T_RES + 1024 + lane * 16) = (u32x4){res0[2][0], res0[2][1], res0[3][0], res0[3][1]};
            }
            poll8(rw, 6 * layer + 0, blk_p1(8 * half + (lane & 7), 8 * part + 2 * E2 + (w >> 1)), tag, dead);
            STAMP(4 + 16 * layer);
            const unsigned so0 = WS_ABT + layer * 4 * MB + part * 2 * MB + ((4 * E2 + w) * 128 + 64 * half) * 1024, vo = lane * 16;
            bf16x8 act[P2_LA + 2];
            auto load_act = [&](int v) { aload(act[v % (P2_LA + 2)], vo, rw, so0, v * 1024); };
#pragma unroll
            for (int v = 0; v < P2_LA; ++v) load_act(v);
            f32x16 acc[4];
#pragma unroll
            for (int t = 0; t < 4; ++t)
#pragma unroll
                for (int r = 0; r < 16; ++r) acc[t][r] = 0.f;
            unsigned rb[2][4];
#pragma unroll
            for (int k = 0; k < 4; ++k) { rb[0][k] = r32 * 128 + (((2 * k + hh) ^ ((r32 >> 1) & 7)) * 16); rb[1][k] = rb[0][k] + 65536; }
            auto ws_frag = [&](int n) {
                const int v = n >> 2, t = n & 3, c = v >> 2;
                return ldsA(L, rb[(c & 7) >> 2][v & 3] + (c & 3) * 16384 + t * 4096);
            };
            const int ws_ = opq_s(wid);
            const unsigned lw = ldsb + ws_ * 8192, g_sb = 64 * ws_ * 3584 + 112 * CT;
            unsigned gg0 = 0, gg1 = 0;
            bf16x8 wf[8];
#pragma unroll
            for (int n = 0; n < 6; ++n) wf[n] = ws_frag(n);
            sfor<64>([&](auto V) {
                constexpr int v = CI(V);
                asm volatile("s_waitcnt vmcnt(%1)" : "+v"(act[v % (P2_LA + 2)]) : "i"(p2_wait(v)) : "memory");
                if constexpr (v == 43) { const int l2 = lane_id(); gg0 = p3_voff(l2, 0, 0); gg1 = p3_voff(l2, 0, 1); }
                sfor<4>([&](auto TB) {
                    constexpr int t = CI(TB), n = 4 * v + t;
                    acc[t] = mfma32(act[v % (P2_LA + 2)], wf[n & 7], acc[t]);
                    SCHED();
                    if constexpr ((v & 3) == 2 && t == 2 && v < 60) bar();
                    if constexpr (n + 6 < 256) wf[(n + 6) & 7] = ws_frag(n + 6);
                    if constexpr (t == 1 && p2_dma(v)) {
                        if constexpr (v <= 39) dma_p2((v >> 2) + 6, v & 3);
                        else dma_p3(0, v - 43, ((v - 43) & 1) ? gg1 : gg0, lw, g_sb);
                    }
                    if constexpr (t == 3 && v + P2_LA < 64) load_act(v + P2_LA);
                    SCHED();
                });
            });
            STAMP(5 + 16 * layer);
            const unsigned pgrp = WS_PART + layer * 8 * MB + (T2 * 4 + E2) * 131072;
            const int lx = lane_id(), i16x = lx & 15, tup = (lx >> 4) & 1, hx = lx >> 5;
#pragma unroll
            for (int a = 0; a < 2; ++a) {
                const int owner = 2 * a + (w >> 1);
                if (owner == j2) {
#pragma unroll
                    for (int u = 0; u < 2; ++u)
#pragma unroll
                        for (int g = 0; g < 4; ++g) {
                            const f32x16& c = acc[2 * a + u];
                            *(LAS f32x4*)(L + LDS_M + (2 * u + tup) * 8192 + (2 * (w & 1) + (g >> 1)) * 1024 + (i16x + 16 * (2 * (g & 1) + hx)) * 16) =
                                (f32x4){c[4 * g], c[4 * g + 1], c[4 * g + 2], c[4 * g + 3]};
                        }
                } else {
                    const unsigned po = pgrp + owner * 32768 + j2 * 8192 + (w & 1) * 1024 + tup * 2048 + (i16x + 16 * hx) * 16;
#pragma unroll
                    for (int u = 0; u < 2; ++u)
#pragma unroll
                        for (int qh = 0; qh < 2; ++qh) {
                            const f32x16& c = acc[2 * a + u];
                            store16((u32x4){pk(c[4 * qh], c[4 * qh + 1]), pk(c[4 * qh + 2], c[4 * qh + 3]),
                                            pk(c[8 + 4 * qh], c[8 + 4 * qh + 1]), pk(c[8 + 4 * qh + 2], c[8 + 4 * qh + 3])}, rw, po + u * 4096 + qh * 512);
                        }
                }
            }
            wait_vm<0>();
            wait_lds();
            bar();
            publish(rw, 6 * layer + 1, blk, tag + 1, w);
            STAMP(13 + 16 * layer);
            const unsigned gu0 = p3_voff(lx, 1, 0), gu1 = p3_voff(lx, 1, 1), vx = lx * 16;
#pragma unroll
            for (int n = 0; n < 8; ++n) dma_p3(1, n, (n & 1) ? gg1 : gg0, lw, g_sb);
#pragma unroll
            for (int n = 0; n < 3; ++n) dma_p3(2, n, (n & 1) ? gu1 : gu0, lw, g_sb);
            poll8(rw, 6 * layer + 1, (blk & ~3) | (lx & 3), tag + 1, dead);
            STAMP(14 + 16 * layer);
            bf16x8 pv[3][2];
#pragma unroll
            for (int d = 1; d < 4; ++d)
#pragma unroll
                for (int h = 0; h < 2; ++h) aload(pv[d - 1][h], vx, rw, pgrp + j2 * 32768 + ((j2 + d) & 3) * 8192 + w * 2048, h * 1024);
            f32x4 v[4];
#pragma unroll
            for (int b = 0; b < 4; ++b) v[b] = *(LAS f32x4*)(L + LDS_M + w * 8192 + b * 1024 + vx);
            asm volatile("s_waitcnt vmcnt(0)" : "+v"(pv[0][0]), "+v"(pv[0][1]), "+v"(pv[1][0]), "+v"(pv[1][1]), "+v"(pv[2][0]), "+v"(pv[2][1]) :: "memory");
#pragma unroll
            for (int n = 3; n < 8; ++n) dma_p3(2, n, (n & 1) ? gu1 : gu0, lw, g_sb);
#pragma unroll
            for (int d = 0; d < 3; ++d)
#pragma unroll
                for (int h = 0; h < 2; ++h) {
                    const u32x4 p = __builtin_bit_cast(u32x4, pv[d][h]);
                    v[2 * h] += (f32x4){lo16(p[0]), hi16(p[0]), lo16(p[1]), hi16(p[1])};
                    v[2 * h + 1] += (f32x4){lo16(p[2]), hi16(p[2]), lo16(p[3]), hi16(p[3])};
                }
            finish_strip(v, false, WS_X1 + layer * 2 * MB, WS_ST + layer * ST_BYTES);
            wait_vm<0>();
            bar();
            publish(rw, 6 * layer + 2, blk, tag + 2, w);
            STAMP(6 + 16 * layer);
#pragma unroll
            for (int n = 0; n < 8; ++n) dma_p3(3, n, (n & 1) ? gu1 : gu0, lw, g_sb);
        }

        {
            const int w = opq_s(wid), lane = lane_id(), i16 = lane & 15, q = lane >> 4;
            poll8(rw, 6 * layer + 2, blk_p24(4 * RT + w, lane & 7), tag + 2, dead);
            if (layer == 0 && blk == 0 && w == 0) {
                u64 e2 = epoch; asm volatile("" : "+s"(e2)); e2 += 64;
                __builtin_amdgcn_raw_buffer_store_b64((u32x2){(unsigned)e2, (unsigned)(e2 >> 32)}, rw, 0, 0, 16);
                wait_vm<0>();
            }
            STAMP(7 + 16 * layer);
            const int sb0 = 16 * RT + 4 * w;
            u32x2 sa[4], sb_[4];
            bf16x8 act[P3_LA + 1][4];
            const unsigned vo = lane * 16, xo = WS_X1 + layer * 2 * MB + sb0 * 16384;
            auto load_x = [&](int g, int t) { aload(act[g % (P3_LA + 1)][t], vo, rw, xo, t * 16384 + (g & 15) * 1024); };
#pragma unroll
            for (int g = 0; g < P3_LA; ++g)
#pragma unroll
                for (int t = 0; t < 4; ++t) load_x(g, t);
            bar();
            const int qq = (lane >> 2) & 3, p4 = lane & 3;
            unsigned tb_[7];
            {
                const unsigned f = 2 * (qq >> 1) + 4 * (q & 1), rowb = (8 * q + qq) * 128 + 8 * (p4 & 1);
#pragma unroll
                for (int b = 0; b < 6; ++b) tb_[b] = (b & 1) * 65536 + rowb + (((2 * (b >> 1) + (p4 >> 1)) ^ f ^ (b & 1)) * 16);
                tb_[6] = (p4 >> 1) * 65536 + rowb + ((6 ^ f ^ (p4 >> 1)) * 16);
            }
            if (layer == 0) {
                f32x4 ac[7];
#pragma unroll
                for (int b = 0; b < 7; ++b) ac[b] = (f32x4){0.f, 0.f, 0.f, 0.f};
#pragma unroll
                for (int k = 0; k < 4; ++k)
#pragma unroll
                    for (int b = 0; b < 7; ++b) ac[b] = mfma(ldsT(L, tb_[b] + w * 16384 + k * 4096), ones, ac[b]);
#pragma unroll
                for (int b = 0; b < 7; ++b) *(LAS f32x4*)(L + T_CS3P + ((w * 7 + b) * 4 + q) * 16) = ac[b];
                wait_lds();
                bar();
                if (w == 0) {
                    const int l = lane < 28 ? lane : 27;
                    f32x4 s = *(LAS f32x4*)(L + T_CS3P + l * 16);
#pragma unroll
                    for (int k = 1; k < 4; ++k) s += *(LAS f32x4*)(L + T_CS3P + (k * 28 + l) * 16);
                    *(LAS f32x4*)(L + T_CS3 + l * 16) = s;
                }
                wait_lds();
                bar();
            }
            auto w_lo = [&](int ks, int b) { return __builtin_amdgcn_ds_read_tr16_b64_v4i16((LAS s16x4*)(L + tb_[b] + ks * 4096)); };
            auto w_hi = [&](int ks, int b) { return __builtin_amdgcn_ds_read_tr16_b64_v4i16((LAS s16x4*)(L + tb_[b] + ks * 4096 + 512)); };
            const unsigned sto = WS_ST + layer * ST_BYTES + sb0 * 1024, sv0 = (2 * q * 16 + i16) * 8;
            const unsigned ho = WS_H + layer * 7 * MB, hw = 14 * (CT >> 3);
            float mean[4], sig[4], kk_[4];
            auto glu = [&](float G, float U, float cg, float cu, int t) {
                const float e = __builtin_amdgcn_exp2f(kk_[t] * (mean[t] * cg - G));
                return (U - mean[t] * cu) * __builtin_amdgcn_rcpf(sig[t] * e + sig[t]);
            };
            u32x2 fl;
            {
                f32x4 a3[4][4];
#pragma unroll
                for (int b = 0; b < 4; ++b)
#pragma unroll
                    for (int t = 0; t < 4; ++t) a3[b][t] = (f32x4){0.f, 0.f, 0.f, 0.f};
                bf16x8 wf[4];
#pragma unroll
                for (int b = 0; b < 3; ++b) wf[b] = ldsT(L, tb_[b]);
                sfor<16>([&](auto KS) {
                    constexpr int ks = CI(KS), g = ks, sl = g % (P3_LA + 1);
                    wait4<p3_wait(g)>(act[sl][0], act[sl][1], act[sl][2], act[sl][3]);
                    if constexpr (ks == 0) STAMP(32 + 8 * layer);
                    sfor<4>([&](auto B) {
                        constexpr int b = CI(B), pb = b == 0 ? 3 : b - 1, pks = b == 0 ? ks : ks + 1;
                        s16x4 lo, hi;
                        a3[b][0] = mfma(wf[b], act[sl][0], a3[b][0]);
                        SCHED();
                        if constexpr (pks < 16) lo = w_lo(pks, pb);
                        SCHED();
                        a3[b][1] = mfma(wf[b], act[sl][1], a3[b][1]);
                        SCHED();
                        if constexpr (pks < 16) { hi = w_hi(pks, pb); wf[pb] = __builtin_shufflevector(lo, hi, 0, 1, 2, 3, 4, 5, 6, 7); }
                        SCHED();
                        a3[b][2] = mfma(wf[b], act[sl][2], a3[b][2]);
                        SCHED();
                        load_x(g + P3_LA, b);
                        SCHED();
                        a3[b][3] = mfma(wf[b], act[sl][3], a3[b][3]);
                        SCHED();
                        if constexpr (g == 8 && b == 3) {
#pragma unroll
                            for (int t = 0; t < 4; ++t) { aload2(sa[t], sv0, rw, sto + t * 1024); aload2(sb_[t], sv0 + 128, rw, sto + t * 1024); }
                            SCHED();
                        }
                    });
                });
                STAMP(33 + 8 * layer);
                asm volatile("" : "+v"(sa[0]), "+v"(sa[1]), "+v"(sa[2]), "+v"(sa[3]), "+v"(sb_[0]), "+v"(sb_[1]), "+v"(sb_[2]), "+v"(sb_[3]));
                {
                    float v[4];
                    combine_stats<4>(sa, sb_, mean, v);
#pragma unroll
                    for (int t = 0; t < 4; ++t) {
                        const float vv = v[t] + EPS, r = __builtin_amdgcn_rsqf(vv);
                        sig[t] = vv * r; kk_[t] = 1.44269504088896f * r;
                    }
                }
                unsigned H[2][4][2];
#pragma unroll
                for (int j = 0; j < 2; ++j) {
                    const f32x4 cg = *(LAS f32x4*)(L + T_CS3 + ((2 * j) * 4 + q) * 16), cu = *(LAS f32x4*)(L + T_CS3 + ((2 * j + 1) * 4 + q) * 16);
#pragma unroll
                    for (int t = 0; t < 4; ++t) {
                        float hv[4];
#pragma unroll
                        for (int r = 0; r < 4; ++r) hv[r] = glu(a3[2 * j][t][r], a3[2 * j + 1][t][r], cg[r], cu[r], t);
                        H[j][t][0] = pk(hv[0], hv[1]); H[j][t][1] = pk(hv[2], hv[3]);
                    }
                }
#pragma unroll
                for (int t = 0; t < 4; ++t)
                    store16(gather8(H[0][t][0], H[0][t][1], H[1][t][0], H[1][t][1]), rw, ho + ((sb0 + t) * 56 + hw + (CT & 7)) * 1024 + vo);
                SCHED();
            }
            STAMP(34 + 8 * layer);
            f32x4 a3[3][4];
            {
#pragma unroll
                for (int b = 0; b < 3; ++b)
#pragma unroll
                    for (int t = 0; t < 4; ++t) a3[b][t] = (f32x4){0.f, 0.f, 0.f, 0.f};
                bf16x8 wf[3];
#pragma unroll
                for (int b = 0; b < 2; ++b) wf[b] = ldsT(L, tb_[4 + b]);
                sfor<16>([&](auto KS) {
                    constexpr int ks = CI(KS), g = 16 + ks, sl = g % (P3_LA + 1);
                    wait4<p3_wait(g)>(act[sl][0], act[sl][1], act[sl][2], act[sl][3]);
                    if constexpr (g == P3_PUB) {
                        wait_vm<p3_wait_pub()>();
                        bar();
                        publish(rw, 6 * layer + 3, blk, tag + 3, w);
                        SCHED();
                    }
                    sfor<3>([&](auto B) {
                        constexpr int b = CI(B), pb = b == 0 ? 2 : b - 1, pks = b == 0 ? ks : ks + 1;
                        s16x4 lo, hi;
                        a3[b][0] = mfma(wf[b], act[sl][0], a3[b][0]);
                        SCHED();
                        if constexpr (pks < 16) lo = w_lo(pks, 4 + pb);
                        SCHED();
                        a3[b][1] = mfma(wf[b], act[sl][1], a3[b][1]);
                        SCHED();
                        if constexpr (pks < 16) { hi = w_hi(pks, 4 + pb); wf[pb] = __builtin_shufflevector(lo, hi, 0, 1, 2, 3, 4, 5, 6, 7); }
                        SCHED();
                        a3[b][2] = mfma(wf[b], act[sl][2], a3[b][2]);
                        SCHED();
                        if constexpr (g + P3_LA < 32) load_x(g + P3_LA, b);
                        SCHED();
                        a3[b][3] = mfma(wf[b], act[sl][3], a3[b][3]);
                        SCHED();
                        if constexpr (b == 2 && g + P3_LA < 32) { load_x(g + P3_LA, 3); SCHED(); }
                        if constexpr (b == 2 && g == 30) {
                            aload2(fl, (unsigned)blk_p3(rt >> 2, 8 * w + (lane & 7)) * 256u, rw, WS_FLAGS + (6 * layer + 3) * 65536u);
                            SCHED();
                        }
                    });
                });
            }
            asm volatile("s_waitcnt vmcnt(0)" : "+v"(fl) :: "memory");
            if (!__all((((u64)fl[1] << 32) | fl[0]) == tag + 3)) poll8(rw, 6 * layer + 3, blk_p3(rt >> 2, 8 * w + (lane & 7)), tag + 3, dead);
            wait_lds();
            bar();
            STAMP(8 + 16 * layer);

            const unsigned vE = (lane >> 3) * 1024 + (((lane & 7) ^ ((lane >> 3) & 2)) * 16), vO = vE ^ 64;
            const int ws_ = opq_s(wid);
            const unsigned lw = ldsb + ws_ * 8192, dsb = 448 * ws_ * 1024;
            const rsrc_t rwd = mk(wdn + 128 * ct, F * D * 2 - 128 * ct);
            auto dma_p4 = [&](int c, int p) {
                dma(lw, ((c + 1) & 3) * REG + p * 1024, (p & 1) ? vO : vE, rwd, dsb, p4_row(2 * c + (p >> 2), p & 3) * 1024);
            };
            unsigned so[4];
#pragma unroll
            for (int d = 0; d < 4; ++d) so[d] = ho + ((4 * rt + ((ws_ + d) & 3)) * 56 + 14 * ws_) * 1024;
            bf16x8 hact[8][4];
            auto load_h = [&](int u, int d) { aload(hact[u & 7][d], vo, rw, so[d], u * 1024); };
            auto pre = [&](auto N) {
                constexpr int n = CI(N);
                if constexpr (n < 8) dma_p4(0, n);
                else if constexpr (n < 16) load_h((n - 8) >> 2, n & 3);
                else if constexpr (n < 24) dma_p4(1, n - 16);
                else if constexpr (n < 36) load_h(2 + ((n - 24) >> 2), n & 3);
            };
            {
                const f32x4 cg = *(LAS f32x4*)(L + T_CS3 + (4 * 4 + q) * 16), cu = *(LAS f32x4*)(L + T_CS3 + (5 * 4 + q) * 16);
                const f32x4 mg = *(LAS f32x4*)(L + T_CS3 + (6 * 4 + (q & 1)) * 16), mu = *(LAS f32x4*)(L + T_CS3 + (6 * 4 + 2 + (q & 1)) * 16);
                const unsigned beta = 3 * (CT & 7) + q;
                const unsigned hb = q < 3 ? ho + (sb0 * 56 + hw + 8 + (beta >> 2)) * 1024 + ((beta & 3) * 16 + i16) * 16 : 0x7ff00000u;
                u32x4 o[4];
                sfor<4>([&](auto T) {
                    constexpr int t = CI(T);
                    float hv[4], hm[4];
                    sfor<4>([&](auto R) {
                        constexpr int r = CI(R), g = 4 * t + r;
                        hv[r] = glu(a3[0][t][r], a3[1][t][r], cg[r], cu[r], t);
                        auto sw = __builtin_amdgcn_permlane32_swap(__float_as_uint(a3[2][t][r]), __float_as_uint(a3[2][t][r]), false, false);
                        hm[r] = glu(__uint_as_float(sw[0]), __uint_as_float(sw[1]), mg[r], mu[r], t);
                        asm volatile("" : "+v"(hv[r]), "+v"(hm[r]));
                        sfor<(36 * (g + 1)) / 16 - (36 * g) / 16>([&](auto K) { pre(std::integral_constant<int, (36 * g) / 16 + CI(K)>{}); });
                        SCHED();
                    });
                    o[t] = gather8(pk(hv[0], hv[1]), pk(hv[2], hv[3]), pk(hm[0], hm[1]), pk(hm[2], hm[3]));
                });
#pragma unroll
                for (int t = 0; t < 4; ++t) store16(o[t], rw, hb + t * 56 * 1024);
                SCHED();
            }
            STAMP(35 + 8 * layer);
            f32x4 acc[4][4];
#pragma unroll
            for (int b = 0; b < 4; ++b)
#pragma unroll
                for (int d = 0; d < 4; ++d) acc[b][d] = (f32x4){0.f, 0.f, 0.f, 0.f};
            unsigned wb_[2][4];
#pragma unroll
            for (int b = 0; b < 4; ++b) {
                wb_[0][b] = w * 8192 + (8 * q + qq) * 128 + (((2 * b + (p4 >> 1)) ^ ((qq & 2) + 4 * (q & 1))) * 16) + 8 * (p4 & 1);
                wb_[1][b] = wb_[0][b] + 2 * REG;
            }
            auto wd_off = [&](int u, int b) { return wb_[(((u >> 1) + 1) & 3) >> 1][b] + (((u >> 1) + 1) & 1) * REG + (u & 1) * 4096; };
            bf16x8 wf[2][4];
            u32x2 flb;
            sfor<14>([&](auto U) {
                constexpr int u = CI(U);
                wait4<p4_wait(u)>(hact[u & 7][0], hact[u & 7][1], hact[u & 7][2], hact[u & 7][3]);
                if constexpr (u == 0) {
#pragma unroll
                    for (int b = 0; b < 4; ++b) wf[0][b] = ldsT(L, wd_off(0, b));
                }
                if constexpr (u == 1) {
                    bar();
                    publish(rw, 6 * layer + 4, blk, tag + 4, w);
                    STAMP(9 + 16 * layer);
                    SCHED();
                }
                if constexpr (u == 3) {
                    aload2(flb, (unsigned)blk_p3(rt >> 2, 8 * w + (lane & 7)) * 256u, rw, WS_FLAGS + (6 * layer + 4) * 65536u);
                    SCHED();
                }
                if constexpr (u == 5) {
                    asm volatile("" : "+v"(flb));
                    if (!__all((((u64)flb[1] << 32) | flb[0]) == tag + 4)) poll8(rw, 6 * layer + 4, blk_p3(rt >> 2, 8 * w + (lane & 7)), tag + 4, dead);
                    STAMP(10 + 16 * layer);
                    SCHED();
                }
                sfor<8>([&](auto J) {
                    constexpr int j = CI(J), b = j >> 1, d0 = 2 * (j & 1);
                    acc[b][d0] = mfma(wf[u & 1][b], hact[u & 7][d0], acc[b][d0]);
                    acc[b][d0 + 1] = mfma(wf[u & 1][b], hact[u & 7][d0 + 1], acc[b][d0 + 1]);
                    SCHED();
                    if constexpr (j < 4 && u < 13) wf[(u + 1) & 1][j] = ldsT(L, wd_off(u + 1, j));
                    if constexpr ((j & 1) == 0) {
                        if constexpr (p4_ndma(u) == 2) { dma_p4(2 + u, j); dma_p4(2 + u, j + 1); }
                        else if constexpr (p4_ndma(u) == 1) dma_p4((u >> 1) + 3, 4 * (u & 1) + (j >> 1));
                    } else if constexpr (p4_nact(u) == 1) load_h(u + 5, j >> 1);
                    else if constexpr (p4_nact(u) == 2) { load_h(8 + 2 * (u - 5) + (j >> 2), (j & 2)); load_h(8 + 2 * (u - 5) + (j >> 2), (j & 2) + 1); }
                    SCHED();
                });
            });
            STAMP(11 + 16 * layer);
            if (layer == 0) dma_p1_all();
#pragma unroll
            for (int d = 1; d < 4; ++d)
#pragma unroll
                for (int b = 0; b < 4; ++b)
                    *(LAS f32x4*)(L + w * 8192 + lane * 16 + (d < 3 ? 3 * REG + (d - 1) * 4096 : LDS_M) + b * 1024) = acc[b][d];
            wait_lds();
            bar();
            if (layer == 0) dma_p2n(4, 6);
            f32x4 v[4];
#pragma unroll
            for (int b = 0; b < 4; ++b) v[b] = acc[b][0];
#pragma unroll
            for (int d = 1; d < 4; ++d) {
                const int src = (w - d) & 3;
#pragma unroll
                for (int b = 0; b < 4; ++b)
                    v[b] += *(LAS f32x4*)(L + src * 8192 + lane * 16 + (d < 3 ? 3 * REG + (d - 1) * 4096 : LDS_M) + b * 1024);
            }
            if (layer == 0) {
                finish_strip(v, false, WS_X2, WS_ST + 2 * ST_BYTES);
                wait_vm<0>();
                bar();
                publish(rw, 5, blk, tag + 5, w);
                STAMP(12);
                dma_p2n(6, 8);
            } else {
                finish_strip(v, true, 0, 0);
                STAMP(28);
            }
        }
        if (layer == 1) break;
        phase1(std::false_type{});
    }
}

}

extern "C" void kernel_launch(void* const* d_in, const int* in_sizes, int n_in,
                              void* d_out, int out_size, void* d_ws, size_t ws_size,
                              hipStream_t stream) {
    (void)in_sizes; (void)n_in; (void)out_size; (void)ws_size;
    (void)hipFuncSetAttribute((const void*)fn::fnet_glu_kernel, hipFuncAttributeMaxDynamicSharedMemorySize, fn::LDS_BYTES);
    fn::fnet_glu_kernel<<<256, 256, fn::LDS_BYTES, stream>>>((const char*)d_in[0], (const char*)d_in[1], (const char*)d_in[2],
        (const char*)d_in[3], (const char*)d_in[4], (const char*)d_in[6], (const char*)d_in[7], (const char*)d_in[8],
        (char*)d_out, (char*)d_ws);
}
```

```cpp
#include <hip/hip_runtime.h>
#include <hip/hip_bf16.h>
#include <stdint.h>
#include <type_traits>
#include <utility>

#define DI __device__ __forceinline__
#define LAS __attribute__((address_space(3)))
#define GAS __attribute__((address_space(1)))
typedef short bf16x8 __attribute__((ext_vector_type(8)));
typedef short s16x4 __attribute__((ext_vector_type(4)));
typedef float f32x4 __attribute__((ext_vector_type(4)));
typedef float f32x2 __attribute__((ext_vector_type(2)));
typedef float f32x16 __attribute__((ext_vector_type(16)));
typedef unsigned u32x4 __attribute__((ext_vector_type(4)));
typedef unsigned u32x2 __attribute__((ext_vector_type(2)));
typedef __bf16 bf16x2 __attribute__((ext_vector_type(2)));
typedef unsigned long long u64;
typedef __amdgpu_buffer_rsrc_t rsrc_t;

#define WARM 2
namespace fn {

constexpr int S = 2048, D = 512, F = 1792;
constexpr unsigned MB = 1u << 20;
constexpr unsigned WS_FLAGS = 4096;
constexpr unsigned WS_ABT = 1 * MB;
constexpr unsigned WS_X1 = 9 * MB;
constexpr unsigned WS_X2 = 13 * MB;
constexpr unsigned WS_H = 15 * MB;
constexpr unsigned WS_ST = 29 * MB;
constexpr unsigned WS_STAMP = 30 * MB;
constexpr unsigned WS_PART = 32 * MB;
constexpr unsigned ST_BYTES = 128 * 1024;

constexpr unsigned REG = 32768;
constexpr unsigned LDS_M = 4 * REG;
constexpr unsigned T_RES = 4096;
constexpr unsigned T_STAT = 6144;
constexpr unsigned T_CS1 = LDS_M + 6400;
constexpr unsigned T_CS3 = LDS_M + 8192 + 6400;
constexpr unsigned T_CS3P = LDS_M + 16384 + 6400;
constexpr unsigned T_DUMMY = LDS_M + 24576 + 6400;
constexpr unsigned LDS_BYTES = 163840;
#define CODE_KB 52
constexpr unsigned CODE_PREFETCH_BYTES = CODE_KB * 1024;
constexpr unsigned SPIN_LIMIT = 200000;
constexpr float EPS = 1e-5f;

#define TLBW 0
#define NT_WS 1
#define NT_FFN 0
#define WDEPTH 2
#define WDELAY 0
DI rsrc_t mk(const void* p, unsigned bytes) { return __builtin_amdgcn_make_buffer_rsrc((void*)p, 0, bytes, 0x00020000); }
template <bool NT = false> DI void dma(unsigned lds, int LI, unsigned voff, rsrc_t r, unsigned sb, int SI) {
    unsigned t;
    if constexpr (NT)
        asm volatile("s_add_u32 m0, %1, %5\n\ts_add_u32 %0, %4, %6\n\tbuffer_load_dwordx4 %2, %3, %0 offen nt lds"
                     : "=&s"(t) : "s"(lds), "v"(voff), "s"(r), "s"(sb), "i"(LI), "i"(SI) : "m0", "scc", "memory");
    else
        asm volatile("s_add_u32 m0, %1, %5\n\ts_add_u32 %0, %4, %6\n\tbuffer_load_dwordx4 %2, %3, %0 offen lds"
                     : "=&s"(t) : "s"(lds), "v"(voff), "s"(r), "s"(sb), "i"(LI), "i"(SI) : "m0", "scc", "memory");
}
DI void aload(bf16x8& d, unsigned voff, rsrc_t r, unsigned sb, int SI) {
    asm volatile("buffer_load_dwordx4 %0, %1, %2, %3 offen offset:%4 sc1"
                 : "=v"(d) : "v"(voff), "s"(r), "s"(sb + (SI & ~4095)), "i"(SI & 4095) : "memory");
}
DI void aload2(u32x2& d, unsigned voff, rsrc_t r, unsigned sb) {
    asm volatile("buffer_load_dwordx2 %0, %1, %2, %3 offen sc1" : "=v"(d) : "v"(voff), "s"(r), "s"(sb) : "memory");
}
template <int... Is, class Fn> DI void sfor_impl(std::integer_sequence<int, Is...>, Fn&& f) { (f(std::integral_constant<int, Is>{}), ...); }
template <int N, class Fn> DI void sfor(Fn&& f) { sfor_impl(std::make_integer_sequence<int, N>{}, f); }
#define CI(x) decltype(x)::value
DI int lane_id() { int l; asm volatile("v_mbcnt_lo_u32_b32 %0, -1, 0\n\tv_mbcnt_hi_u32_b32 %0, -1, %0" : "=v"(l)); return l; }
DI int opq_s(int v) { asm volatile("" : "+s"(v)); return v; }
template <int N> DI void wait_vm() { asm volatile("s_waitcnt vmcnt(%0)" :: "i"(N) : "memory"); }
template <int N> DI void wait2(bf16x8& a, bf16x8& b) { asm volatile("s_waitcnt vmcnt(%2)" : "+v"(a), "+v"(b) : "i"(N) : "memory"); }
template <int N> DI void wait4(bf16x8& a, bf16x8& b, bf16x8& c, bf16x8& d) {
    asm volatile("s_waitcnt vmcnt(%4)" : "+v"(a), "+v"(b), "+v"(c), "+v"(d) : "i"(N) : "memory");
}
template <int N> DI void wait8(bf16x8 (&a)[8]) {
    asm volatile("s_waitcnt vmcnt(%8)" : "+v"(a[0]), "+v"(a[1]), "+v"(a[2]), "+v"(a[3]), "+v"(a[4]), "+v"(a[5]), "+v"(a[6]), "+v"(a[7]) : "i"(N) : "memory");
}
DI void wait_lds() { asm volatile("s_waitcnt lgkmcnt(0)" ::: "memory"); }
DI void bar() { asm volatile("" ::: "memory"); __builtin_amdgcn_s_barrier(); asm volatile("" ::: "memory"); }
#define SCHED() __builtin_amdgcn_sched_barrier(0)
DI f32x4 mfma(bf16x8 a, bf16x8 b, f32x4 c) { return __builtin_amdgcn_mfma_f32_16x16x32_bf16(a, b, c, 0, 0, 0); }
DI f32x16 mfma32(bf16x8 a, bf16x8 b, f32x16 c) { return __builtin_amdgcn_mfma_f32_32x32x16_bf16(a, b, c, 0, 0, 0); }
DI unsigned pk(float lo, float hi) { f32x2 v = {lo, hi}; return __builtin_bit_cast(unsigned, __builtin_convertvector(v, bf16x2)); }
DI float lo16(unsigned u) { return __uint_as_float(u << 16); }
DI float hi16(unsigned u) { return __uint_as_float(u & 0xffff0000u); }
DI float sum16(float v) { auto r = __builtin_amdgcn_permlane16_swap(__float_as_uint(v), __float_as_uint(v), false, false); return __uint_as_float(r[0]) + __uint_as_float(r[1]); }
DI float sum32(float v) { auto r = __builtin_amdgcn_permlane32_swap(__float_as_uint(v), __float_as_uint(v), false, false); return __uint_as_float(r[0]) + __uint_as_float(r[1]); }
DI float sumq(float v) { return sum32(sum16(v)); }
DI u32x4 gather8(unsigned x0, unsigned x1, unsigned y0, unsigned y1) {
    auto a = __builtin_amdgcn_permlane32_swap(x0, y0, false, false);
    auto b = __builtin_amdgcn_permlane32_swap(x1, y1, false, false);
    auto c = __builtin_amdgcn_permlane16_swap(a[0], a[1], false, false);
    auto d = __builtin_amdgcn_permlane16_swap(b[0], b[1], false, false);
    return (u32x4){c[0], d[0], c[1], d[1]};
}
DI void store16(u32x4 v, rsrc_t r, unsigned voff) { __builtin_amdgcn_raw_buffer_store_b128(v, r, voff, 0, 16); }
DI bf16x8 ldsA(LAS char* L, unsigned off) { return *(LAS bf16x8*)(L + off); }
DI bf16x8 ldsT(LAS char* L, unsigned off) {
    s16x4 lo = __builtin_amdgcn_ds_read_tr16_b64_v4i16((LAS s16x4*)(L + off));
    s16x4 hi = __builtin_amdgcn_ds_read_tr16_b64_v4i16((LAS s16x4*)(L + off + 512));
    return __builtin_shufflevector(lo, hi, 0, 1, 2, 3, 4, 5, 6, 7);
}
DI int blk_p24(int rt, int ct) { return 8 * (4 * ((rt >> 1) & 7) + (ct >> 1)) + 4 * (rt >> 4) + 2 * (rt & 1) + (ct & 1); }
DI int blk_p1(int rt1, int et) { return 8 * (8 * (rt1 & 3) + (et & 7)) + 2 * (rt1 >> 2) + (et >> 3); }
DI int blk_p3(int RT, int CT) { return 8 * (8 * (RT & 3) + (CT & 7)) + 2 * (CT >> 3) + (RT >> 2); }

DI void poll8(rsrc_t rw, int phase, int prod, u64 tag, bool& dead) {
    const unsigned voff = (unsigned)prod * 256u, sb = WS_FLAGS + (unsigned)phase * 65536u;
    if (!dead) {
        unsigned spins = 0;
        for (;;) {
            u64 v;
            asm volatile("buffer_load_dwordx2 %0, %1, %2, %3 offen sc1\n\ts_waitcnt vmcnt(0)" : "=v"(v) : "v"(voff), "s"(rw), "s"(sb) : "memory");
            if (__all(v == tag)) break;
            if (++spins > SPIN_LIMIT) { dead = true; break; }
        }
    }
    asm volatile("" ::: "memory");
}
DI void publish(rsrc_t rw, int phase, int blk, u64 tag, int w) {
    if (w == 0)
        __builtin_amdgcn_raw_buffer_store_b64((u32x2){(unsigned)tag, (unsigned)(tag >> 32)}, rw, WS_FLAGS + (unsigned)(phase * 256 + blk) * 256u, 0, 16);
}

#define STAMP(n) do {} while (0)

constexpr int P1_LA = 12;
constexpr int p1_issued(int ks) { return 2 * P1_LA + 2 * (ks < 16 - P1_LA ? ks : 16 - P1_LA); }
constexpr int p1_wait(int ks) { return p1_issued(ks) - 1 - (ks < P1_LA ? 2 * ks + 1 : p1_issued(ks - P1_LA + 1) - 1); }
constexpr int P2_LA = 22;
constexpr int p2_dma(int v) { return (v >= 8 && v <= 39) || (v >= 43 && v <= 50) ? 1 : 0; }
constexpr int p2_ops(int v) { return p2_dma(v) + (v + P2_LA < 64 ? 1 : 0); }
constexpr int p2_issued(int v) { int n = P2_LA; for (int j = 0; j < v; ++j) n += p2_ops(j); return n; }
constexpr int p2_wait(int v) {
    const int pa = v < P2_LA ? v : p2_issued(v - P2_LA + 1) - 1;
    const int c = (v >> 2) + 1;
    const int pd = (v & 3) == 2 && c >= 8 && c < 16 ? p2_issued(4 * (c - 6) + 3) : -1;
    return p2_issued(v) - 1 - (pa > pd ? pa : pd);
}
constexpr int P3_LA = 6;
constexpr int p3_ops(int g) { return (g + P3_LA < 32 ? 4 : 0) + (g == 8 ? 8 : 0) + (g == 15 ? 4 : 0) + (g == 30 ? 1 : 0); }
constexpr int p3_issued(int g) { int n = 4 * P3_LA; for (int j = 0; j < g; ++j) n += p3_ops(j); return n; }
constexpr int p3_xlast(int j) { return p3_issued(j) + 3; }
constexpr int p3_wait(int g) { return p3_issued(g) - 1 - (g < P3_LA ? 4 * g + 3 : p3_xlast(g - P3_LA)); }
constexpr int P3_PUB = 19;
constexpr int p3_wait_pub() { return p3_issued(P3_PUB) - p3_issued(16); }
constexpr int p4_ndma(int u) { return u <= 1 ? 2 : u <= 7 ? 1 : 0; }
constexpr int p4_nact(int u) { return u <= 2 ? 1 : u >= 5 && u <= 7 ? 2 : 0; }
constexpr int p4_ops(int u) { return (u == 3 ? 1 : 0) + 4 * p4_ndma(u) + 4 * p4_nact(u); }
constexpr int p4_issued(int u) { int n = 40; for (int j = 0; j < u; ++j) n += p4_ops(j); return n; }
constexpr int p4_slot_end(int u, int j) {
    int n = p4_issued(u) + (u == 3 ? 1 : 0);
    for (int k = 0; k <= j; ++k) n += (k & 1) ? p4_nact(u) : p4_ndma(u);
    return n - 1;
}
constexpr int p4_wait(int u) {
    int p = u == 0 ? 11 : u == 1 ? 15 : u == 2 ? 27 : u == 3 ? 31 : u == 4 ? 35 : u < 8 ? p4_slot_end(u - 5, 7)
          : p4_slot_end(5 + ((u - 8) >> 1), (u & 1) ? 7 : 3);
    if (u == 1 && p < 39) p = 39;
    if (u == 5 && p < p4_issued(3)) p = p4_issued(3);
    if ((u & 1) && u < 13) {
        const int c = (u + 1) >> 1;
        const int pd = c == 1 ? 23 : c <= 3 ? p4_slot_end(c - 2, 6) : p4_slot_end(2 * c - 5, 6);
        if (pd > p) p = pd;
    }
    return p4_issued(u) - 1 - p;
}
constexpr int p4_row(int u, int qq) { return u < 8 ? 56 * u + 8 * qq : 56 * ((4 * (u - 8) + qq) / 3) + 32 + 8 * ((4 * (u - 8) + qq) % 3); }

template <int N> DI void combine_stats(const u32x2 (&a)[N], const u32x2 (&b)[N], float (&mean)[N], float (&var)[N]) {
    float s[N], t[N];
#pragma unroll
    for (int i = 0; i < N; ++i) { s[i] = __uint_as_float(a[i][0]) + __uint_as_float(b[i][0]); t[i] = __uint_as_float(a[i][1]) + __uint_as_float(b[i][1]); }
#pragma unroll
    for (int i = 0; i < N; ++i) { s[i] = sum16(s[i]); t[i] = sum16(t[i]); }
#pragma unroll
    for (int i = 0; i < N; ++i) { s[i] = sum32(s[i]); t[i] = sum32(t[i]); }
#pragma unroll
    for (int i = 0; i < N; ++i) { mean[i] = s[i] * (1.0f / 512.0f); var[i] = fmaxf(t[i] * (1.0f / 512.0f) - mean[i] * mean[i], 0.f); }
}

__global__ __launch_bounds__(256, 2) void fnet_glu_kernel(const char* __restrict__ x, const char* __restrict__ wsre, const char* __restrict__ wsim,
        const char* __restrict__ wdre, const char* __restrict__ wdim_, const char* __restrict__ wg, const char* __restrict__ wu,
        const char* __restrict__ wdn, char* __restrict__ out, char* __restrict__ ws) {
    extern __shared__ __attribute__((aligned(16))) char smem[];
    LAS char* L = (LAS char*)smem;
    int wid;
    asm volatile("s_nop 1\n\tv_readfirstlane_b32 %0, %1\n\ts_nop 4" : "=s"(wid) : "v"(threadIdx.x >> 6));
    const int blk = blockIdx.x, xcd = blk & 7, idx = blk >> 3;

    const unsigned ldsb = (unsigned)(size_t)L;
    const rsrc_t rw = mk(ws, 48 * MB);
    const int T2 = 8 * (xcd >> 2) + (idx >> 2), E2 = idx & 3, j2 = xcd & 3;
    const int rt = 2 * T2 + (j2 >> 1), ct = 2 * E2 + (j2 & 1);
    const int rt1 = 4 * (xcd >> 1) + (idx >> 3), et = 8 * (xcd & 1) + (idx & 7);
    const int RT = 4 * (xcd & 1) + (idx >> 3), CT = 8 * (xcd >> 1) + (idx & 7);
    STAMP(0);
    if (CODE_PREFETCH_BYTES) {
        u64 pc;
        asm volatile("s_getpc_b64 %0" : "=s"(pc));
        const rsrc_t rc = mk((const void*)(pc & ~(u64)127), CODE_PREFETCH_BYTES);
        const unsigned seg = (unsigned)(idx * 4 + wid) % ((CODE_PREFETCH_BYTES + 8191) / 8192);
        const unsigned o = seg * 8192u + lane_id() * 128u, l = ldsb + T_DUMMY + 768;
        asm volatile("s_mov_b32 m0, %0\n\ts_nop 0\n\tbuffer_load_dword %1, %2, 0 offen lds" :: "s"(l), "v"(o), "s"(rc) : "m0", "memory");
    }

    auto p1_rsrc = [&](unsigned bytes) { return mk(((et >> 3) ? wdim_ : wdre) + (size_t)(et & 7) * 64 * 1024, bytes); };
    auto dma_p1 = [&](int j, int p, rsrc_t r, unsigned v0, unsigned lw, unsigned sb) {
        dma(lw, (32 * j + p) * 1024, v0 ^ (16 * p), r, sb, (32 * j + p) * 1024);
    };
    auto p3_voff = [&](int ln, int up, int odd) -> unsigned {
        const unsigned c = (ln & 7) ^ ((2 * ((ln >> 4) & 1) + 4 * odd) ^ up);
        return (ln >> 3) * 3584 + 16 * (c > 6 ? 6 : c);
    };
    auto dma_p3 = [&](int j, int p, unsigned v, unsigned lw, unsigned sb) {
        dma<NT_FFN>(lw, j * REG + p * 1024, v, mk((j >> 1) ? wu : wg, D * F * 2), sb, (256 * (j & 1) + 8 * p) * 3584);
    };
    auto dma_p2n = [&](int c0, int c1) {
        const int w = opq_s(wid), ln = lane_id();
        const rsrc_t r = mk(((j2 >> 1) ? wsim : wsre) + ((size_t)128 * T2 * S + 1024 * (j2 & 1)) * 2, (128 * S - 1024 * (j2 & 1)) * 2);
        const unsigned vE = (ln >> 3) * 4096 + (((ln & 7) ^ (ln >> 4)) * 16), vO = vE ^ 64;
#pragma unroll
        for (int c = c0; c < c1; ++c)
#pragma unroll
            for (int p = 0; p < 4; ++p) dma<NT_WS>(ldsb + w * 4096, (c & 7) * 16384 + p * 1024, (p & 1) ? vO : vE, r, 32 * w * 4096, p * 32768 + c * 128);
    };
    auto dma_p1_all = [&]() {
        const int w = opq_s(wid), ln = lane_id();
        const rsrc_t r = p1_rsrc(65536);
        const unsigned v0 = (unsigned)(ln * 16) ^ (128 * (w & 1)), lw = ldsb + w * 8192;
#pragma unroll
        for (int j = 0; j < 2; ++j)
#pragma unroll
            for (int p = 0; p < 8; ++p) dma_p1(j, p, r, v0, lw, w * 8192);
    };
    auto warm_ffn = [&]() {
        const int w = opq_s(wid), ln = lane_id();
        const unsigned o = blk * 7168u + (ln < 56 ? ln : 55) * 128u, l = ldsb + T_DUMMY + w * 256;
        auto touch = [&](rsrc_t r) {
            asm volatile("s_mov_b32 m0, %0\n\ts_nop 0\n\tbuffer_load_dword %1, %2, 0 offen lds" :: "s"(l), "v"(o), "s"(r) : "m0", "memory");
        };
        if (w == 0) touch(mk(wg, D * F * 2));
        if (w == 1) touch(mk(wu, D * F * 2));
        if (w == 2) touch(mk(wdn, D * F * 2));
    };
    dma_p1_all();

    u32x2 ev;
    aload2(ev, 0u, rw, 0u);
    u64 epoch = 0;
    bool dead = false;
    const bf16x8 ones = {0x3F80, 0x3F80, 0x3F80, 0x3F80, 0x3F80, 0x3F80, 0x3F80, 0x3F80};

        auto phase1 = [&](auto first_tag) {
            constexpr bool FIRST = decltype(first_tag)::value;
            constexpr int layer = FIRST ? 0 : 1;
            const int w = opq_s(wid), lane = lane_id(), i16 = lane & 15, q = lane >> 4;
            const int sbg = 8 * rt1 + 2 * w;
            bf16x8 xa[2][16];
            u32x2 sa[2], sb_[2];
            f32x4 acc[2][4];
#pragma unroll
            for (int s = 0; s < 2; ++s)
#pragma unroll
                for (int e = 0; e < 4; ++e) acc[s][e] = (f32x4){0.f, 0.f, 0.f, 0.f};
            unsigned wb[4];
#pragma unroll
            for (int k = 0; k < 4; ++k) wb[k] = i16 * 1024 + (((4 * k + q) ^ i16) * 16);
            LAS f32x2* tst = (LAS f32x2*)(L + LDS_M + w * 8192 + T_STAT);
            const float sgn = (et >> 3) ? -1.0f : 1.0f;
            f32x4 aS[2], aQ[2], aC = {0.f, 0.f, 0.f, 0.f};
#pragma unroll
            for (int s = 0; s < 2; ++s) { aS[s] = (f32x4){0.f, 0.f, 0.f, 0.f}; aQ[s] = aS[s]; }
            const unsigned wown = w * 16384;
            const unsigned vo = lane * 16, sbase = WS_X2 + sbg * 16384;
            if constexpr (FIRST) {
                const rsrc_t rx = mk(x, S * D * 2);
                bf16x8 xr[32];
#pragma unroll
                for (int r = 0; r < 32; ++r) aload(xr[r], vo, rx, sbg * 16384, r * 1024);
                LAS char* T0 = L + 2 * REG + w * 8192;
                sfor<2>([&](auto SB) {
                    constexpr int s = CI(SB);
                    sfor<4>([&](auto G) {
                        constexpr int g = CI(G), r0 = 16 * s + 4 * g;
                        wait4<28 - r0>(xr[r0], xr[r0 + 1], xr[r0 + 2], xr[r0 + 3]);
#pragma unroll
                        for (int r = 0; r < 4; ++r)
                            *(LAS bf16x8*)(T0 + ((4 * g + r) >> 3) * REG + ((4 * g + r) & 7) * 1024 + ((lane * 16) ^ ((4 * g + r) * 16))) = xr[r0 + r];
                    });
#pragma unroll
                    for (int ks = 0; ks < 16; ++ks)
                        xa[s][ks] = *(LAS bf16x8*)(T0 + (i16 >> 3) * REG + (i16 & 7) * 1024 + (((4 * ks + q) ^ i16) * 16));
                    wait_lds();
                });
                asm volatile("" : "+v"(ev));
                epoch = ((u64)(unsigned)__builtin_amdgcn_readfirstlane((int)ev[1]) << 32) | (unsigned)__builtin_amdgcn_readfirstlane((int)ev[0]);
                wait_lds();
                bar();
                STAMP(1);
                dma_p2n(4, 8);
                warm_ffn();
            } else {
                poll8(rw, 5, blk_p24(2 * rt1 + (w >> 1), lane & 7), epoch + 6, dead);
                STAMP(17);
#pragma unroll
                for (int s = 0; s < 2; ++s) {
                    const unsigned so = WS_ST + 2 * ST_BYTES + (sbg + s) * 1024;
                    aload2(sa[s], (2 * q * 16 + i16) * 8, rw, so);
                    aload2(sb_[s], ((2 * q + 1) * 16 + i16) * 8, rw, so);
                }
#pragma unroll
                for (int ks = 0; ks < P1_LA; ++ks)
#pragma unroll
                    for (int s = 0; s < 2; ++s) aload(xa[s][ks], vo, rw, sbase, s * 16384 + ks * 1024);
                wait2<p1_wait(0)>(xa[0][0], xa[1][0]);
                asm volatile("" : "+v"(sa[0]), "+v"(sa[1]), "+v"(sb_[0]), "+v"(sb_[1]));
            }
            bf16x8 wf[2][4], wo;
#pragma unroll
            for (int e = 0; e < 4; ++e) wf[0][e] = ldsA(L, wb[0] + e * 16384);
            sfor<16>([&](auto KS) {
                constexpr int ks = CI(KS);
                if constexpr (!FIRST && ks > 0) wait2<p1_wait(ks)>(xa[0][ks], xa[1][ks]);
                if constexpr (FIRST) wo = ldsA(L, wb[ks & 3] + wown + 256 * (ks >> 2));
                sfor<4>([&](auto E) {
                    constexpr int e = CI(E);
                    acc[0][e] = mfma(xa[0][ks], wf[ks & 1][e], acc[0][e]);
                    acc[1][e] = mfma(xa[1][ks], wf[ks & 1][e], acc[1][e]);
                    SCHED();
                    if constexpr (ks < 15) wf[(ks + 1) & 1][e] = ldsA(L, wb[(ks + 1) & 3] + e * 16384 + 256 * ((ks + 1) >> 2));
                    if constexpr (!FIRST && e >= 2 && ks + P1_LA < 16) aload(xa[e - 2][ks + P1_LA], vo, rw, sbase, (e - 2) * 16384 + (ks + P1_LA) * 1024);
                    SCHED();
                });
                if constexpr (FIRST) {
                    aC = mfma(ones, wo, aC);
#pragma unroll
                    for (int s = 0; s < 2; ++s) { aS[s] = mfma(ones, xa[s][ks], aS[s]); aQ[s] = mfma(xa[s][ks], xa[s][ks], aQ[s]); }
                    SCHED();
                }
            });
            if constexpr (FIRST) {
                ((LAS float*)(L + T_CS1))[16 * w + i16] = aC[0];
#pragma unroll
                for (int s = 0; s < 2; ++s) {
                    const int r3 = i16 & 3;
                    float dq = r3 == 0 ? aQ[s][0] : r3 == 1 ? aQ[s][1] : r3 == 2 ? aQ[s][2] : aQ[s][3];
                    dq = (q == (i16 >> 2)) ? dq : 0.f;
                    const float mean = aS[s][0] * (1.0f / 512.0f);
                    const float var = fmaxf(sumq(dq) * (1.0f / 512.0f) - mean * mean, 0.f);
                    tst[16 * s + i16] = (f32x2){mean, sgn * __builtin_amdgcn_rsqf(var + EPS)};
                }
            } else {
                float mean[2], var[2];
                combine_stats<2>(sa, sb_, mean, var);
#pragma unroll
                for (int s = 0; s < 2; ++s) tst[16 * s + i16] = (f32x2){mean[s], sgn * __builtin_amdgcn_rsqf(var[s] + EPS)};
            }
            wait_lds();
            bar();
            STAMP(2 + 16 * layer);
            dma_p2n(0, 2);
            f32x4 st0[2], st1[2];
#pragma unroll
            for (int s = 0; s < 2; ++s) {
                const f32x4 a = *(LAS f32x4*)(tst + 16 * s + 4 * q), b = *(LAS f32x4*)(tst + 16 * s + 4 * q + 2);
                st0[s] = (f32x4){a[0], a[2], b[0], b[2]}; st1[s] = (f32x4){a[1], a[3], b[1], b[3]};
            }
#pragma unroll
            for (int E = 0; E < 2; ++E) {
                const float cs0 = ((LAS float*)(L + T_CS1))[32 * E + i16], cs1 = ((LAS float*)(L + T_CS1))[32 * E + 16 + i16];
#pragma unroll
                for (int s = 0; s < 2; ++s) {
                    f32x4 v0, v1;
#pragma unroll
                    for (int r = 0; r < 4; ++r) {
                        v0[r] = (acc[s][2 * E][r] - st0[s][r] * cs0) * st1[s][r];
                        v1[r] = (acc[s][2 * E + 1][r] - st0[s][r] * cs1) * st1[s][r];
                    }
                    auto a = __builtin_amdgcn_permlane16_swap(pk(v0[0], v0[1]), pk(v1[0], v1[1]), false, false);
                    auto b = __builtin_amdgcn_permlane16_swap(pk(v0[2], v0[3]), pk(v1[2], v1[3]), false, false);
                    store16((u32x4){a[0], b[0], a[1], b[1]}, rw,
                            WS_ABT + layer * 4 * MB + (et >> 3) * 2 * MB + ((2 * (et & 7) + E) * 128 + sbg + s) * 1024 + lane * 16);
                }
            }
        };

    phase1(std::true_type{});
    for (int layer = 0; layer < 2; ++layer) {
        const u64 tag = epoch + 1 + 6 * layer;
        auto finish_strip = [&](f32x4 (&v)[4], bool last, unsigned xo, unsigned sto) {
            const int w = opq_s(wid), lane = lane_id(), i16 = lane & 15, q = lane >> 4;
            float sum = 0.f, sq = 0.f;
            u32x2 res[4];
            {
                const u32x4 r0 = *(LAS u32x4*)(L + LDS_M + w * 8192 + T_RES + lane * 16), r1 = *(LAS u32x4*)(L + LDS_M + w * 8192 + T_RES + 1024 + lane * 16);
                res[0] = (u32x2){r0[0], r0[1]}; res[1] = (u32x2){r0[2], r0[3]}; res[2] = (u32x2){r1[0], r1[1]}; res[3] = (u32x2){r1[2], r1[3]};
            }
#pragma unroll
            for (int b = 0; b < 4; ++b) {
                res[b][0] = pk(lo16(res[b][0]) + v[b][0], hi16(res[b][0]) + v[b][1]);
                res[b][1] = pk(lo16(res[b][1]) + v[b][2], hi16(res[b][1]) + v[b][3]);
                const float x0 = lo16(res[b][0]), x1 = hi16(res[b][0]), x2 = lo16(res[b][1]), x3 = hi16(res[b][1]);
                sum += (x0 + x1) + (x2 + x3);
                sq += (x0 * x0 + x1 * x1) + (x2 * x2 + x3 * x3);
            }
            const u32x4 o0 = gather8(res[0][0], res[0][1], res[1][0], res[1][1]);
            const u32x4 o1 = gather8(res[2][0], res[2][1], res[3][0], res[3][1]);
            if (last) {
                const rsrc_t rxo = mk(out, S * D * 2);
                const unsigned a = (64 * rt + 16 * w + i16) * 1024 + (64 * ct + 8 * q) * 2;
                __builtin_amdgcn_raw_buffer_store_b128(o0, rxo, a, 0, 0);
                __builtin_amdgcn_raw_buffer_store_b128(o1, rxo, a + 64, 0, 0);
            } else {
                const unsigned a = xo + ((4 * rt + w) * 16 + 2 * ct) * 1024 + lane * 16;
                store16(o0, rw, a); store16(o1, rw, a + 1024);
                *(LAS u32x4*)(L + LDS_M + w * 8192 + T_RES + lane * 16) = (u32x4){res[0][0], res[0][1], res[1][0], res[1][1]};
                *(LAS u32x4*)(L + LDS_M + w * 8192 + T_RES + 1024 + lane * 16) = (u32x4){res[2][0], res[2][1], res[3][0], res[3][1]};
                sum = sum16(sum); sq = sum16(sq);
                sum = sum32(sum); sq = sum32(sq);
                __builtin_amdgcn_raw_buffer_store_b64((u32x2){__float_as_uint(sum), __float_as_uint(sq)}, rw, sto + (((4 * rt + w) * 8 + ct) * 16 + i16) * 8, 0, 16);
            }
        };

        {
            const int w = opq_s(wid), lane = lane_id(), i16 = lane & 15, q = lane >> 4;
            const int part = j2 >> 1, half = j2 & 1, r32 = lane & 31, hh = lane >> 5;
            const rsrc_t rws = mk((part ? wsim : wsre) + ((size_t)128 * T2 * S + 1024 * half) * 2, (128 * S - 1024 * half) * 2);
            const unsigned vsE = (lane >> 3) * 4096 + (((lane & 7) ^ (lane >> 4)) * 16), vsO = vsE ^ 64;
            const int w4_ = opq_s(wid);
            const unsigned lw4 = ldsb + w4_ * 4096, wsb = 32 * w4_ * 4096;
            auto dma_p2 = [&](int c, int p) { dma<NT_WS>(lw4, (c & 7) * 16384 + p * 1024, (p & 1) ? vsO : vsE, rws, wsb, p * 32768 + c * 128); };
            wait_vm<0>();
            bar();
            publish(rw, 6 * layer + 0, blk, tag, w);
            STAMP(3 + 16 * layer);
            dma_p2n(2, 4);
            if (layer == 0) {
                u32x2 res0[4];
                const rsrc_t rx = mk(x, S * D * 2);
                const unsigned vr = (64 * rt + 16 * w + i16) * 1024 + (64 * ct + 4 * q) * 2;
#pragma unroll
                for (int b = 0; b < 4; ++b) aload2(res0[b], vr + b * 32, rx, 0u);
                asm volatile("s_waitcnt vmcnt(0)" : "+v"(res0[0]), "+v"(res0[1]), "+v"(res0[2]), "+v"(res0[3]) :: "memory");
                *(LAS u32x4*)(L + LDS_M + w * 8192 + T_RES + lane * 16) = (u32x4){res0[0][0], res0[0][1], res0[1][0], res0[1][1]};
                *(LAS u32x4*)(L + LDS_M + w * 8192 + T_RES + 1024 + lane * 16) = (u32x4){res0[2][0], res0[2][1], res0[3][0], res0[3][1]};
            }
            poll8(rw, 6 * layer + 0, blk_p1(8 * half + (lane & 7), 8 * part + 2 * E2 + (w >> 1)), tag, dead);
            STAMP(4 + 16 * layer);
            const unsigned so0 = WS_ABT + layer * 4 * MB + part * 2 * MB + ((4 * E2 + w) * 128 + 64 * half) * 1024, vo = lane * 16;
            bf16x8 act[P2_LA + 2];
            auto load_act = [&](int v) { aload(act[v % (P2_LA + 2)], vo, rw, so0, v * 1024); };
#pragma unroll
            for (int v = 0; v < P2_LA; ++v) load_act(v);
            f32x16 acc[4];
#pragma unroll
            for (int t = 0; t < 4; ++t)
#pragma unroll
                for (int r = 0; r < 16; ++r) acc[t][r] = 0.f;
            unsigned rb[2][4];
#pragma unroll
            for (int k = 0; k < 4; ++k) { rb[0][k] = r32 * 128 + (((2 * k + hh) ^ ((r32 >> 1) & 7)) * 16); rb[1][k] = rb[0][k] + 65536; }
            auto ws_frag = [&](int n) {
                const int v = n >> 2, t = n & 3, c = v >> 2;
                return ldsA(L, rb[(c & 7) >> 2][v & 3] + (c & 3) * 16384 + t * 4096);
            };
            const int ws_ = opq_s(wid);
            const unsigned lw = ldsb + ws_ * 8192, g_sb = 64 * ws_ * 3584 + 112 * CT;
            unsigned gg0 = 0, gg1 = 0;
            bf16x8 wf[8];
#pragma unroll
            for (int n = 0; n < 6; ++n) wf[n] = ws_frag(n);
            sfor<64>([&](auto V) {
                constexpr int v = CI(V);
                asm volatile("s_waitcnt vmcnt(%1)" : "+v"(act[v % (P2_LA + 2)]) : "i"(p2_wait(v)) : "memory");
                if constexpr (v == 43) { const int l2 = lane_id(); gg0 = p3_voff(l2, 0, 0); gg1 = p3_voff(l2, 0, 1); }
                sfor<4>([&](auto TB) {
                    constexpr int t = CI(TB), n = 4 * v + t;
                    acc[t] = mfma32(act[v % (P2_LA + 2)], wf[n & 7], acc[t]);
                    SCHED();
                    if constexpr ((v & 3) == 2 && t == 2 && v < 60) bar();
                    if constexpr (n + 6 < 256) wf[(n + 6) & 7] = ws_frag(n + 6);
                    if constexpr (t == 1 && p2_dma(v)) {
                        if constexpr (v <= 39) dma_p2((v >> 2) + 6, v & 3);
                        else dma_p3(0, v - 43, ((v - 43) & 1) ? gg1 : gg0, lw, g_sb);
                    }
                    if constexpr (t == 3 && v + P2_LA < 64) load_act(v + P2_LA);
                    SCHED();
                });
            });
            STAMP(5 + 16 * layer);
            const unsigned pgrp = WS_PART + layer * 8 * MB + (T2 * 4 + E2) * 131072;
            const int lx = lane_id(), i16x = lx & 15, tup = (lx >> 4) & 1, hx = lx >> 5;
#pragma unroll
            for (int a = 0; a < 2; ++a) {
                const int owner = 2 * a + (w >> 1);
                if (owner == j2) {
#pragma unroll
                    for (int u = 0; u < 2; ++u)
#pragma unroll
                        for (int g = 0; g < 4; ++g) {
                            const f32x16& c = acc[2 * a + u];
                            *(LAS f32x4*)(L + LDS_M + (2 * u + tup) * 8192 + (2 * (w & 1) + (g >> 1)) * 1024 + (i16x + 16 * (2 * (g & 1) + hx)) * 16) =
                                (f32x4){c[4 * g], c[4 * g + 1], c[4 * g + 2], c[4 * g + 3]};
                        }
                } else {
                    const unsigned po = pgrp + owner * 32768 + j2 * 8192 + (w & 1) * 1024 + tup * 2048 + (i16x + 16 * hx) * 16;
#pragma unroll
                    for (int u = 0; u < 2; ++u)
#pragma unroll
                        for (int qh = 0; qh < 2; ++qh) {
                            const f32x16& c = acc[2 * a + u];
                            store16((u32x4){pk(c[4 * qh], c[4 * qh + 1]), pk(c[4 * qh + 2], c[4 * qh + 3]),
                                            pk(c[8 + 4 * qh], c[8 + 4 * qh + 1]), pk(c[8 + 4 * qh + 2], c[8 + 4 * qh + 3])}, rw, po + u * 4096 + qh * 512);
                        }
                }
            }
            wait_vm<0>();
            wait_lds();
            bar();
            publish(rw, 6 * layer + 1, blk, tag + 1, w);
            STAMP(13 + 16 * layer);
            const unsigned gu0 = p3_voff(lx, 1, 0), gu1 = p3_voff(lx, 1, 1), vx = lx * 16;
#pragma unroll
            for (int n = 0; n < 8; ++n) dma_p3(1, n, (n & 1) ? gg1 : gg0, lw, g_sb);
#pragma unroll
            for (int n = 0; n < 3; ++n) dma_p3(2, n, (n & 1) ? gu1 : gu0, lw, g_sb);
            poll8(rw, 6 * layer + 1, (blk & ~3) | (lx & 3), tag + 1, dead);
            STAMP(14 + 16 * layer);
            bf16x8 pv[3][2];
#pragma unroll
            for (int d = 1; d < 4; ++d)
#pragma unroll
                for (int h = 0; h < 2; ++h) aload(pv[d - 1][h], vx, rw, pgrp + j2 * 32768 + ((j2 + d) & 3) * 8192 + w * 2048, h * 1024);
            f32x4 v[4];
#pragma unroll
            for (int b = 0; b < 4; ++b) v[b] = *(LAS f32x4*)(L + LDS_M + w * 8192 + b * 1024 + vx);
            asm volatile("s_waitcnt vmcnt(0)" : "+v"(pv[0][0]), "+v"(pv[0][1]), "+v"(pv[1][0]), "+v"(pv[1][1]), "+v"(pv[2][0]), "+v"(pv[2][1]) :: "memory");
#pragma unroll
            for (int n = 3; n < 8; ++n) dma_p3(2, n, (n & 1) ? gu1 : gu0, lw, g_sb);
#pragma unroll
            for (int d = 0; d < 3; ++d)
#pragma unroll
                for (int h = 0; h < 2; ++h) {
                    const u32x4 p = __builtin_bit_cast(u32x4, pv[d][h]);
                    v[2 * h] += (f32x4){lo16(p[0]), hi16(p[0]), lo16(p[1]), hi16(p[1])};
                    v[2 * h + 1] += (f32x4){lo16(p[2]), hi16(p[2]), lo16(p[3]), hi16(p[3])};
                }
            finish_strip(v, false, WS_X1 + layer * 2 * MB, WS_ST + layer * ST_BYTES);
            wait_vm<0>();
            bar();
            publish(rw, 6 * layer + 2, blk, tag + 2, w);
            STAMP(6 + 16 * layer);
#pragma unroll
            for (int n = 0; n < 8; ++n) dma_p3(3, n, (n & 1) ? gu1 : gu0, lw, g_sb);
        }

        {
            const int w = opq_s(wid), lane = lane_id(), i16 = lane & 15, q = lane >> 4;
            poll8(rw, 6 * layer + 2, blk_p24(4 * RT + w, lane & 7), tag + 2, dead);
            if (layer == 0 && blk == 0 && w == 0) {
                u64 e2 = epoch; asm volatile("" : "+s"(e2)); e2 += 64;
                __builtin_amdgcn_raw_buffer_store_b64((u32x2){(unsigned)e2, (unsigned)(e2 >> 32)}, rw, 0, 0, 16);
                wait_vm<0>();
            }
            STAMP(7 + 16 * layer);
            const int sb0 = 16 * RT + 4 * w;
            u32x2 sa[4], sb_[4];
            bf16x8 act[P3_LA + 1][4];
            const unsigned vo = lane * 16, xo = WS_X1 + layer * 2 * MB + sb0 * 16384;
            auto load_x = [&](int g, int t) { aload(act[g % (P3_LA + 1)][t], vo, rw, xo, t * 16384 + (g & 15) * 1024); };
#pragma unroll
            for (int g = 0; g < P3_LA; ++g)
#pragma unroll
                for (int t = 0; t < 4; ++t) load_x(g, t);
            bar();
            const int qq = (lane >> 2) & 3, p4 = lane & 3;
            unsigned tb_[7];
            {
                const unsigned f = 2 * (qq >> 1) + 4 * (q & 1), rowb = (8 * q + qq) * 128 + 8 * (p4 & 1);
#pragma unroll
                for (int b = 0; b < 6; ++b) tb_[b] = (b & 1) * 65536 + rowb + (((2 * (b >> 1) + (p4 >> 1)) ^ f ^ (b & 1)) * 16);
                tb_[6] = (p4 >> 1) * 65536 + rowb + ((6 ^ f ^ (p4 >> 1)) * 16);
            }
            if (layer == 0) {
                f32x4 ac[7];
#pragma unroll
                for (int b = 0; b < 7; ++b) ac[b] = (f32x4){0.f, 0.f, 0.f, 0.f};
#pragma unroll
                for (int k = 0; k < 4; ++k)
#pragma unroll
                    for (int b = 0; b < 7; ++b) ac[b] = mfma(ldsT(L, tb_[b] + w * 16384 + k * 4096), ones, ac[b]);
#pragma unroll
                for (int b = 0; b < 7; ++b) *(LAS f32x4*)(L + T_CS3P + ((w * 7 + b) * 4 + q) * 16) = ac[b];
                wait_lds();
                bar();
                if (w == 0) {
                    const int l = lane < 28 ? lane : 27;
                    f32x4 s = *(LAS f32x4*)(L + T_CS3P + l * 16);
#pragma unroll
                    for (int k = 1; k < 4; ++k) s += *(LAS f32x4*)(L + T_CS3P + (k * 28 + l) * 16);
                    *(LAS f32x4*)(L + T_CS3 + l * 16) = s;
                }
                wait_lds();
                bar();
            }
            auto w_lo = [&](int ks, int b) { return __builtin_amdgcn_ds_read_tr16_b64_v4i16((LAS s16x4*)(L + tb_[b] + ks * 4096)); };
            auto w_hi = [&](int ks, int b) { return __builtin_amdgcn_ds_read_tr16_b64_v4i16((LAS s16x4*)(L + tb_[b] + ks * 4096 + 512)); };
            const unsigned sto = WS_ST + layer * ST_BYTES + sb0 * 1024, sv0 = (2 * q * 16 + i16) * 8;
            const unsigned ho = WS_H + layer * 7 * MB, hw = 14 * (CT >> 3);
            float mean[4], sig[4], kk_[4];
            auto glu = [&](float G, float U, float cg, float cu, int t) {
                const float e = __builtin_amdgcn_exp2f(kk_[t] * (mean[t] * cg - G));
                return (U - mean[t] * cu) * __builtin_amdgcn_rcpf(sig[t] * e + sig[t]);
            };
            u32x2 fl;
            {
                f32x4 a3[4][4];
#pragma unroll
                for (int b = 0; b < 4; ++b)
#pragma unroll
                    for (int t = 0; t < 4; ++t) a3[b][t] = (f32x4){0.f, 0.f, 0.f, 0.f};
                bf16x8 wf[4];
#pragma unroll
                for (int b = 0; b < 3; ++b) wf[b] = ldsT(L, tb_[b]);
                sfor<16>([&](auto KS) {
                    constexpr int ks = CI(KS), g = ks, sl = g % (P3_LA + 1);
                    wait4<p3_wait(g)>(act[sl][0], act[sl][1], act[sl][2], act[sl][3]);
                    if constexpr (ks == 0) STAMP(32 + 8 * layer);
                    sfor<4>([&](auto B) {
                        constexpr int b = CI(B), pb = b == 0 ? 3 : b - 1, pks = b == 0 ? ks : ks + 1;
                        s16x4 lo, hi;
                        a3[b][0] = mfma(wf[b], act[sl][0], a3[b][0]);
                        SCHED();
                        if constexpr (pks < 16) lo = w_lo(pks, pb);
                        SCHED();
                        a3[b][1] = mfma(wf[b], act[sl][1], a3[b][1]);
                        SCHED();
                        if constexpr (pks < 16) { hi = w_hi(pks, pb); wf[pb] = __builtin_shufflevector(lo, hi, 0, 1, 2, 3, 4, 5, 6, 7); }
                        SCHED();
                        a3[b][2] = mfma(wf[b], act[sl][2], a3[b][2]);
                        SCHED();
                        load_x(g + P3_LA, b);
                        SCHED();
                        a3[b][3] = mfma(wf[b], act[sl][3], a3[b][3]);
                        SCHED();
                        if constexpr (g == 8 && b == 3) {
#pragma unroll
                            for (int t = 0; t < 4; ++t) { aload2(sa[t], sv0, rw, sto + t * 1024); aload2(sb_[t], sv0 + 128, rw, sto + t * 1024); }
                            SCHED();
                        }
                    });
                });
                STAMP(33 + 8 * layer);
                asm volatile("" : "+v"(sa[0]), "+v"(sa[1]), "+v"(sa[2]), "+v"(sa[3]), "+v"(sb_[0]), "+v"(sb_[1]), "+v"(sb_[2]), "+v"(sb_[3]));
                {
                    float v[4];
                    combine_stats<4>(sa, sb_, mean, v);
#pragma unroll
                    for (int t = 0; t < 4; ++t) {
                        const float vv = v[t] + EPS, r = __builtin_amdgcn_rsqf(vv);
                        sig[t] = vv * r; kk_[t] = 1.44269504088896f * r;
                    }
                }
                unsigned H[2][4][2];
#pragma unroll
                for (int j = 0; j < 2; ++j) {
                    const f32x4 cg = *(LAS f32x4*)(L + T_CS3 + ((2 * j) * 4 + q) * 16), cu = *(LAS f32x4*)(L + T_CS3 + ((2 * j + 1) * 4 + q) * 16);
#pragma unroll
                    for (int t = 0; t < 4; ++t) {
                        float hv[4];
#pragma unroll
                        for (int r = 0; r < 4; ++r) hv[r] = glu(a3[2 * j][t][r], a3[2 * j + 1][t][r], cg[r], cu[r], t);
                        H[j][t][0] = pk(hv[0], hv[1]); H[j][t][1] = pk(hv[2], hv[3]);
                    }
                }
#pragma unroll
                for (int t = 0; t < 4; ++t)
                    store16(gather8(H[0][t][0], H[0][t][1], H[1][t][0], H[1][t][1]), rw, ho + ((sb0 + t) * 56 + hw + (CT & 7)) * 1024 + vo);
                SCHED();
            }
            STAMP(34 + 8 * layer);
            f32x4 a3[3][4];
            {
#pragma unroll
                for (int b = 0; b < 3; ++b)
#pragma unroll
                    for (int t = 0; t < 4; ++t) a3[b][t] = (f32x4){0.f, 0.f, 0.f, 0.f};
                bf16x8 wf[3];
#pragma unroll
                for (int b = 0; b < 2; ++b) wf[b] = ldsT(L, tb_[4 + b]);
                sfor<16>([&](auto KS) {
                    constexpr int ks = CI(KS), g = 16 + ks, sl = g % (P3_LA + 1);
                    wait4<p3_wait(g)>(act[sl][0], act[sl][1], act[sl][2], act[sl][3]);
                    if constexpr (g == P3_PUB) {
                        wait_vm<p3_wait_pub()>();
                        bar();
                        publish(rw, 6 * layer + 3, blk, tag + 3, w);
                        SCHED();
                    }
                    sfor<3>([&](auto B) {
                        constexpr int b = CI(B), pb = b == 0 ? 2 : b - 1, pks = b == 0 ? ks : ks + 1;
                        s16x4 lo, hi;
                        a3[b][0] = mfma(wf[b], act[sl][0], a3[b][0]);
                        SCHED();
                        if constexpr (pks < 16) lo = w_lo(pks, 4 + pb);
                        SCHED();
                        a3[b][1] = mfma(wf[b], act[sl][1], a3[b][1]);
                        SCHED();
                        if constexpr (pks < 16) { hi = w_hi(pks, 4 + pb); wf[pb] = __builtin_shufflevector(lo, hi, 0, 1, 2, 3, 4, 5, 6, 7); }
                        SCHED();
                        a3[b][2] = mfma(wf[b], act[sl][2], a3[b][2]);
                        SCHED();
                        if constexpr (g + P3_LA < 32) load_x(g + P3_LA, b);
                        SCHED();
                        a3[b][3] = mfma(wf[b], act[sl][3], a3[b][3]);
                        SCHED();
                        if constexpr (b == 2 && g + P3_LA < 32) { load_x(g + P3_LA, 3); SCHED(); }
                        if constexpr (b == 2 && g == 30) {
                            aload2(fl, (unsigned)blk_p3(rt >> 2, 8 * w + (lane & 7)) * 256u, rw, WS_FLAGS + (6 * layer + 3) * 65536u);
                            SCHED();
                        }
                    });
                });
            }
            asm volatile("s_waitcnt vmcnt(0)" : "+v"(fl) :: "memory");
            if (!__all((((u64)fl[1] << 32) | fl[0]) == tag + 3)) poll8(rw, 6 * layer + 3, blk_p3(rt >> 2, 8 * w + (lane & 7)), tag + 3, dead);
            wait_lds();
            bar();
            STAMP(8 + 16 * layer);

            const unsigned vE = (lane >> 3) * 1024 + (((lane & 7) ^ ((lane >> 3) & 2)) * 16), vO = vE ^ 64;
            const int ws_ = opq_s(wid);
            const unsigned lw = ldsb + ws_ * 8192, dsb = 448 * ws_ * 1024;
            const rsrc_t rwd = mk(wdn + 128 * ct, F * D * 2 - 128 * ct);
            auto dma_p4 = [&](int c, int p) {
                dma<NT_FFN>(lw, ((c + 1) & 3) * REG + p * 1024, (p & 1) ? vO : vE, rwd, dsb, p4_row(2 * c + (p >> 2), p & 3) * 1024);
            };
            unsigned so[4];
#pragma unroll
            for (int d = 0; d < 4; ++d) so[d] = ho + ((4 * rt + ((ws_ + d) & 3)) * 56 + 14 * ws_) * 1024;
            bf16x8 hact[8][4];
            auto load_h = [&](int u, int d) { aload(hact[u & 7][d], vo, rw, so[d], u * 1024); };
            auto pre = [&](auto N) {
                constexpr int n = CI(N);
                if constexpr (n < 8) dma_p4(0, n);
                else if constexpr (n < 16) load_h((n - 8) >> 2, n & 3);
                else if constexpr (n < 24) dma_p4(1, n - 16);
                else if constexpr (n < 36) load_h(2 + ((n - 24) >> 2), n & 3);
            };
            {
                const f32x4 cg = *(LAS f32x4*)(L + T_CS3 + (4 * 4 + q) * 16), cu = *(LAS f32x4*)(L + T_CS3 + (5 * 4 + q) * 16);
                const f32x4 mg = *(LAS f32x4*)(L + T_CS3 + (6 * 4 + (q & 1)) * 16), mu = *(LAS f32x4*)(L + T_CS3 + (6 * 4 + 2 + (q & 1)) * 16);
                const unsigned beta = 3 * (CT & 7) + q;
                const unsigned hb = q < 3 ? ho + (sb0 * 56 + hw + 8 + (beta >> 2)) * 1024 + ((beta & 3) * 16 + i16) * 16 : 0x7ff00000u;
                u32x4 o[4];
                sfor<4>([&](auto T) {
                    constexpr int t = CI(T);
                    float hv[4], hm[4];
                    sfor<4>([&](auto R) {
                        constexpr int r = CI(R), g = 4 * t + r;
                        hv[r] = glu(a3[0][t][r], a3[1][t][r], cg[r], cu[r], t);
                        auto sw = __builtin_amdgcn_permlane32_swap(__float_as_uint(a3[2][t][r]), __float_as_uint(a3[2][t][r]), false, false);
                        hm[r] = glu(__uint_as_float(sw[0]), __uint_as_float(sw[1]), mg[r], mu[r], t);
                        asm volatile("" : "+v"(hv[r]), "+v"(hm[r]));
                        sfor<(36 * (g + 1)) / 16 - (36 * g) / 16>([&](auto K) { pre(std::integral_constant<int, (36 * g) / 16 + CI(K)>{}); });
                        SCHED();
                    });
                    o[t] = gather8(pk(hv[0], hv[1]), pk(hv[2], hv[3]), pk(hm[0], hm[1]), pk(hm[2], hm[3]));
                });
#pragma unroll
                for (int t = 0; t < 4; ++t) store16(o[t], rw, hb + t * 56 * 1024);
                SCHED();
            }
            STAMP(35 + 8 * layer);
            f32x4 acc[4][4];
#pragma unroll
            for (int b = 0; b < 4; ++b)
#pragma unroll
                for (int d = 0; d < 4; ++d) acc[b][d] = (f32x4){0.f, 0.f, 0.f, 0.f};
            unsigned wb_[2][4];
#pragma unroll
            for (int b = 0; b < 4; ++b) {
                wb_[0][b] = w * 8192 + (8 * q + qq) * 128 + (((2 * b + (p4 >> 1)) ^ ((qq & 2) + 4 * (q & 1))) * 16) + 8 * (p4 & 1);
                wb_[1][b] = wb_[0][b] + 2 * REG;
            }
            auto wd_off = [&](int u, int b) { return wb_[(((u >> 1) + 1) & 3) >> 1][b] + (((u >> 1) + 1) & 1) * REG + (u & 1) * 4096; };
            bf16x8 wf[2][4];
            u32x2 flb;
            sfor<14>([&](auto U) {
                constexpr int u = CI(U);
                wait4<p4_wait(u)>(hact[u & 7][0], hact[u & 7][1], hact[u & 7][2], hact[u & 7][3]);
                if constexpr (u == 0) {
#pragma unroll
                    for (int b = 0; b < 4; ++b) wf[0][b] = ldsT(L, wd_off(0, b));
                }
                if constexpr (u == 1) {
                    bar();
                    publish(rw, 6 * layer + 4, blk, tag + 4, w);
                    STAMP(9 + 16 * layer);
                    SCHED();
                }
                if constexpr (u == 3) {
                    aload2(flb, (unsigned)blk_p3(rt >> 2, 8 * w + (lane & 7)) * 256u, rw, WS_FLAGS + (6 * layer + 4) * 65536u);
                    SCHED();
                }
                if constexpr (u == 5) {
                    asm volatile("" : "+v"(flb));
                    if (!__all((((u64)flb[1] << 32) | flb[0]) == tag + 4)) poll8(rw, 6 * layer + 4, blk_p3(rt >> 2, 8 * w + (lane & 7)), tag + 4, dead);
                    STAMP(10 + 16 * layer);
                    SCHED();
                }
                sfor<8>([&](auto J) {
                    constexpr int j = CI(J), b = j >> 1, d0 = 2 * (j & 1);
                    acc[b][d0] = mfma(wf[u & 1][b], hact[u & 7][d0], acc[b][d0]);
                    acc[b][d0 + 1] = mfma(wf[u & 1][b], hact[u & 7][d0 + 1], acc[b][d0 + 1]);
                    SCHED();
                    if constexpr (j < 4 && u < 13) wf[(u + 1) & 1][j] = ldsT(L, wd_off(u + 1, j));
                    if constexpr ((j & 1) == 0) {
                        if constexpr (p4_ndma(u) == 2) { dma_p4(2 + u, j); dma_p4(2 + u, j + 1); }
                        else if constexpr (p4_ndma(u) == 1) dma_p4((u >> 1) + 3, 4 * (u & 1) + (j >> 1));
                    } else if constexpr (p4_nact(u) == 1) load_h(u + 5, j >> 1);
                    else if constexpr (p4_nact(u) == 2) { load_h(8 + 2 * (u - 5) + (j >> 2), (j & 2)); load_h(8 + 2 * (u - 5) + (j >> 2), (j & 2) + 1); }
                    SCHED();
                });
            });
            STAMP(11 + 16 * layer);
            if (layer == 0) dma_p1_all();
#pragma unroll
            for (int d = 1; d < 4; ++d)
#pragma unroll
                for (int b = 0; b < 4; ++b)
                    *(LAS f32x4*)(L + w * 8192 + lane * 16 + (d < 3 ? 3 * REG + (d - 1) * 4096 : LDS_M) + b * 1024) = acc[b][d];
            wait_lds();
            bar();
            if (layer == 0) dma_p2n(4, 6);
            f32x4 v[4];
#pragma unroll
            for (int b = 0; b < 4; ++b) v[b] = acc[b][0];
#pragma unroll
            for (int d = 1; d < 4; ++d) {
                const int src = (w - d) & 3;
#pragma unroll
                for (int b = 0; b < 4; ++b)
                    v[b] += *(LAS f32x4*)(L + src * 8192 + lane * 16 + (d < 3 ? 3 * REG + (d - 1) * 4096 : LDS_M) + b * 1024);
            }
            if (layer == 0) {
                finish_strip(v, false, WS_X2, WS_ST + 2 * ST_BYTES);
                wait_vm<0>();
                bar();
                publish(rw, 5, blk, tag + 5, w);
                STAMP(12);
                dma_p2n(6, 8);
            } else {
                finish_strip(v, true, 0, 0);
                STAMP(28);
            }
        }
        if (layer == 1) break;
        phase1(std::false_type{});
    }
}

}

extern "C" void kernel_launch(void* const* d_in, const int* in_sizes, int n_in,
                              void* d_out, int out_size, void* d_ws, size_t ws_size,
                              hipStream_t stream) {
    (void)in_sizes; (void)n_in; (void)out_size; (void)ws_size;
    (void)hipFuncSetAttribute((const void*)fn::fnet_glu_kernel, hipFuncAttributeMaxDynamicSharedMemorySize, fn::LDS_BYTES);
    fn::fnet_glu_kernel<<<256, 256, fn::LDS_BYTES, stream>>>((const char*)d_in[0], (const char*)d_in[1], (const char*)d_in[2],
        (const char*)d_in[3], (const char*)d_in[4], (const char*)d_in[6], (const char*)d_in[7], (const char*)d_in[8],
        (char*)d_out, (char*)d_ws);
}
```

```cpp
#include <hip/hip_runtime.h>
#include <hip/hip_bf16.h>
#include <stdint.h>
#include <type_traits>
#include <utility>

#define DI __device__ __forceinline__
#define LAS __attribute__((address_space(3)))
#define GAS __attribute__((address_space(1)))
typedef short bf16x8 __attribute__((ext_vector_type(8)));
typedef short s16x4 __attribute__((ext_vector_type(4)));
typedef float f32x4 __attribute__((ext_vector_type(4)));
typedef float f32x2 __attribute__((ext_vector_type(2)));
typedef float f32x16 __attribute__((ext_vector_type(16)));
typedef unsigned u32x4 __attribute__((ext_vector_type(4)));
typedef unsigned u32x2 __attribute__((ext_vector_type(2)));
typedef __bf16 bf16x2 __attribute__((ext_vector_type(2)));
typedef unsigned long long u64;
typedef __amdgpu_buffer_rsrc_t rsrc_t;

#define WARM 2
namespace fn {

constexpr int S = 2048, D = 512, F = 1792;
constexpr unsigned MB = 1u << 20;
constexpr unsigned WS_FLAGS = 4096;
constexpr unsigned WS_ABT = 1 * MB;
constexpr unsigned WS_X1 = 9 * MB;
constexpr unsigned WS_X2 = 13 * MB;
constexpr unsigned WS_H = 15 * MB;
constexpr unsigned WS_ST = 29 * MB;
constexpr unsigned WS_STAMP = 30 * MB;
constexpr unsigned WS_PART = 32 * MB;
constexpr unsigned ST_BYTES = 128 * 1024;

constexpr unsigned REG = 32768;
constexpr unsigned LDS_M = 4 * REG;
constexpr unsigned T_RES = 4096;
constexpr unsigned T_STAT = 6144;
constexpr unsigned T_CS1 = LDS_M + 6400;
constexpr unsigned T_CS3 = LDS_M + 8192 + 6400;
constexpr unsigned T_CS3P = LDS_M + 16384 + 6400;
constexpr unsigned T_DUMMY = LDS_M + 24576 + 6400;
constexpr unsigned LDS_BYTES = 163840;
#define CODE_KB 52
constexpr unsigned CODE_PREFETCH_BYTES = CODE_KB * 1024;
constexpr unsigned SPIN_LIMIT = 200000;
constexpr float EPS = 1e-5f;

#define TLBW 0
#define P1_STAT_KS 15
#define WDEPTH 2
#define WDELAY 0
DI rsrc_t mk(const void* p, unsigned bytes) { return __builtin_amdgcn_make_buffer_rsrc((void*)p, 0, bytes, 0x00020000); }
DI void dma(unsigned lds, int LI, unsigned voff, rsrc_t r, unsigned sb, int SI) {
    unsigned t;
    asm volatile("s_add_u32 m0, %1, %5\n\ts_add_u32 %0, %4, %6\n\tbuffer_load_dwordx4 %2, %3, %0 offen lds"
                 : "=&s"(t) : "s"(lds), "v"(voff), "s"(r), "s"(sb), "i"(LI), "i"(SI) : "m0", "scc", "memory");
}
DI void aload(bf16x8& d, unsigned voff, rsrc_t r, unsigned sb, int SI) {
    asm volatile("buffer_load_dwordx4 %0, %1, %2, %3 offen offset:%4 sc1"
                 : "=v"(d) : "v"(voff), "s"(r), "s"(sb + (SI & ~4095)), "i"(SI & 4095) : "memory");
}
DI void aload2(u32x2& d, unsigned voff, rsrc_t r, unsigned sb) {
    asm volatile("buffer_load_dwordx2 %0, %1, %2, %3 offen sc1" : "=v"(d) : "v"(voff), "s"(r), "s"(sb) : "memory");
}
template <int... Is, class Fn> DI void sfor_impl(std::integer_sequence<int, Is...>, Fn&& f) { (f(std::integral_constant<int, Is>{}), ...); }
template <int N, class Fn> DI void sfor(Fn&& f) { sfor_impl(std::make_integer_sequence<int, N>{}, f); }
#define CI(x) decltype(x)::value
DI int lane_id() { int l; asm volatile("v_mbcnt_lo_u32_b32 %0, -1, 0\n\tv_mbcnt_hi_u32_b32 %0, -1, %0" : "=v"(l)); return l; }
DI int opq_s(int v) { asm volatile("" : "+s"(v)); return v; }
template <int N> DI void wait_vm() { asm volatile("s_waitcnt vmcnt(%0)" :: "i"(N) : "memory"); }
template <int N> DI void wait2(bf16x8& a, bf16x8& b) { asm volatile("s_waitcnt vmcnt(%2)" : "+v"(a), "+v"(b) : "i"(N) : "memory"); }
template <int N> DI void wait4(bf16x8& a, bf16x8& b, bf16x8& c, bf16x8& d) {
    asm volatile("s_waitcnt vmcnt(%4)" : "+v"(a), "+v"(b), "+v"(c), "+v"(d) : "i"(N) : "memory");
}
template <int N> DI void wait8(bf16x8 (&a)[8]) {
    asm volatile("s_waitcnt vmcnt(%8)" : "+v"(a[0]), "+v"(a[1]), "+v"(a[2]), "+v"(a[3]), "+v"(a[4]), "+v"(a[5]), "+v"(a[6]), "+v"(a[7]) : "i"(N) : "memory");
}
DI void wait_lds() { asm volatile("s_waitcnt lgkmcnt(0)" ::: "memory"); }
DI void bar() { asm volatile("" ::: "memory"); __builtin_amdgcn_s_barrier(); asm volatile("" ::: "memory"); }
#define SCHED() __builtin_amdgcn_sched_barrier(0)
DI f32x4 mfma(bf16x8 a, bf16x8 b, f32x4 c) { return __builtin_amdgcn_mfma_f32_16x16x32_bf16(a, b, c, 0, 0, 0); }
DI f32x16 mfma32(bf16x8 a, bf16x8 b, f32x16 c) { return __builtin_amdgcn_mfma_f32_32x32x16_bf16(a, b, c, 0, 0, 0); }
DI unsigned pk(float lo, float hi) { f32x2 v = {lo, hi}; return __builtin_bit_cast(unsigned, __builtin_convertvector(v, bf16x2)); }
DI float lo16(unsigned u) { return __uint_as_float(u << 16); }
DI float hi16(unsigned u) { return __uint_as_float(u & 0xffff0000u); }
DI float sum16(float v) { auto r = __builtin_amdgcn_permlane16_swap(__float_as_uint(v), __float_as_uint(v), false, false); return __uint_as_float(r[0]) + __uint_as_float(r[1]); }
DI float sum32(float v) { auto r = __builtin_amdgcn_permlane32_swap(__float_as_uint(v), __float_as_uint(v), false, false); return __uint_as_float(r[0]) + __uint_as_float(r[1]); }
DI float sumq(float v) { return sum32(sum16(v)); }
DI u32x4 gather8(unsigned x0, unsigned x1, unsigned y0, unsigned y1) {
    auto a = __builtin_amdgcn_permlane32_swap(x0, y0, false, false);
    auto b = __builtin_amdgcn_permlane32_swap(x1, y1, false, false);
    auto c = __builtin_amdgcn_permlane16_swap(a[0], a[1], false, false);
    auto d = __builtin_amdgcn_permlane16_swap(b[0], b[1], false, false);
    return (u32x4){c[0], d[0], c[1], d[1]};
}
DI void store16(u32x4 v, rsrc_t r, unsigned voff) { __builtin_amdgcn_raw_buffer_store_b128(v, r, voff, 0, 16); }
DI bf16x8 ldsA(LAS char* L, unsigned off) { return *(LAS bf16x8*)(L + off); }
DI bf16x8 ldsT(LAS char* L, unsigned off) {
    s16x4 lo = __builtin_amdgcn_ds_read_tr16_b64_v4i16((LAS s16x4*)(L + off));
    s16x4 hi = __builtin_amdgcn_ds_read_tr16_b64_v4i16((LAS s16x4*)(L + off + 512));
    return __builtin_shufflevector(lo, hi, 0, 1, 2, 3, 4, 5, 6, 7);
}
DI int blk_p24(int rt, int ct) { return 8 * (4 * ((rt >> 1) & 7) + (ct >> 1)) + 4 * (rt >> 4) + 2 * (rt & 1) + (ct & 1); }
DI int blk_p1(int rt1, int et) { return 8 * (8 * (rt1 & 3) + (et & 7)) + 2 * (rt1 >> 2) + (et >> 3); }
DI int blk_p3(int RT, int CT) { return 8 * (8 * (RT & 3) + (CT & 7)) + 2 * (CT >> 3) + (RT >> 2); }

DI void poll8(rsrc_t rw, int phase, int prod, u64 tag, bool& dead) {
    const unsigned voff = (unsigned)prod * 256u, sb = WS_FLAGS + (unsigned)phase * 65536u;
    if (!dead) {
        unsigned spins = 0;
        for (;;) {
            u64 v;
            asm volatile("buffer_load_dwordx2 %0, %1, %2, %3 offen sc1\n\ts_waitcnt vmcnt(0)" : "=v"(v) : "v"(voff), "s"(rw), "s"(sb) : "memory");
            if (__all(v == tag)) break;
            if (++spins > SPIN_LIMIT) { dead = true; break; }
        }
    }
    asm volatile("" ::: "memory");
}
DI void publish(rsrc_t rw, int phase, int blk, u64 tag, int w) {
    if (w == 0)
        __builtin_amdgcn_raw_buffer_store_b64((u32x2){(unsigned)tag, (unsigned)(tag >> 32)}, rw, WS_FLAGS + (unsigned)(phase * 256 + blk) * 256u, 0, 16);
}

#define STAMP(n) do {} while (0)

constexpr int P1_LA = 12;
constexpr int p1_issued(int ks) { return 2 * P1_LA + 2 * (ks < 16 - P1_LA ? ks : 16 - P1_LA); }
constexpr int p1_wait(int ks) { return p1_issued(ks) - 1 - (ks < P1_LA ? 2 * ks + 1 : p1_issued(ks - P1_LA + 1) - 1); }
constexpr int P2_LA = 22;
constexpr int p2_dma(int v) { return (v >= 8 && v <= 39) || (v >= 43 && v <= 50) ? 1 : 0; }
constexpr int p2_ops(int v) { return p2_dma(v) + (v + P2_LA < 64 ? 1 : 0); }
constexpr int p2_issued(int v) { int n = P2_LA; for (int j = 0; j < v; ++j) n += p2_ops(j); return n; }
constexpr int p2_wait(int v) {
    const int pa = v < P2_LA ? v : p2_issued(v - P2_LA + 1) - 1;
    const int c = (v >> 2) + 1;
    const int pd = (v & 3) == 2 && c >= 8 && c < 16 ? p2_issued(4 * (c - 6) + 3) : -1;
    return p2_issued(v) - 1 - (pa > pd ? pa : pd);
}
constexpr int P3_LA = 3;
constexpr int p3_ops(int g) { return (g + P3_LA < 16 ? 4 : 0) + (g == 8 ? 8 : 0); }
constexpr int p3_issued(int g) { int n = 4 * P3_LA; for (int j = 0; j < g; ++j) n += p3_ops(j); return n; }
constexpr int p3_wait(int g) { return p3_issued(g) - 1 - (g < P3_LA ? 4 * g + 3 : p3_issued(g - P3_LA) + 3); }
constexpr int p4_ops(int u) { return (u == 1 ? 1 : 0) + (u >= 1 && u <= 6 ? 4 : 0) + (u <= 8 ? 4 : 0); }
constexpr int p4_issued(int u) { int n = 20; for (int j = 0; j < u; ++j) n += p4_ops(j); return n; }
constexpr int p4_wait(int u) {
    int p = u < 5 ? 4 * u + 3 : p4_issued(u - 4) - 1;
    if (u == 3 && p < p4_issued(1)) p = p4_issued(1);
    if ((u & 1) && u < 13) {
        const int c = (u + 1) >> 1;
        if (c >= 4 && p4_issued(2 * c - 6) + 6 > p) p = p4_issued(2 * c - 6) + 6;
    }
    return p4_issued(u) - 1 - p;
}
constexpr int p4_row(int u, int qq) { return u < 8 ? 56 * u + 8 * qq : 56 * ((4 * (u - 8) + qq) / 3) + 32 + 8 * ((4 * (u - 8) + qq) % 3); }

template <int N> DI void combine_stats(const u32x2 (&a)[N], const u32x2 (&b)[N], float (&mean)[N], float (&var)[N]) {
    float s[N], t[N], inv = 1.0f / 512.0f;
    asm volatile("" : "+s"(inv));
#pragma unroll
    for (int i = 0; i < N; ++i) { s[i] = __uint_as_float(a[i][0]) + __uint_as_float(b[i][0]); t[i] = __uint_as_float(a[i][1]) + __uint_as_float(b[i][1]); }
#pragma unroll
    for (int i = 0; i < N; ++i) { s[i] = sum16(s[i]); t[i] = sum16(t[i]); }
#pragma unroll
    for (int i = 0; i < N; ++i) { s[i] = sum32(s[i]); t[i] = sum32(t[i]); }
#pragma unroll
    for (int i = 0; i < N; ++i) { mean[i] = s[i] * inv; var[i] = fmaxf(t[i] * inv - mean[i] * mean[i], 0.f); }
}

__global__ __launch_bounds__(256, 2) void fnet_glu_kernel(const char* __restrict__ x, const char* __restrict__ wsre, const char* __restrict__ wsim,
        const char* __restrict__ wdre, const char* __restrict__ wdim_, const char* __restrict__ wg, const char* __restrict__ wu,
        const char* __restrict__ wdn, char* __restrict__ out, char* __restrict__ ws) {
    extern __shared__ __attribute__((aligned(16))) char smem[];
    LAS char* L = (LAS char*)smem;
    int wid;
    asm volatile("s_nop 1\n\tv_readfirstlane_b32 %0, %1\n\ts_nop 4" : "=s"(wid) : "v"(threadIdx.x >> 6));
    const int blk = blockIdx.x, xcd = blk & 7, idx = blk >> 3;

    const unsigned ldsb = (unsigned)(size_t)L;
    const rsrc_t rw = mk(ws, 48 * MB);
    const int T2 = 8 * (xcd >> 2) + (idx >> 2), E2 = idx & 3, j2 = xcd & 3;
    const int rt = 2 * T2 + (j2 >> 1), ct = 2 * E2 + (j2 & 1);
    const int rt1 = 4 * (xcd >> 1) + (idx >> 3), et = 8 * (xcd & 1) + (idx & 7);
    const int RT = 4 * (xcd & 1) + (idx >> 3), CT = 8 * (xcd >> 1) + (idx & 7);
    STAMP(0);
    if (CODE_PREFETCH_BYTES) {
        u64 pc;
        asm volatile("s_getpc_b64 %0" : "=s"(pc));
        const rsrc_t rc = mk((const void*)(pc & ~(u64)127), CODE_PREFETCH_BYTES);
        const unsigned seg = (unsigned)(idx * 4 + wid) % ((CODE_PREFETCH_BYTES + 8191) / 8192);
        const unsigned o = seg * 8192u + lane_id() * 128u, l = ldsb + T_DUMMY + 768;
        asm volatile("s_mov_b32 m0, %0\n\ts_nop 0\n\tbuffer_load_dword %1, %2, 0 offen lds" :: "s"(l), "v"(o), "s"(rc) : "m0", "memory");
    }

    auto p1_rsrc = [&](unsigned bytes) { return mk(((et >> 3) ? wdim_ : wdre) + (size_t)(et & 7) * 64 * 1024, bytes); };
    auto dma_p1 = [&](int j, int p, rsrc_t r, unsigned v0, unsigned lw, unsigned sb) {
        dma(lw, (32 * j + p) * 1024, v0 ^ (16 * p), r, sb, (32 * j + p) * 1024);
    };
    auto p3_voff = [&](int ln, int up, int odd) -> unsigned {
        const unsigned c = (ln & 7) ^ ((2 * ((ln >> 4) & 1) + 4 * odd) ^ up);
        return (ln >> 3) * 3584 + 16 * (c > 6 ? 6 : c);
    };
    auto dma_p3 = [&](int j, int p, unsigned v, unsigned lw, unsigned sb) {
        dma(lw, j * REG + p * 1024, v, mk((j >> 1) ? wu : wg, D * F * 2), sb, (256 * (j & 1) + 8 * p) * 3584);
    };
    auto dma_p2n = [&](int c0, int c1) {
        const int w = opq_s(wid), ln = lane_id();
        const rsrc_t r = mk(((j2 >> 1) ? wsim : wsre) + ((size_t)128 * T2 * S + 1024 * (j2 & 1)) * 2, (128 * S - 1024 * (j2 & 1)) * 2);
        const unsigned vE = (ln >> 3) * 4096 + (((ln & 7) ^ (ln >> 4)) * 16), vO = vE ^ 64;
#pragma unroll
        for (int c = c0; c < c1; ++c)
#pragma unroll
            for (int p = 0; p < 4; ++p) dma(ldsb + w * 4096, (c & 7) * 16384 + p * 1024, (p & 1) ? vO : vE, r, 32 * w * 4096, p * 32768 + c * 128);
    };
    auto dma_p1_all = [&]() {
        const int w = opq_s(wid), ln = lane_id();
        const rsrc_t r = p1_rsrc(65536);
        const unsigned v0 = (unsigned)(ln * 16) ^ (128 * (w & 1)), lw = ldsb + w * 8192;
#pragma unroll
        for (int j = 0; j < 2; ++j)
#pragma unroll
            for (int p = 0; p < 8; ++p) dma_p1(j, p, r, v0, lw, w * 8192);
    };
    auto warm_ffn = [&]() {
        const int w = opq_s(wid), ln = lane_id();
        const unsigned o = blk * 7168u + (ln < 56 ? ln : 55) * 128u, l = ldsb + T_DUMMY + w * 256;
        auto touch = [&](rsrc_t r) {
            asm volatile("s_mov_b32 m0, %0\n\ts_nop 0\n\tbuffer_load_dword %1, %2, 0 offen lds" :: "s"(l), "v"(o), "s"(r) : "m0", "memory");
        };
        if (w == 0) touch(mk(wg, D * F * 2));
        if (w == 1) touch(mk(wu, D * F * 2));
        if (w == 2) touch(mk(wdn, D * F * 2));
    };
    dma_p1_all();

    u32x2 ev;
    aload2(ev, 0u, rw, 0u);
    u64 epoch = 0;
    bool dead = false;
    const bf16x8 ones = {0x3F80, 0x3F80, 0x3F80, 0x3F80, 0x3F80, 0x3F80, 0x3F80, 0x3F80};

        auto phase1 = [&](auto first_tag) {
            constexpr bool FIRST = decltype(first_tag)::value;
            constexpr int layer = FIRST ? 0 : 1;
            const int w = opq_s(wid), lane = lane_id(), i16 = lane & 15, q = lane >> 4;
            const int sbg = 8 * rt1 + 2 * w;
            bf16x8 xa[2][16];
            u32x2 sa[2], sb_[2];
            f32x4 acc[2][4];
#pragma unroll
            for (int s = 0; s < 2; ++s)
#pragma unroll
                for (int e = 0; e < 4; ++e) acc[s][e] = (f32x4){0.f, 0.f, 0.f, 0.f};
            unsigned wb[4];
#pragma unroll
            for (int k = 0; k < 4; ++k) wb[k] = i16 * 1024 + (((4 * k + q) ^ i16) * 16);
            LAS f32x2* tst = (LAS f32x2*)(L + LDS_M + w * 8192 + T_STAT);
            const float sgn = (et >> 3) ? -1.0f : 1.0f;
            f32x4 aS[2], aQ[2], aC = {0.f, 0.f, 0.f, 0.f};
#pragma unroll
            for (int s = 0; s < 2; ++s) { aS[s] = (f32x4){0.f, 0.f, 0.f, 0.f}; aQ[s] = aS[s]; }
            const unsigned wown = w * 16384;
            const unsigned vo = lane * 16, sbase = WS_X2 + sbg * 16384;
            if constexpr (FIRST) {
                const rsrc_t rx = mk(x, S * D * 2);
                bf16x8 xr[32];
#pragma unroll
                for (int r = 0; r < 32; ++r) aload(xr[r], vo, rx, sbg * 16384, r * 1024);
                LAS char* T0 = L + 2 * REG + w * 8192;
                sfor<2>([&](auto SB) {
                    constexpr int s = CI(SB);
                    sfor<4>([&](auto G) {
                        constexpr int g = CI(G), r0 = 16 * s + 4 * g;
                        wait4<28 - r0>(xr[r0], xr[r0 + 1], xr[r0 + 2], xr[r0 + 3]);
#pragma unroll
                        for (int r = 0; r < 4; ++r)
                            *(LAS bf16x8*)(T0 + ((4 * g + r) >> 3) * REG + ((4 * g + r) & 7) * 1024 + ((lane * 16) ^ ((4 * g + r) * 16))) = xr[r0 + r];
                    });
#pragma unroll
                    for (int ks = 0; ks < 16; ++ks)
                        xa[s][ks] = *(LAS bf16x8*)(T0 + (i16 >> 3) * REG + (i16 & 7) * 1024 + (((4 * ks + q) ^ i16) * 16));
                    wait_lds();
                });
                asm volatile("" : "+v"(ev));
                epoch = ((u64)(unsigned)__builtin_amdgcn_readfirstlane((int)ev[1]) << 32) | (unsigned)__builtin_amdgcn_readfirstlane((int)ev[0]);
                wait_lds();
                bar();
                STAMP(1);
                dma_p2n(4, 8);
                warm_ffn();
            } else {
                poll8(rw, 5, blk_p24(2 * rt1 + (w >> 1), lane & 7), epoch + 6, dead);
                STAMP(17);
#pragma unroll
                for (int s = 0; s < 2; ++s) {
                    const unsigned so = WS_ST + 2 * ST_BYTES + (sbg + s) * 1024;
                    aload2(sa[s], (2 * q * 16 + i16) * 8, rw, so);
                    aload2(sb_[s], ((2 * q + 1) * 16 + i16) * 8, rw, so);
                }
#pragma unroll
                for (int ks = 0; ks < P1_LA; ++ks)
#pragma unroll
                    for (int s = 0; s < 2; ++s) aload(xa[s][ks], vo, rw, sbase, s * 16384 + ks * 1024);
                wait2<p1_wait(0)>(xa[0][0], xa[1][0]);
                asm volatile("" : "+v"(sa[0]), "+v"(sa[1]), "+v"(sb_[0]), "+v"(sb_[1]));
            }
            bf16x8 wf[2][4], wo;
#pragma unroll
            for (int e = 0; e < 4; ++e) wf[0][e] = ldsA(L, wb[0] + e * 16384);
            sfor<16>([&](auto KS) {
                constexpr int ks = CI(KS);
                if constexpr (!FIRST && ks > 0) wait2<p1_wait(ks)>(xa[0][ks], xa[1][ks]);
                if constexpr (FIRST) wo = ldsA(L, wb[ks & 3] + wown + 256 * (ks >> 2));
                sfor<4>([&](auto E) {
                    constexpr int e = CI(E);
                    acc[0][e] = mfma(xa[0][ks], wf[ks & 1][e], acc[0][e]);
                    acc[1][e] = mfma(xa[1][ks], wf[ks & 1][e], acc[1][e]);
                    SCHED();
                    if constexpr (ks < 15) wf[(ks + 1) & 1][e] = ldsA(L, wb[(ks + 1) & 3] + e * 16384 + 256 * ((ks + 1) >> 2));
                    if constexpr (!FIRST && e >= 2 && ks + P1_LA < 16) aload(xa[e - 2][ks + P1_LA], vo, rw, sbase, (e - 2) * 16384 + (ks + P1_LA) * 1024);
                    SCHED();
                });
                if constexpr (!FIRST && ks == P1_STAT_KS) {
                    float mean[2], var[2];
                    combine_stats<2>(sa, sb_, mean, var);
#pragma unroll
                    for (int s = 0; s < 2; ++s) tst[16 * s + i16] = (f32x2){mean[s], sgn * __builtin_amdgcn_rsqf(var[s] + EPS)};
                    SCHED();
                }
                if constexpr (FIRST) {
                    aC = mfma(ones, wo, aC);
#pragma unroll
                    for (int s = 0; s < 2; ++s) { aS[s] = mfma(ones, xa[s][ks], aS[s]); aQ[s] = mfma(xa[s][ks], xa[s][ks], aQ[s]); }
                    SCHED();
                }
            });
            if constexpr (FIRST) {
                ((LAS float*)(L + T_CS1))[16 * w + i16] = aC[0];
#pragma unroll
                for (int s = 0; s < 2; ++s) {
                    const int r3 = i16 & 3;
                    float dq = r3 == 0 ? aQ[s][0] : r3 == 1 ? aQ[s][1] : r3 == 2 ? aQ[s][2] : aQ[s][3];
                    dq = (q == (i16 >> 2)) ? dq : 0.f;
                    const float mean = aS[s][0] * (1.0f / 512.0f);
                    const float var = fmaxf(sumq(dq) * (1.0f / 512.0f) - mean * mean, 0.f);
                    tst[16 * s + i16] = (f32x2){mean, sgn * __builtin_amdgcn_rsqf(var + EPS)};
                }
            }
            wait_lds();
            bar();
            STAMP(2 + 16 * layer);
            dma_p2n(0, 2);
            f32x4 st0[2], st1[2];
#pragma unroll
            for (int s = 0; s < 2; ++s) {
                const f32x4 a = *(LAS f32x4*)(tst + 16 * s + 4 * q), b = *(LAS f32x4*)(tst + 16 * s + 4 * q + 2);
                st0[s] = (f32x4){a[0], a[2], b[0], b[2]}; st1[s] = (f32x4){a[1], a[3], b[1], b[3]};
            }
#pragma unroll
            for (int E = 0; E < 2; ++E) {
                const float cs0 = ((LAS float*)(L + T_CS1))[32 * E + i16], cs1 = ((LAS float*)(L + T_CS1))[32 * E + 16 + i16];
#pragma unroll
                for (int s = 0; s < 2; ++s) {
                    f32x4 v0, v1;
#pragma unroll
                    for (int r = 0; r < 4; ++r) {
                        v0[r] = (acc[s][2 * E][r] - st0[s][r] * cs0) * st1[s][r];
                        v1[r] = (acc[s][2 * E + 1][r] - st0[s][r] * cs1) * st1[s][r];
                    }
                    auto a = __builtin_amdgcn_permlane16_swap(pk(v0[0], v0[1]), pk(v1[0], v1[1]), false, false);
                    auto b = __builtin_amdgcn_permlane16_swap(pk(v0[2], v0[3]), pk(v1[2], v1[3]), false, false);
                    store16((u32x4){a[0], b[0], a[1], b[1]}, rw,
                            WS_ABT + layer * 4 * MB + (et >> 3) * 2 * MB + ((2 * (et & 7) + E) * 128 + sbg + s) * 1024 + lane * 16);
                }
            }
        };

    phase1(std::true_type{});
    for (int layer = 0; layer < 2; ++layer) {
        const u64 tag = epoch + 1 + 6 * layer;
        auto finish_strip = [&](f32x4 (&v)[4], bool last, unsigned xo, unsigned sto) {
            const int w = opq_s(wid), lane = lane_id(), i16 = lane & 15, q = lane >> 4;
            float sum = 0.f, sq = 0.f;
            u32x2 res[4];
            {
                const u32x4 r0 = *(LAS u32x4*)(L + LDS_M + w * 8192 + T_RES + lane * 16), r1 = *(LAS u32x4*)(L + LDS_M + w * 8192 + T_RES + 1024 + lane * 16);
                res[0] = (u32x2){r0[0], r0[1]}; res[1] = (u32x2){r0[2], r0[3]}; res[2] = (u32x2){r1[0], r1[1]}; res[3] = (u32x2){r1[2], r1[3]};
            }
#pragma unroll
            for (int b = 0; b < 4; ++b) {
                res[b][0] = pk(lo16(res[b][0]) + v[b][0], hi16(res[b][0]) + v[b][1]);
                res[b][1] = pk(lo16(res[b][1]) + v[b][2], hi16(res[b][1]) + v[b][3]);
                const float x0 = lo16(res[b][0]), x1 = hi16(res[b][0]), x2 = lo16(res[b][1]), x3 = hi16(res[b][1]);
                sum += (x0 + x1) + (x2 + x3);
                sq += (x0 * x0 + x1 * x1) + (x2 * x2 + x3 * x3);
            }
            const u32x4 o0 = gather8(res[0][0], res[0][1], res[1][0], res[1][1]);
            const u32x4 o1 = gather8(res[2][0], res[2][1], res[3][0], res[3][1]);
            if (last) {
                const rsrc_t rxo = mk(out, S * D * 2);
                const unsigned a = (64 * rt + 16 * w + i16) * 1024 + (64 * ct + 8 * q) * 2;
                __builtin_amdgcn_raw_buffer_store_b128(o0, rxo, a, 0, 0);
                __builtin_amdgcn_raw_buffer_store_b128(o1, rxo, a + 64, 0, 0);
            } else {
                const unsigned a = xo + ((4 * rt + w) * 16 + 2 * ct) * 1024 + lane * 16;
                store16(o0, rw, a); store16(o1, rw, a + 1024);
                *(LAS u32x4*)(L + LDS_M + w * 8192 + T_RES + lane * 16) = (u32x4){res[0][0], res[0][1], res[1][0], res[1][1]};
                *(LAS u32x4*)(L + LDS_M + w * 8192 + T_RES + 1024 + lane * 16) = (u32x4){res[2][0], res[2][1], res[3][0], res[3][1]};
                sum = sum16(sum); sq = sum16(sq);
                sum = sum32(sum); sq = sum32(sq);
                __builtin_amdgcn_raw_buffer_store_b64((u32x2){__float_as_uint(sum), __float_as_uint(sq)}, rw, sto + (((4 * rt + w) * 8 + ct) * 16 + i16) * 8, 0, 16);
            }
        };

        {
            const int w = opq_s(wid), lane = lane_id(), i16 = lane & 15, q = lane >> 4;
            const int part = j2 >> 1, half = j2 & 1, r32 = lane & 31, hh = lane >> 5;
            const rsrc_t rws = mk((part ? wsim : wsre) + ((size_t)128 * T2 * S + 1024 * half) * 2, (128 * S - 1024 * half) * 2);
            const unsigned vsE = (lane >> 3) * 4096 + (((lane & 7) ^ (lane >> 4)) * 16), vsO = vsE ^ 64;
            const int w4_ = opq_s(wid);
            const unsigned lw4 = ldsb + w4_ * 4096, wsb = 32 * w4_ * 4096;
            auto dma_p2 = [&](int c, int p) { dma(lw4, (c & 7) * 16384 + p * 1024, (p & 1) ? vsO : vsE, rws, wsb, p * 32768 + c * 128); };
            wait_vm<0>();
            bar();
            publish(rw, 6 * layer + 0, blk, tag, w);
            STAMP(3 + 16 * layer);
            dma_p2n(2, 4);
            if (layer == 0) {
                u32x2 res0[4];
                const rsrc_t rx = mk(x, S * D * 2);
                const unsigned vr = (64 * rt + 16 * w + i16) * 1024 + (64 * ct + 4 * q) * 2;
#pragma unroll
                for (int b = 0; b < 4; ++b) aload2(res0[b], vr + b * 32, rx, 0u);
                asm volatile("s_waitcnt vmcnt(0)" : "+v"(res0[0]), "+v"(res0[1]), "+v"(res0[2]), "+v"(res0[3]) :: "memory");
                *(LAS u32x4*)(L + LDS_M + w * 8192 + T_RES + lane * 16) = (u32x4){res0[0][0], res0[0][1], res0[1][0], res0[1][1]};
                *(LAS u32x4*)(L + LDS_M + w * 8192 + T_RES + 1024 + lane * 16) = (u32x4){res0[2][0], res0[2][1], res0[3][0], res0[3][1]};
            }
            poll8(rw, 6 * layer + 0, blk_p1(8 * half + (lane & 7), 8 * part + 2 * E2 + (w >> 1)), tag, dead);
            STAMP(4 + 16 * layer);
            const unsigned so0 = WS_ABT + layer * 4 * MB + part * 2 * MB + ((4 * E2 + w) * 128 + 64 * half) * 1024, vo = lane * 16;
            bf16x8 act[P2_LA + 2];
            auto load_act = [&](int v) { aload(act[v % (P2_LA + 2)], vo, rw, so0, v * 1024); };
#pragma unroll
            for (int v = 0; v < P2_LA; ++v) load_act(v);
            f32x16 acc[4];
#pragma unroll
            for (int t = 0; t < 4; ++t)
#pragma unroll
                for (int r = 0; r < 16; ++r) acc[t][r] = 0.f;
            unsigned rb[2][4];
#pragma unroll
            for (int k = 0; k < 4; ++k) { rb[0][k] = r32 * 128 + (((2 * k + hh) ^ ((r32 >> 1) & 7)) * 16); rb[1][k] = rb[0][k] + 65536; }
            auto ws_frag = [&](int n) {
                const int v = n >> 2, t = n & 3, c = v >> 2;
                return ldsA(L, rb[(c & 7) >> 2][v & 3] + (c & 3) * 16384 + t * 4096);
            };
            const int ws_ = opq_s(wid);
            const unsigned lw = ldsb + ws_ * 8192, g_sb = 64 * ws_ * 3584 + 112 * CT;
            unsigned gg0 = 0, gg1 = 0;
            bf16x8 wf[8];
#pragma unroll
            for (int n = 0; n < 6; ++n) wf[n] = ws_frag(n);
            sfor<64>([&](auto V) {
                constexpr int v = CI(V);
                asm volatile("s_waitcnt vmcnt(%1)" : "+v"(act[v % (P2_LA + 2)]) : "i"(p2_wait(v)) : "memory");
                if constexpr (v == 43) { const int l2 = lane_id(); gg0 = p3_voff(l2, 0, 0); gg1 = p3_voff(l2, 0, 1); }
                sfor<4>([&](auto TB) {
                    constexpr int t = CI(TB), n = 4 * v + t;
                    acc[t] = mfma32(act[v % (P2_LA + 2)], wf[n & 7], acc[t]);
                    SCHED();
                    if constexpr ((v & 3) == 2 && t == 2 && v < 60) bar();
                    if constexpr (n + 6 < 256) wf[(n + 6) & 7] = ws_frag(n + 6);
                    if constexpr (t == 1 && p2_dma(v)) {
                        if constexpr (v <= 39) dma_p2((v >> 2) + 6, v & 3);
                        else dma_p3(0, v - 43, ((v - 43) & 1) ? gg1 : gg0, lw, g_sb);
                    }
                    if constexpr (t == 3 && v + P2_LA < 64) load_act(v + P2_LA);
                    SCHED();
                });
            });
            STAMP(5 + 16 * layer);
            const unsigned pgrp = WS_PART + layer * 8 * MB + (T2 * 4 + E2) * 131072;
            const int lx = lane_id(), i16x = lx & 15, tup = (lx >> 4) & 1, hx = lx >> 5;
#pragma unroll
            for (int a = 0; a < 2; ++a) {
                const int owner = 2 * a + (w >> 1);
                if (owner == j2) {
#pragma unroll
                    for (int u = 0; u < 2; ++u)
#pragma unroll
                        for (int g = 0; g < 4; ++g) {
                            const f32x16& c = acc[2 * a + u];
                            *(LAS f32x4*)(L + LDS_M + (2 * u + tup) * 8192 + (2 * (w & 1) + (g >> 1)) * 1024 + (i16x + 16 * (2 * (g & 1) + hx)) * 16) =
                                (f32x4){c[4 * g], c[4 * g + 1], c[4 * g + 2], c[4 * g + 3]};
                        }
                } else {
                    const unsigned po = pgrp + owner * 32768 + j2 * 8192 + (w & 1) * 1024 + tup * 2048 + (i16x + 16 * hx) * 16;
#pragma unroll
                    for (int u = 0; u < 2; ++u)
#pragma unroll
                        for (int qh = 0; qh < 2; ++qh) {
                            const f32x16& c = acc[2 * a + u];
                            store16((u32x4){pk(c[4 * qh], c[4 * qh + 1]), pk(c[4 * qh + 2], c[4 * qh + 3]),
                                            pk(c[8 + 4 * qh], c[8 + 4 * qh + 1]), pk(c[8 + 4 * qh + 2], c[8 + 4 * qh + 3])}, rw, po + u * 4096 + qh * 512);
                        }
                }
            }
            wait_vm<0>();
            wait_lds();
            bar();
            publish(rw, 6 * layer + 1, blk, tag + 1, w);
            STAMP(13 + 16 * layer);
            const unsigned gu0 = p3_voff(lx, 1, 0), gu1 = p3_voff(lx, 1, 1), vx = lx * 16;
#pragma unroll
            for (int n = 0; n < 8; ++n) dma_p3(1, n, (n & 1) ? gg1 : gg0, lw, g_sb);
#pragma unroll
            for (int n = 0; n < 3; ++n) dma_p3(2, n, (n & 1) ? gu1 : gu0, lw, g_sb);
            poll8(rw, 6 * layer + 1, (blk & ~3) | (lx & 3), tag + 1, dead);
            STAMP(14 + 16 * layer);
            bf16x8 pv[3][2];
#pragma unroll
            for (int d = 1; d < 4; ++d)
#pragma unroll
                for (int h = 0; h < 2; ++h) aload(pv[d - 1][h], vx, rw, pgrp + j2 * 32768 + ((j2 + d) & 3) * 8192 + w * 2048, h * 1024);
            f32x4 v[4];
#pragma unroll
            for (int b = 0; b < 4; ++b) v[b] = *(LAS f32x4*)(L + LDS_M + w * 8192 + b * 1024 + vx);
            asm volatile("s_waitcnt vmcnt(0)" : "+v"(pv[0][0]), "+v"(pv[0][1]), "+v"(pv[1][0]), "+v"(pv[1][1]), "+v"(pv[2][0]), "+v"(pv[2][1]) :: "memory");
#pragma unroll
            for (int n = 3; n < 8; ++n) dma_p3(2, n, (n & 1) ? gu1 : gu0, lw, g_sb);
#pragma unroll
            for (int d = 0; d < 3; ++d)
#pragma unroll
                for (int h = 0; h < 2; ++h) {
                    const u32x4 p = __builtin_bit_cast(u32x4, pv[d][h]);
                    v[2 * h] += (f32x4){lo16(p[0]), hi16(p[0]), lo16(p[1]), hi16(p[1])};
                    v[2 * h + 1] += (f32x4){lo16(p[2]), hi16(p[2]), lo16(p[3]), hi16(p[3])};
                }
            finish_strip(v, false, WS_X1 + layer * 2 * MB, WS_ST + layer * ST_BYTES);
            wait_vm<0>();
            bar();
            publish(rw, 6 * layer + 2, blk, tag + 2, w);
            STAMP(6 + 16 * layer);
#pragma unroll
            for (int n = 0; n < 8; ++n) dma_p3(3, n, (n & 1) ? gu1 : gu0, lw, g_sb);
        }

        {
            const int w = opq_s(wid), lane = lane_id(), i16 = lane & 15, q = lane >> 4;
            poll8(rw, 6 * layer + 2, blk_p24(4 * RT + w, lane & 7), tag + 2, dead);
            if (layer == 0 && blk == 0 && w == 0) {
                u64 e2 = epoch; asm volatile("" : "+s"(e2)); e2 += 64;
                __builtin_amdgcn_raw_buffer_store_b64((u32x2){(unsigned)e2, (unsigned)(e2 >> 32)}, rw, 0, 0, 16);
                wait_vm<0>();
            }
            STAMP(7 + 16 * layer);
            const int sb0 = 16 * RT + 4 * w;
            u32x2 sa[4], sb_[4];
            bf16x8 act[P3_LA + 1][4];
            const unsigned vo = lane * 16, xo = WS_X1 + layer * 2 * MB + sb0 * 16384;
            auto load_x = [&](int g, int t) { aload(act[g % (P3_LA + 1)][t], vo, rw, xo, t * 16384 + g * 1024); };
#pragma unroll
            for (int g = 0; g < P3_LA; ++g)
#pragma unroll
                for (int t = 0; t < 4; ++t) load_x(g, t);
            bar();
            const int qq = (lane >> 2) & 3, p4 = lane & 3;
            unsigned tb_[7];
            {
                const unsigned f = 2 * (qq >> 1) + 4 * (q & 1), rowb = (8 * q + qq) * 128 + 8 * (p4 & 1);
#pragma unroll
                for (int b = 0; b < 6; ++b) tb_[b] = (b & 1) * 65536 + rowb + (((2 * (b >> 1) + (p4 >> 1)) ^ f ^ (b & 1)) * 16);
                tb_[6] = (p4 >> 1) * 65536 + rowb + ((6 ^ f ^ (p4 >> 1)) * 16);
            }
            if (layer == 0) {
                f32x4 ac[7];
#pragma unroll
                for (int b = 0; b < 7; ++b) ac[b] = (f32x4){0.f, 0.f, 0.f, 0.f};
#pragma unroll
                for (int k = 0; k < 4; ++k)
#pragma unroll
                    for (int b = 0; b < 7; ++b) ac[b] = mfma(ldsT(L, tb_[b] + w * 16384 + k * 4096), ones, ac[b]);
#pragma unroll
                for (int b = 0; b < 7; ++b) *(LAS f32x4*)(L + T_CS3P + ((w * 7 + b) * 4 + q) * 16) = ac[b];
                wait_lds();
                bar();
                if (w == 0) {
                    const int l = lane < 28 ? lane : 27;
                    f32x4 s = *(LAS f32x4*)(L + T_CS3P + l * 16);
#pragma unroll
                    for (int k = 1; k < 4; ++k) s += *(LAS f32x4*)(L + T_CS3P + (k * 28 + l) * 16);
                    *(LAS f32x4*)(L + T_CS3 + l * 16) = s;
                }
            }
            auto w_lo = [&](int ks, int b) { return __builtin_amdgcn_ds_read_tr16_b64_v4i16((LAS s16x4*)(L + tb_[b] + ks * 4096)); };
            auto w_hi = [&](int ks, int b) { return __builtin_amdgcn_ds_read_tr16_b64_v4i16((LAS s16x4*)(L + tb_[b] + ks * 4096 + 512)); };
            const unsigned sto = WS_ST + layer * ST_BYTES + sb0 * 1024, sv0 = (2 * q * 16 + i16) * 8;
            f32x4 a3[7][4];
#pragma unroll
            for (int b = 0; b < 7; ++b)
#pragma unroll
                for (int t = 0; t < 4; ++t) a3[b][t] = (f32x4){0.f, 0.f, 0.f, 0.f};
            bf16x8 wf[7];
#pragma unroll
            for (int b = 0; b < 6; ++b) wf[b] = ldsT(L, tb_[b]);
            sfor<16>([&](auto KS) {
                constexpr int ks = CI(KS), sl = ks % (P3_LA + 1);
                wait4<p3_wait(ks)>(act[sl][0], act[sl][1], act[sl][2], act[sl][3]);
                if constexpr (ks == 0) STAMP(32 + 8 * layer);
                sfor<7>([&](auto B) {
                    constexpr int b = CI(B), pb = b == 0 ? 6 : b - 1, pks = b == 0 ? ks : ks + 1;
                    s16x4 lo, hi;
                    a3[b][0] = mfma(wf[b], act[sl][0], a3[b][0]);
                    SCHED();
                    if constexpr (pks < 16) lo = w_lo(pks, pb);
                    SCHED();
                    a3[b][1] = mfma(wf[b], act[sl][1], a3[b][1]);
                    SCHED();
                    if constexpr (pks < 16) { hi = w_hi(pks, pb); wf[pb] = __builtin_shufflevector(lo, hi, 0, 1, 2, 3, 4, 5, 6, 7); }
                    SCHED();
                    a3[b][2] = mfma(wf[b], act[sl][2], a3[b][2]);
                    SCHED();
                    if constexpr (b >= 1 && b <= 4 && ks + P3_LA < 16) load_x(ks + P3_LA, b - 1);
                    SCHED();
                    a3[b][3] = mfma(wf[b], act[sl][3], a3[b][3]);
                    SCHED();
                    if constexpr (ks == 8 && b == 6) {
#pragma unroll
                        for (int t = 0; t < 4; ++t) { aload2(sa[t], sv0, rw, sto + t * 1024); aload2(sb_[t], sv0 + 128, rw, sto + t * 1024); }
                        SCHED();
                    }
                });
            });
            wait_lds();
            bar();
            STAMP(8 + 16 * layer);

            const int l2 = lane_id();
            const unsigned vE = (l2 >> 3) * 1024 + (((l2 & 7) ^ ((l2 >> 3) & 2)) * 16), vO = vE ^ 64;
            const int ws_ = opq_s(wid);
            const unsigned lw = ldsb + ws_ * 8192, dsb = 448 * ws_ * 1024;
            const rsrc_t rwd = mk(wdn + 128 * ct, F * D * 2 - 128 * ct);
            auto dma_p4 = [&](int c, int p) {
                dma(lw, ((c + 1) & 3) * REG + p * 1024, (p & 1) ? vO : vE, rwd, dsb, p4_row(2 * c + (p >> 2), p & 3) * 1024);
            };
            float mean[4], sig[4], kk_[4];
            asm volatile("" : "+v"(sa[0]), "+v"(sa[1]), "+v"(sa[2]), "+v"(sa[3]), "+v"(sb_[0]), "+v"(sb_[1]), "+v"(sb_[2]), "+v"(sb_[3]));
            {
                float v[4];
                combine_stats<4>(sa, sb_, mean, v);
                float l2e = 1.44269504088896f;
                asm volatile("" : "+s"(l2e));
#pragma unroll
                for (int t = 0; t < 4; ++t) {
                    const float vv = v[t] + EPS, r = __builtin_amdgcn_rsqf(vv);
                    sig[t] = vv * r; kk_[t] = l2e * r;
                }
            }
            auto glu = [&](float G, float U, float cg, float cu, int t) {
                const float e = __builtin_amdgcn_exp2f(kk_[t] * (mean[t] * cg - G));
                return (U - mean[t] * cu) * __builtin_amdgcn_rcpf(sig[t] * e + sig[t]);
            };
            const unsigned ho = WS_H + layer * 7 * MB, hw = 14 * (CT >> 3);
            {
                u32x4 o[4];
                const f32x4 cg0 = *(LAS f32x4*)(L + T_CS3 + (0 * 4 + q) * 16), cu0 = *(LAS f32x4*)(L + T_CS3 + (1 * 4 + q) * 16);
                const f32x4 cg1 = *(LAS f32x4*)(L + T_CS3 + (2 * 4 + q) * 16), cu1 = *(LAS f32x4*)(L + T_CS3 + (3 * 4 + q) * 16);
                sfor<4>([&](auto T) {
                    constexpr int t = CI(T);
                    float h0[4], h1[4];
                    sfor<4>([&](auto R) {
                        constexpr int r = CI(R);
                        h0[r] = glu(a3[0][t][r], a3[1][t][r], cg0[r], cu0[r], t);
                        h1[r] = glu(a3[2][t][r], a3[3][t][r], cg1[r], cu1[r], t);
                        asm volatile("" : "+v"(h0[r]), "+v"(h1[r]));
                        dma_p4(t >> 1, 4 * (t & 1) + r);
                        SCHED();
                    });
                    o[t] = gather8(pk(h0[0], h0[1]), pk(h0[2], h0[3]), pk(h1[0], h1[1]), pk(h1[2], h1[3]));
                });
#pragma unroll
                for (int t = 0; t < 4; ++t) store16(o[t], rw, ho + ((sb0 + t) * 56 + hw + (CT & 7)) * 1024 + vo);
                SCHED();
            }
            STAMP(33 + 8 * layer);
            {
                const f32x4 cg = *(LAS f32x4*)(L + T_CS3 + (4 * 4 + q) * 16), cu = *(LAS f32x4*)(L + T_CS3 + (5 * 4 + q) * 16);
                const f32x4 mg = *(LAS f32x4*)(L + T_CS3 + (6 * 4 + (q & 1)) * 16), mu = *(LAS f32x4*)(L + T_CS3 + (6 * 4 + 2 + (q & 1)) * 16);
                const unsigned beta = 3 * (CT & 7) + q;
                const unsigned hb = q < 3 ? ho + (sb0 * 56 + hw + 8 + (beta >> 2)) * 1024 + ((beta & 3) * 16 + i16) * 16 : 0x7ff00000u;
                u32x4 o[4];
                sfor<4>([&](auto T) {
                    constexpr int t = CI(T);
                    float hv[4], hm[4];
                    if constexpr (t == 2) {
                        wait_vm<8>();
                        bar();
                        publish(rw, 6 * layer + 3, blk, tag + 3, w);
                        STAMP(34 + 8 * layer);
                        SCHED();
                    }
                    sfor<4>([&](auto R) {
                        constexpr int r = CI(R);
                        hv[r] = glu(a3[4][t][r], a3[5][t][r], cg[r], cu[r], t);
                        auto sw = __builtin_amdgcn_permlane32_swap(__float_as_uint(a3[6][t][r]), __float_as_uint(a3[6][t][r]), false, false);
                        hm[r] = glu(__uint_as_float(sw[0]), __uint_as_float(sw[1]), mg[r], mu[r], t);
                        asm volatile("" : "+v"(hv[r]), "+v"(hm[r]));
                        dma_p4(2 + (t >> 1), 4 * (t & 1) + r);
                        SCHED();
                    });
                    o[t] = gather8(pk(hv[0], hv[1]), pk(hv[2], hv[3]), pk(hm[0], hm[1]), pk(hm[2], hm[3]));
                });
#pragma unroll
                for (int t = 0; t < 4; ++t) store16(o[t], rw, hb + t * 56 * 1024);
                SCHED();
            }
            STAMP(35 + 8 * layer);

            poll8(rw, 6 * layer + 3, blk_p3(rt >> 2, 8 * w + (lane & 7)), tag + 3, dead);
            bar();
            publish(rw, 6 * layer + 4, blk, tag + 4, w);
            STAMP(9 + 16 * layer);
            unsigned so[4];
#pragma unroll
            for (int d = 0; d < 4; ++d) so[d] = ho + ((4 * rt + ((ws_ + d) & 3)) * 56 + 14 * ws_) * 1024;
            bf16x8 hact[6][4];
            auto load_h = [&](int u, int d) { aload(hact[u % 6][d], vo, rw, so[d], u * 1024); };
#pragma unroll
            for (int u = 0; u < 5; ++u)
#pragma unroll
                for (int d = 0; d < 4; ++d) load_h(u, d);
            f32x4 acc[4][4];
#pragma unroll
            for (int b = 0; b < 4; ++b)
#pragma unroll
                for (int d = 0; d < 4; ++d) acc[b][d] = (f32x4){0.f, 0.f, 0.f, 0.f};
            unsigned wb_[2][4];
#pragma unroll
            for (int b = 0; b < 4; ++b) {
                wb_[0][b] = w * 8192 + (8 * q + qq) * 128 + (((2 * b + (p4 >> 1)) ^ ((qq & 2) + 4 * (q & 1))) * 16) + 8 * (p4 & 1);
                wb_[1][b] = wb_[0][b] + 2 * REG;
            }
            auto wd_off = [&](int u, int b) { return wb_[(((u >> 1) + 1) & 3) >> 1][b] + (((u >> 1) + 1) & 1) * REG + (u & 1) * 4096; };
            bf16x8 wf4[2][4];
#pragma unroll
            for (int b = 0; b < 4; ++b) wf4[0][b] = ldsT(L, wd_off(0, b));
            u32x2 flb;
            sfor<14>([&](auto U) {
                constexpr int u = CI(U);
                wait4<p4_wait(u)>(hact[u % 6][0], hact[u % 6][1], hact[u % 6][2], hact[u % 6][3]);
                if constexpr (u == 1) {
                    aload2(flb, (unsigned)blk_p3(rt >> 2, 8 * w + (lane & 7)) * 256u, rw, WS_FLAGS + (6 * layer + 4) * 65536u);
                    SCHED();
                }
                if constexpr (u == 3) {
                    asm volatile("" : "+v"(flb));
                    if (!__all((((u64)flb[1] << 32) | flb[0]) == tag + 4)) poll8(rw, 6 * layer + 4, blk_p3(rt >> 2, 8 * w + (lane & 7)), tag + 4, dead);
                    STAMP(10 + 16 * layer);
                    SCHED();
                }
                sfor<8>([&](auto J) {
                    constexpr int j = CI(J), b = j >> 1, d0 = 2 * (j & 1);
                    acc[b][d0] = mfma(wf4[u & 1][b], hact[u % 6][d0], acc[b][d0]);
                    acc[b][d0 + 1] = mfma(wf4[u & 1][b], hact[u % 6][d0 + 1], acc[b][d0 + 1]);
                    SCHED();
                    if constexpr (j < 4 && u < 13) wf4[(u + 1) & 1][j] = ldsT(L, wd_off(u + 1, j));
                    if constexpr ((j & 1) == 0) {
                        constexpr int p = 4 * ((u + 1) & 1) + (j >> 1);
                        if constexpr (u >= 1 && u <= 6) dma_p4(((u - 1) >> 1) + 4, p);
                    } else if constexpr (u <= 8) load_h(u + 5, j >> 1);
                    SCHED();
                });
            });
            STAMP(11 + 16 * layer);
            if (layer == 0) dma_p1_all();
            LAS char* x3 = L + w * 8192 + lane * 16 + 3 * REG;
            LAS char* xm = L + w * 8192 + lane * 16 + LDS_M;
            asm volatile("" : "+v"(x3), "+v"(xm));
#pragma unroll
            for (int d = 1; d < 4; ++d)
#pragma unroll
                for (int b = 0; b < 4; ++b)
                    *(LAS f32x4*)((d < 3 ? x3 : xm) + (d < 3 ? (d - 1) * 4096 : 0) + b * 1024) = acc[b][d];
            wait_lds();
            bar();
            if (layer == 0) dma_p2n(4, 6);
            f32x4 v[4], t[3][4];
#pragma unroll
            for (int d = 1; d < 4; ++d) {
                LAS char* src = L + ((w - d) & 3) * 8192 + lane * 16 + (d < 3 ? 3 * REG + (d - 1) * 4096 : LDS_M);
                asm volatile("" : "+v"(src));
#pragma unroll
                for (int b = 0; b < 4; ++b) t[d - 1][b] = *(LAS f32x4*)(src + b * 1024);
            }
            SCHED();
#pragma unroll
            for (int b = 0; b < 4; ++b) v[b] = ((acc[b][0] + t[0][b]) + t[1][b]) + t[2][b];
            if (layer == 0) {
                finish_strip(v, false, WS_X2, WS_ST + 2 * ST_BYTES);
                wait_vm<0>();
                bar();
                publish(rw, 5, blk, tag + 5, w);
                STAMP(12);
                dma_p2n(6, 8);
            } else {
                finish_strip(v, true, 0, 0);
                STAMP(28);
            }
        }
        if (layer == 1) break;
        phase1(std::false_type{});
    }
}

}

extern "C" void kernel_launch(void* const* d_in, const int* in_sizes, int n_in,
                              void* d_out, int out_size, void* d_ws, size_t ws_size,
                              hipStream_t stream) {
    (void)in_sizes; (void)n_in; (void)out_size; (void)ws_size;
    (void)hipFuncSetAttribute((const void*)fn::fnet_glu_kernel, hipFuncAttributeMaxDynamicSharedMemorySize, fn::LDS_BYTES);
    fn::fnet_glu_kernel<<<256, 256, fn::LDS_BYTES, stream>>>((const char*)d_in[0], (const char*)d_in[1], (const char*)d_in[2],
        (const char*)d_in[3], (const char*)d_in[4], (const char*)d_in[6], (const char*)d_in[7], (const char*)d_in[8],
        (char*)d_out, (char*)d_ws);
}
```

```cpp
#include <hip/hip_runtime.h>
#include <hip/hip_bf16.h>
#include <stdint.h>
#include <type_traits>
#include <utility>
#include <stdio.h>

#define DI __device__ __forceinline__
#define LAS __attribute__((address_space(3)))
#define GAS __attribute__((address_space(1)))
typedef short bf16x8 __attribute__((ext_vector_type(8)));
typedef short s16x4 __attribute__((ext_vector_type(4)));
typedef float f32x4 __attribute__((ext_vector_type(4)));
typedef float f32x2 __attribute__((ext_vector_type(2)));
typedef float f32x16 __attribute__((ext_vector_type(16)));
typedef unsigned u32x4 __attribute__((ext_vector_type(4)));
typedef unsigned u32x2 __attribute__((ext_vector_type(2)));
typedef __bf16 bf16x2 __attribute__((ext_vector_type(2)));
typedef unsigned long long u64;
typedef __amdgpu_buffer_rsrc_t rsrc_t;

#define WARM 2
namespace fn {

constexpr int S = 2048, D = 512, F = 1792;
constexpr unsigned MB = 1u << 20;
constexpr unsigned WS_FLAGS = 4096;
constexpr unsigned WS_ABT = 1 * MB;
constexpr unsigned WS_X1 = 9 * MB;
constexpr unsigned WS_X2 = 13 * MB;
constexpr unsigned WS_H = 15 * MB;
constexpr unsigned WS_ST = 29 * MB;
constexpr unsigned WS_STAMP = 30 * MB;
constexpr unsigned WS_PART = 32 * MB;
constexpr unsigned ST_BYTES = 128 * 1024;

constexpr unsigned REG = 32768;
constexpr unsigned LDS_M = 4 * REG;
constexpr unsigned T_RES = 4096;
constexpr unsigned T_STAT = 6144;
constexpr unsigned T_CS1 = LDS_M + 6400;
constexpr unsigned T_CS3 = LDS_M + 8192 + 6400;
constexpr unsigned T_CS3P = LDS_M + 16384 + 6400;
constexpr unsigned T_DUMMY = LDS_M + 24576 + 6400;
constexpr unsigned LDS_BYTES = 163840;
#define CODE_KB 52
constexpr unsigned CODE_PREFETCH_BYTES = CODE_KB * 1024;
constexpr unsigned SPIN_LIMIT = 200000;
constexpr float EPS = 1e-5f;

#define TLBW 0
#define P1_STAT_KS 15
#define WDEPTH 2
#define WDELAY 0
DI rsrc_t mk(const void* p, unsigned bytes) { return __builtin_amdgcn_make_buffer_rsrc((void*)p, 0, bytes, 0x00020000); }
DI void dma(unsigned lds, int LI, unsigned voff, rsrc_t r, unsigned sb, int SI) {
    unsigned t;
    asm volatile("s_add_u32 m0, %1, %5\n\ts_add_u32 %0, %4, %6\n\tbuffer_load_dwordx4 %2, %3, %0 offen lds"
                 : "=&s"(t) : "s"(lds), "v"(voff), "s"(r), "s"(sb), "i"(LI), "i"(SI) : "m0", "scc", "memory");
}
DI void aload(bf16x8& d, unsigned voff, rsrc_t r, unsigned sb, int SI) {
    asm volatile("buffer_load_dwordx4 %0, %1, %2, %3 offen offset:%4 sc1"
                 : "=v"(d) : "v"(voff), "s"(r), "s"(sb + (SI & ~4095)), "i"(SI & 4095) : "memory");
}
DI void aload2(u32x2& d, unsigned voff, rsrc_t r, unsigned sb) {
    asm volatile("buffer_load_dwordx2 %0, %1, %2, %3 offen sc1" : "=v"(d) : "v"(voff), "s"(r), "s"(sb) : "memory");
}
template <int... Is, class Fn> DI void sfor_impl(std::integer_sequence<int, Is...>, Fn&& f) { (f(std::integral_constant<int, Is>{}), ...); }
template <int N, class Fn> DI void sfor(Fn&& f) { sfor_impl(std::make_integer_sequence<int, N>{}, f); }
#define CI(x) decltype(x)::value
DI int lane_id() { int l; asm volatile("v_mbcnt_lo_u32_b32 %0, -1, 0\n\tv_mbcnt_hi_u32_b32 %0, -1, %0" : "=v"(l)); return l; }
DI int opq_s(int v) { asm volatile("" : "+s"(v)); return v; }
template <int N> DI void wait_vm() { asm volatile("s_waitcnt vmcnt(%0)" :: "i"(N) : "memory"); }
template <int N> DI void wait2(bf16x8& a, bf16x8& b) { asm volatile("s_waitcnt vmcnt(%2)" : "+v"(a), "+v"(b) : "i"(N) : "memory"); }
template <int N> DI void wait4(bf16x8& a, bf16x8& b, bf16x8& c, bf16x8& d) {
    asm volatile("s_waitcnt vmcnt(%4)" : "+v"(a), "+v"(b), "+v"(c), "+v"(d) : "i"(N) : "memory");
}
template <int N> DI void wait8(bf16x8 (&a)[8]) {
    asm volatile("s_waitcnt vmcnt(%8)" : "+v"(a[0]), "+v"(a[1]), "+v"(a[2]), "+v"(a[3]), "+v"(a[4]), "+v"(a[5]), "+v"(a[6]), "+v"(a[7]) : "i"(N) : "memory");
}
DI void wait_lds() { asm volatile("s_waitcnt lgkmcnt(0)" ::: "memory"); }
DI void bar() { asm volatile("" ::: "memory"); __builtin_amdgcn_s_barrier(); asm volatile("" ::: "memory"); }
#define SCHED() __builtin_amdgcn_sched_barrier(0)
DI f32x4 mfma(bf16x8 a, bf16x8 b, f32x4 c) { return __builtin_amdgcn_mfma_f32_16x16x32_bf16(a, b, c, 0, 0, 0); }
DI f32x16 mfma32(bf16x8 a, bf16x8 b, f32x16 c) { return __builtin_amdgcn_mfma_f32_32x32x16_bf16(a, b, c, 0, 0, 0); }
DI unsigned pk(float lo, float hi) { f32x2 v = {lo, hi}; return __builtin_bit_cast(unsigned, __builtin_convertvector(v, bf16x2)); }
DI float lo16(unsigned u) { return __uint_as_float(u << 16); }
DI float hi16(unsigned u) { return __uint_as_float(u & 0xffff0000u); }
DI float sum16(float v) { auto r = __builtin_amdgcn_permlane16_swap(__float_as_uint(v), __float_as_uint(v), false, false); return __uint_as_float(r[0]) + __uint_as_float(r[1]); }
DI float sum32(float v) { auto r = __builtin_amdgcn_permlane32_swap(__float_as_uint(v), __float_as_uint(v), false, false); return __uint_as_float(r[0]) + __uint_as_float(r[1]); }
DI float sumq(float v) { return sum32(sum16(v)); }
DI u32x4 gather8(unsigned x0, unsigned x1, unsigned y0, unsigned y1) {
    auto a = __builtin_amdgcn_permlane32_swap(x0, y0, false, false);
    auto b = __builtin_amdgcn_permlane32_swap(x1, y1, false, false);
    auto c = __builtin_amdgcn_permlane16_swap(a[0], a[1], false, false);
    auto d = __builtin_amdgcn_permlane16_swap(b[0], b[1], false, false);
    return (u32x4){c[0], d[0], c[1], d[1]};
}
DI void store16(u32x4 v, rsrc_t r, unsigned voff) { __builtin_amdgcn_raw_buffer_store_b128(v, r, voff, 0, 16); }
DI bf16x8 ldsA(LAS char* L, unsigned off) { return *(LAS bf16x8*)(L + off); }
DI bf16x8 ldsT(LAS char* L, unsigned off) {
    s16x4 lo = __builtin_amdgcn_ds_read_tr16_b64_v4i16((LAS s16x4*)(L + off));
    s16x4 hi = __builtin_amdgcn_ds_read_tr16_b64_v4i16((LAS s16x4*)(L + off + 512));
    return __builtin_shufflevector(lo, hi, 0, 1, 2, 3, 4, 5, 6, 7);
}
DI int blk_p24(int rt, int ct) { return 8 * (4 * ((rt >> 1) & 7) + (ct >> 1)) + 4 * (rt >> 4) + 2 * (rt & 1) + (ct & 1); }
DI int blk_p1(int rt1, int et) { return 8 * (8 * (rt1 & 3) + (et & 7)) + 2 * (rt1 >> 2) + (et >> 3); }
DI int blk_p3(int RT, int CT) { return 8 * (8 * (RT & 3) + (CT & 7)) + 2 * (CT >> 3) + (RT >> 2); }

DI void poll8(rsrc_t rw, int phase, int prod, u64 tag, bool& dead) {
    const unsigned voff = (unsigned)prod * 256u, sb = WS_FLAGS + (unsigned)phase * 65536u;
    if (!dead) {
        unsigned spins = 0;
        for (;;) {
            u64 v;
            asm volatile("buffer_load_dwordx2 %0, %1, %2, %3 offen sc1\n\ts_waitcnt vmcnt(0)" : "=v"(v) : "v"(voff), "s"(rw), "s"(sb) : "memory");
            if (__all(v == tag)) break;
            if (++spins > SPIN_LIMIT) { dead = true; break; }
        }
    }
    asm volatile("" ::: "memory");
}
DI void publish(rsrc_t rw, int phase, int blk, u64 tag, int w) {
    if (w == 0)
        __builtin_amdgcn_raw_buffer_store_b64((u32x2){(unsigned)tag, (unsigned)(tag >> 32)}, rw, WS_FLAGS + (unsigned)(phase * 256 + blk) * 256u, 0, 16);
}

#define NOW_() ((unsigned)__builtin_amdgcn_s_memrealtime())

constexpr int P1_LA = 12;
constexpr int p1_issued(int ks) { return 2 * P1_LA + 2 * (ks < 16 - P1_LA ? ks : 16 - P1_LA); }
constexpr int p1_wait(int ks) { return p1_issued(ks) - 1 - (ks < P1_LA ? 2 * ks + 1 : p1_issued(ks - P1_LA + 1) - 1); }
constexpr int P2_LA = 22;
constexpr int p2_dma(int v) { return (v >= 8 && v <= 39) || (v >= 43 && v <= 50) ? 1 : 0; }
constexpr int p2_ops(int v) { return p2_dma(v) + (v + P2_LA < 64 ? 1 : 0); }
constexpr int p2_issued(int v) { int n = P2_LA; for (int j = 0; j < v; ++j) n += p2_ops(j); return n; }
constexpr int p2_wait(int v) {
    const int pa = v < P2_LA ? v : p2_issued(v - P2_LA + 1) - 1;
    const int c = (v >> 2) + 1;
    const int pd = (v & 3) == 2 && c >= 8 && c < 16 ? p2_issued(4 * (c - 6) + 3) : -1;
    return p2_issued(v) - 1 - (pa > pd ? pa : pd);
}
constexpr int P3_LA = 3;
constexpr int p3_ops(int g) { return (g + P3_LA < 16 ? 4 : 0) + (g == 8 ? 8 : 0); }
constexpr int p3_issued(int g) { int n = 4 * P3_LA; for (int j = 0; j < g; ++j) n += p3_ops(j); return n; }
constexpr int p3_wait(int g) { return p3_issued(g) - 1 - (g < P3_LA ? 4 * g + 3 : p3_issued(g - P3_LA) + 3); }
constexpr int p4_ops(int u) { return (u == 1 ? 1 : 0) + (u >= 1 && u <= 6 ? 4 : 0) + (u <= 8 ? 4 : 0); }
constexpr int p4_issued(int u) { int n = 20; for (int j = 0; j < u; ++j) n += p4_ops(j); return n; }
constexpr int p4_wait(int u) {
    int p = u < 5 ? 4 * u + 3 : p4_issued(u - 4) - 1;
    if (u == 3 && p < p4_issued(1)) p = p4_issued(1);
    if ((u & 1) && u < 13) {
        const int c = (u + 1) >> 1;
        if (c >= 4 && p4_issued(2 * c - 6) + 6 > p) p = p4_issued(2 * c - 6) + 6;
    }
    return p4_issued(u) - 1 - p;
}
constexpr int p4_row(int u, int qq) { return u < 8 ? 56 * u + 8 * qq : 56 * ((4 * (u - 8) + qq) / 3) + 32 + 8 * ((4 * (u - 8) + qq) % 3); }

template <int N> DI void combine_stats(const u32x2 (&a)[N], const u32x2 (&b)[N], float (&mean)[N], float (&var)[N]) {
    float s[N], t[N], inv = 1.0f / 512.0f;
    asm volatile("" : "+s"(inv));
#pragma unroll
    for (int i = 0; i < N; ++i) { s[i] = __uint_as_float(a[i][0]) + __uint_as_float(b[i][0]); t[i] = __uint_as_float(a[i][1]) + __uint_as_float(b[i][1]); }
#pragma unroll
    for (int i = 0; i < N; ++i) { s[i] = sum16(s[i]); t[i] = sum16(t[i]); }
#pragma unroll
    for (int i = 0; i < N; ++i) { s[i] = sum32(s[i]); t[i] = sum32(t[i]); }
#pragma unroll
    for (int i = 0; i < N; ++i) { mean[i] = s[i] * inv; var[i] = fmaxf(t[i] * inv - mean[i] * mean[i], 0.f); }
}

__global__ __launch_bounds__(256, 2) void fnet_glu_kernel(const char* __restrict__ x, const char* __restrict__ wsre, const char* __restrict__ wsim,
        const char* __restrict__ wdre, const char* __restrict__ wdim_, const char* __restrict__ wg, const char* __restrict__ wu,
        const char* __restrict__ wdn, char* __restrict__ out, char* __restrict__ ws) {
    extern __shared__ __attribute__((aligned(16))) char smem[];
    LAS char* L = (LAS char*)smem;
    int wid;
    asm volatile("s_nop 1\n\tv_readfirstlane_b32 %0, %1\n\ts_nop 4" : "=s"(wid) : "v"(threadIdx.x >> 6));
    const int blk = blockIdx.x, xcd = blk & 7, idx = blk >> 3;

    unsigned a_t0 = 0, b_t0 = 0, a_t1 = 0, b_t1 = 0, a_tA = 0, b_tA = 0, a_tB = 0, b_tB = 0, a_tC = 0, b_tC = 0, a_tD = 0, b_tD = 0, a_tE = 0, b_tE = 0, a_tF = 0, b_tF = 0, a_tG = 0, b_tG = 0, a_tH = 0, b_tH = 0, a_tI = 0, b_tI = 0, a_tJ = 0, b_tJ = 0, a_tK = 0, b_tK = 0;
    const unsigned ldsb = (unsigned)(size_t)L;
    const rsrc_t rw = mk(ws, 48 * MB);
    const int T2 = 8 * (xcd >> 2) + (idx >> 2), E2 = idx & 3, j2 = xcd & 3;
    const int rt = 2 * T2 + (j2 >> 1), ct = 2 * E2 + (j2 & 1);
    const int rt1 = 4 * (xcd >> 1) + (idx >> 3), et = 8 * (xcd & 1) + (idx & 7);
    const int RT = 4 * (xcd & 1) + (idx >> 3), CT = 8 * (xcd >> 1) + (idx & 7);
    a_t0 = NOW_();
    if (CODE_PREFETCH_BYTES) {
        u64 pc;
        asm volatile("s_getpc_b64 %0" : "=s"(pc));
        const rsrc_t rc = mk((const void*)(pc & ~(u64)127), CODE_PREFETCH_BYTES);
        const unsigned seg = (unsigned)(idx * 4 + wid) % ((CODE_PREFETCH_BYTES + 8191) / 8192);
        const unsigned o = seg * 8192u + lane_id() * 128u, l = ldsb + T_DUMMY + 768;
        asm volatile("s_mov_b32 m0, %0\n\ts_nop 0\n\tbuffer_load_dword %1, %2, 0 offen lds" :: "s"(l), "v"(o), "s"(rc) : "m0", "memory");
    }

    auto p1_rsrc = [&](unsigned bytes) { return mk(((et >> 3) ? wdim_ : wdre) + (size_t)(et & 7) * 64 * 1024, bytes); };
    auto dma_p1 = [&](int j, int p, rsrc_t r, unsigned v0, unsigned lw, unsigned sb) {
        dma(lw, (32 * j + p) * 1024, v0 ^ (16 * p), r, sb, (32 * j + p) * 1024);
    };
    auto p3_voff = [&](int ln, int up, int odd) -> unsigned {
        const unsigned c = (ln & 7) ^ ((2 * ((ln >> 4) & 1) + 4 * odd) ^ up);
        return (ln >> 3) * 3584 + 16 * (c > 6 ? 6 : c);
    };
    auto dma_p3 = [&](int j, int p, unsigned v, unsigned lw, unsigned sb) {
        dma(lw, j * REG + p * 1024, v, mk((j >> 1) ? wu : wg, D * F * 2), sb, (256 * (j & 1) + 8 * p) * 3584);
    };
    auto dma_p2n = [&](int c0, int c1) {
        const int w = opq_s(wid), ln = lane_id();
        const rsrc_t r = mk(((j2 >> 1) ? wsim : wsre) + ((size_t)128 * T2 * S + 1024 * (j2 & 1)) * 2, (128 * S - 1024 * (j2 & 1)) * 2);
        const unsigned vE = (ln >> 3) * 4096 + (((ln & 7) ^ (ln >> 4)) * 16), vO = vE ^ 64;
#pragma unroll
        for (int c = c0; c < c1; ++c)
#pragma unroll
            for (int p = 0; p < 4; ++p) dma(ldsb + w * 4096, (c & 7) * 16384 + p * 1024, (p & 1) ? vO : vE, r, 32 * w * 4096, p * 32768 + c * 128);
    };
    auto dma_p1_all = [&]() {
        const int w = opq_s(wid), ln = lane_id();
        const rsrc_t r = p1_rsrc(65536);
        const unsigned v0 = (unsigned)(ln * 16) ^ (128 * (w & 1)), lw = ldsb + w * 8192;
#pragma unroll
        for (int j = 0; j < 2; ++j)
#pragma unroll
            for (int p = 0; p < 8; ++p) dma_p1(j, p, r, v0, lw, w * 8192);
    };
    auto warm_ffn = [&]() {
        const int w = opq_s(wid), ln = lane_id();
        const unsigned o = blk * 7168u + (ln < 56 ? ln : 55) * 128u, l = ldsb + T_DUMMY + w * 256;
        auto touch = [&](rsrc_t r) {
            asm volatile("s_mov_b32 m0, %0\n\ts_nop 0\n\tbuffer_load_dword %1, %2, 0 offen lds" :: "s"(l), "v"(o), "s"(r) : "m0", "memory");
        };
        if (w == 0) touch(mk(wg, D * F * 2));
        if (w == 1) touch(mk(wu, D * F * 2));
        if (w == 2) touch(mk(wdn, D * F * 2));
    };
    dma_p1_all();

    u32x2 ev;
    aload2(ev, 0u, rw, 0u);
    u64 epoch = 0;
    bool dead = false;
    const bf16x8 ones = {0x3F80, 0x3F80, 0x3F80, 0x3F80, 0x3F80, 0x3F80, 0x3F80, 0x3F80};

        auto phase1 = [&](auto first_tag) {
            constexpr bool FIRST = decltype(first_tag)::value;
            constexpr int layer = FIRST ? 0 : 1;
            const int w = opq_s(wid), lane = lane_id(), i16 = lane & 15, q = lane >> 4;
            const int sbg = 8 * rt1 + 2 * w;
            bf16x8 xa[2][16];
            u32x2 sa[2], sb_[2];
            f32x4 acc[2][4];
#pragma unroll
            for (int s = 0; s < 2; ++s)
#pragma unroll
                for (int e = 0; e < 4; ++e) acc[s][e] = (f32x4){0.f, 0.f, 0.f, 0.f};
            unsigned wb[4];
#pragma unroll
            for (int k = 0; k < 4; ++k) wb[k] = i16 * 1024 + (((4 * k + q) ^ i16) * 16);
            LAS f32x2* tst = (LAS f32x2*)(L + LDS_M + w * 8192 + T_STAT);
            const float sgn = (et >> 3) ? -1.0f : 1.0f;
            f32x4 aS[2], aQ[2], aC = {0.f, 0.f, 0.f, 0.f};
#pragma unroll
            for (int s = 0; s < 2; ++s) { aS[s] = (f32x4){0.f, 0.f, 0.f, 0.f}; aQ[s] = aS[s]; }
            const unsigned wown = w * 16384;
            const unsigned vo = lane * 16, sbase = WS_X2 + sbg * 16384;
            if constexpr (FIRST) {
                const rsrc_t rx = mk(x, S * D * 2);
                bf16x8 xr[32];
#pragma unroll
                for (int r = 0; r < 32; ++r) aload(xr[r], vo, rx, sbg * 16384, r * 1024);
                LAS char* T0 = L + 2 * REG + w * 8192;
                sfor<2>([&](auto SB) {
                    constexpr int s = CI(SB);
                    sfor<4>([&](auto G) {
                        constexpr int g = CI(G), r0 = 16 * s + 4 * g;
                        wait4<28 - r0>(xr[r0], xr[r0 + 1], xr[r0 + 2], xr[r0 + 3]);
#pragma unroll
                        for (int r = 0; r < 4; ++r)
                            *(LAS bf16x8*)(T0 + ((4 * g + r) >> 3) * REG + ((4 * g + r) & 7) * 1024 + ((lane * 16) ^ ((4 * g + r) * 16))) = xr[r0 + r];
                    });
#pragma unroll
                    for (int ks = 0; ks < 16; ++ks)
                        xa[s][ks] = *(LAS bf16x8*)(T0 + (i16 >> 3) * REG + (i16 & 7) * 1024 + (((4 * ks + q) ^ i16) * 16));
                    wait_lds();
                });
                asm volatile("" : "+v"(ev));
                epoch = ((u64)(unsigned)__builtin_amdgcn_readfirstlane((int)ev[1]) << 32) | (unsigned)__builtin_amdgcn_readfirstlane((int)ev[0]);
                wait_lds();
                bar();
                a_t1 = NOW_();
                dma_p2n(4, 8);
                warm_ffn();
            } else {
                poll8(rw, 5, blk_p24(2 * rt1 + (w >> 1), lane & 7), epoch + 6, dead);
                b_t1 = NOW_();
#pragma unroll
                for (int s = 0; s < 2; ++s) {
                    const unsigned so = WS_ST + 2 * ST_BYTES + (sbg + s) * 1024;
                    aload2(sa[s], (2 * q * 16 + i16) * 8, rw, so);
                    aload2(sb_[s], ((2 * q + 1) * 16 + i16) * 8, rw, so);
                }
#pragma unroll
                for (int ks = 0; ks < P1_LA; ++ks)
#pragma unroll
                    for (int s = 0; s < 2; ++s) aload(xa[s][ks], vo, rw, sbase, s * 16384 + ks * 1024);
                wait2<p1_wait(0)>(xa[0][0], xa[1][0]);
                asm volatile("" : "+v"(sa[0]), "+v"(sa[1]), "+v"(sb_[0]), "+v"(sb_[1]));
            }
            bf16x8 wf[2][4], wo;
#pragma unroll
            for (int e = 0; e < 4; ++e) wf[0][e] = ldsA(L, wb[0] + e * 16384);
            sfor<16>([&](auto KS) {
                constexpr int ks = CI(KS);
                if constexpr (!FIRST && ks > 0) wait2<p1_wait(ks)>(xa[0][ks], xa[1][ks]);
                if constexpr (FIRST) wo = ldsA(L, wb[ks & 3] + wown + 256 * (ks >> 2));
                sfor<4>([&](auto E) {
                    constexpr int e = CI(E);
                    acc[0][e] = mfma(xa[0][ks], wf[ks & 1][e], acc[0][e]);
                    acc[1][e] = mfma(xa[1][ks], wf[ks & 1][e], acc[1][e]);
                    SCHED();
                    if constexpr (ks < 15) wf[(ks + 1) & 1][e] = ldsA(L, wb[(ks + 1) & 3] + e * 16384 + 256 * ((ks + 1) >> 2));
                    if constexpr (!FIRST && e >= 2 && ks + P1_LA < 16) aload(xa[e - 2][ks + P1_LA], vo, rw, sbase, (e - 2) * 16384 + (ks + P1_LA) * 1024);
                    SCHED();
                });
                if constexpr (!FIRST && ks == P1_STAT_KS) {
                    float mean[2], var[2];
                    combine_stats<2>(sa, sb_, mean, var);
#pragma unroll
                    for (int s = 0; s < 2; ++s) tst[16 * s + i16] = (f32x2){mean[s], sgn * __builtin_amdgcn_rsqf(var[s] + EPS)};
                    SCHED();
                }
                if constexpr (FIRST) {
                    aC = mfma(ones, wo, aC);
#pragma unroll
                    for (int s = 0; s < 2; ++s) { aS[s] = mfma(ones, xa[s][ks], aS[s]); aQ[s] = mfma(xa[s][ks], xa[s][ks], aQ[s]); }
                    SCHED();
                }
            });
            if constexpr (FIRST) {
                ((LAS float*)(L + T_CS1))[16 * w + i16] = aC[0];
#pragma unroll
                for (int s = 0; s < 2; ++s) {
                    const int r3 = i16 & 3;
                    float dq = r3 == 0 ? aQ[s][0] : r3 == 1 ? aQ[s][1] : r3 == 2 ? aQ[s][2] : aQ[s][3];
                    dq = (q == (i16 >> 2)) ? dq : 0.f;
                    const float mean = aS[s][0] * (1.0f / 512.0f);
                    const float var = fmaxf(sumq(dq) * (1.0f / 512.0f) - mean * mean, 0.f);
                    tst[16 * s + i16] = (f32x2){mean, sgn * __builtin_amdgcn_rsqf(var + EPS)};
                }
            }
            wait_lds();
            bar();
            { const unsigned t_ = NOW_(); if (layer == 0) a_tA = t_; else b_tA = t_; };
            dma_p2n(0, 2);
            f32x4 st0[2], st1[2];
#pragma unroll
            for (int s = 0; s < 2; ++s) {
                const f32x4 a = *(LAS f32x4*)(tst + 16 * s + 4 * q), b = *(LAS f32x4*)(tst + 16 * s + 4 * q + 2);
                st0[s] = (f32x4){a[0], a[2], b[0], b[2]}; st1[s] = (f32x4){a[1], a[3], b[1], b[3]};
            }
#pragma unroll
            for (int E = 0; E < 2; ++E) {
                const float cs0 = ((LAS float*)(L + T_CS1))[32 * E + i16], cs1 = ((LAS float*)(L + T_CS1))[32 * E + 16 + i16];
#pragma unroll
                for (int s = 0; s < 2; ++s) {
                    f32x4 v0, v1;
#pragma unroll
                    for (int r = 0; r < 4; ++r) {
                        v0[r] = (acc[s][2 * E][r] - st0[s][r] * cs0) * st1[s][r];
                        v1[r] = (acc[s][2 * E + 1][r] - st0[s][r] * cs1) * st1[s][r];
                    }
                    auto a = __builtin_amdgcn_permlane16_swap(pk(v0[0], v0[1]), pk(v1[0], v1[1]), false, false);
                    auto b = __builtin_amdgcn_permlane16_swap(pk(v0[2], v0[3]), pk(v1[2], v1[3]), false, false);
                    store16((u32x4){a[0], b[0], a[1], b[1]}, rw,
                            WS_ABT + layer * 4 * MB + (et >> 3) * 2 * MB + ((2 * (et & 7) + E) * 128 + sbg + s) * 1024 + lane * 16);
                }
            }
        };

    phase1(std::true_type{});
    for (int layer = 0; layer < 2; ++layer) {
        const u64 tag = epoch + 1 + 6 * layer;
        auto finish_strip = [&](f32x4 (&v)[4], bool last, unsigned xo, unsigned sto) {
            const int w = opq_s(wid), lane = lane_id(), i16 = lane & 15, q = lane >> 4;
            float sum = 0.f, sq = 0.f;
            u32x2 res[4];
            {
                const u32x4 r0 = *(LAS u32x4*)(L + LDS_M + w * 8192 + T_RES + lane * 16), r1 = *(LAS u32x4*)(L + LDS_M + w * 8192 + T_RES + 1024 + lane * 16);
                res[0] = (u32x2){r0[0], r0[1]}; res[1] = (u32x2){r0[2], r0[3]}; res[2] = (u32x2){r1[0], r1[1]}; res[3] = (u32x2){r1[2], r1[3]};
            }
#pragma unroll
            for (int b = 0; b < 4; ++b) {
                res[b][0] = pk(lo16(res[b][0]) + v[b][0], hi16(res[b][0]) + v[b][1]);
                res[b][1] = pk(lo16(res[b][1]) + v[b][2], hi16(res[b][1]) + v[b][3]);
                const float x0 = lo16(res[b][0]), x1 = hi16(res[b][0]), x2 = lo16(res[b][1]), x3 = hi16(res[b][1]);
                sum += (x0 + x1) + (x2 + x3);
                sq += (x0 * x0 + x1 * x1) + (x2 * x2 + x3 * x3);
            }
            const u32x4 o0 = gather8(res[0][0], res[0][1], res[1][0], res[1][1]);
            const u32x4 o1 = gather8(res[2][0], res[2][1], res[3][0], res[3][1]);
            if (last) {
                const rsrc_t rxo = mk(out, S * D * 2);
                const unsigned a = (64 * rt + 16 * w + i16) * 1024 + (64 * ct + 8 * q) * 2;
                __builtin_amdgcn_raw_buffer_store_b128(o0, rxo, a, 0, 0);
                __builtin_amdgcn_raw_buffer_store_b128(o1, rxo, a + 64, 0, 0);
            } else {
                const unsigned a = xo + ((4 * rt + w) * 16 + 2 * ct) * 1024 + lane * 16;
                store16(o0, rw, a); store16(o1, rw, a + 1024);
                *(LAS u32x4*)(L + LDS_M + w * 8192 + T_RES + lane * 16) = (u32x4){res[0][0], res[0][1], res[1][0], res[1][1]};
                *(LAS u32x4*)(L + LDS_M + w * 8192 + T_RES + 1024 + lane * 16) = (u32x4){res[2][0], res[2][1], res[3][0], res[3][1]};
                sum = sum16(sum); sq = sum16(sq);
                sum = sum32(sum); sq = sum32(sq);
                __builtin_amdgcn_raw_buffer_store_b64((u32x2){__float_as_uint(sum), __float_as_uint(sq)}, rw, sto + (((4 * rt + w) * 8 + ct) * 16 + i16) * 8, 0, 16);
            }
        };

        {
            const int w = opq_s(wid), lane = lane_id(), i16 = lane & 15, q = lane >> 4;
            const int part = j2 >> 1, half = j2 & 1, r32 = lane & 31, hh = lane >> 5;
            const rsrc_t rws = mk((part ? wsim : wsre) + ((size_t)128 * T2 * S + 1024 * half) * 2, (128 * S - 1024 * half) * 2);
            const unsigned vsE = (lane >> 3) * 4096 + (((lane & 7) ^ (lane >> 4)) * 16), vsO = vsE ^ 64;
            const int w4_ = opq_s(wid);
            const unsigned lw4 = ldsb + w4_ * 4096, wsb = 32 * w4_ * 4096;
            auto dma_p2 = [&](int c, int p) { dma(lw4, (c & 7) * 16384 + p * 1024, (p & 1) ? vsO : vsE, rws, wsb, p * 32768 + c * 128); };
            wait_vm<0>();
            bar();
            publish(rw, 6 * layer + 0, blk, tag, w);
            { const unsigned t_ = NOW_(); if (layer == 0) a_tB = t_; else b_tB = t_; };
            dma_p2n(2, 4);
            if (layer == 0) {
                u32x2 res0[4];
                const rsrc_t rx = mk(x, S * D * 2);
                const unsigned vr = (64 * rt + 16 * w + i16) * 1024 + (64 * ct + 4 * q) * 2;
#pragma unroll
                for (int b = 0; b < 4; ++b) aload2(res0[b], vr + b * 32, rx, 0u);
                asm volatile("s_waitcnt vmcnt(0)" : "+v"(res0[0]), "+v"(res0[1]), "+v"(res0[2]), "+v"(res0[3]) :: "memory");
                *(LAS u32x4*)(L + LDS_M + w * 8192 + T_RES + lane * 16) = (u32x4){res0[0][0], res0[0][1], res0[1][0], res0[1][1]};
                *(LAS u32x4*)(L + LDS_M + w * 8192 + T_RES + 1024 + lane * 16) = (u32x4){res0[2][0], res0[2][1], res0[3][0], res0[3][1]};
            }
            poll8(rw, 6 * layer + 0, blk_p1(8 * half + (lane & 7), 8 * part + 2 * E2 + (w >> 1)), tag, dead);
            { const unsigned t_ = NOW_(); if (layer == 0) a_tC = t_; else b_tC = t_; };
            const unsigned so0 = WS_ABT + layer * 4 * MB + part * 2 * MB + ((4 * E2 + w) * 128 + 64 * half) * 1024, vo = lane * 16;
            bf16x8 act[P2_LA + 2];
            auto load_act = [&](int v) { aload(act[v % (P2_LA + 2)], vo, rw, so0, v * 1024); };
#pragma unroll
            for (int v = 0; v < P2_LA; ++v) load_act(v);
            f32x16 acc[4];
#pragma unroll
            for (int t = 0; t < 4; ++t)
#pragma unroll
                for (int r = 0; r < 16; ++r) acc[t][r] = 0.f;
            unsigned rb[2][4];
#pragma unroll
            for (int k = 0; k < 4; ++k) { rb[0][k] = r32 * 128 + (((2 * k + hh) ^ ((r32 >> 1) & 7)) * 16); rb[1][k] = rb[0][k] + 65536; }
            auto ws_frag = [&](int n) {
                const int v = n >> 2, t = n & 3, c = v >> 2;
                return ldsA(L, rb[(c & 7) >> 2][v & 3] + (c & 3) * 16384 + t * 4096);
            };
            const int ws_ = opq_s(wid);
            const unsigned lw = ldsb + ws_ * 8192, g_sb = 64 * ws_ * 3584 + 112 * CT;
            unsigned gg0 = 0, gg1 = 0;
            bf16x8 wf[8];
#pragma unroll
            for (int n = 0; n < 6; ++n) wf[n] = ws_frag(n);
            sfor<64>([&](auto V) {
                constexpr int v = CI(V);
                asm volatile("s_waitcnt vmcnt(%1)" : "+v"(act[v % (P2_LA + 2)]) : "i"(p2_wait(v)) : "memory");
                if constexpr (v == 43) { const int l2 = lane_id(); gg0 = p3_voff(l2, 0, 0); gg1 = p3_voff(l2, 0, 1); }
                sfor<4>([&](auto TB) {
                    constexpr int t = CI(TB), n = 4 * v + t;
                    acc[t] = mfma32(act[v % (P2_LA + 2)], wf[n & 7], acc[t]);
                    SCHED();
                    if constexpr ((v & 3) == 2 && t == 2 && v < 60) bar();
                    if constexpr (n + 6 < 256) wf[(n + 6) & 7] = ws_frag(n + 6);
                    if constexpr (t == 1 && p2_dma(v)) {
                        if constexpr (v <= 39) dma_p2((v >> 2) + 6, v & 3);
                        else dma_p3(0, v - 43, ((v - 43) & 1) ? gg1 : gg0, lw, g_sb);
                    }
                    if constexpr (t == 3 && v + P2_LA < 64) load_act(v + P2_LA);
                    SCHED();
                });
            });
            { const unsigned t_ = NOW_(); if (layer == 0) a_tD = t_; else b_tD = t_; };
            const unsigned pgrp = WS_PART + layer * 8 * MB + (T2 * 4 + E2) * 131072;
            const int lx = lane_id(), i16x = lx & 15, tup = (lx >> 4) & 1, hx = lx >> 5;
#pragma unroll
            for (int a = 0; a < 2; ++a) {
                const int owner = 2 * a + (w >> 1);
                if (owner == j2) {
#pragma unroll
                    for (int u = 0; u < 2; ++u)
#pragma unroll
                        for (int g = 0; g < 4; ++g) {
                            const f32x16& c = acc[2 * a + u];
                            *(LAS f32x4*)(L + LDS_M + (2 * u + tup) * 8192 + (2 * (w & 1) + (g >> 1)) * 1024 + (i16x + 16 * (2 * (g & 1) + hx)) * 16) =
                                (f32x4){c[4 * g], c[4 * g + 1], c[4 * g + 2], c[4 * g + 3]};
                        }
                } else {
                    const unsigned po = pgrp + owner * 32768 + j2 * 8192 + (w & 1) * 1024 + tup * 2048 + (i16x + 16 * hx) * 16;
#pragma unroll
                    for (int u = 0; u < 2; ++u)
#pragma unroll
                        for (int qh = 0; qh < 2; ++qh) {
                            const f32x16& c = acc[2 * a + u];
                            store16((u32x4){pk(c[4 * qh], c[4 * qh + 1]), pk(c[4 * qh + 2], c[4 * qh + 3]),
                                            pk(c[8 + 4 * qh], c[8 + 4 * qh + 1]), pk(c[8 + 4 * qh + 2], c[8 + 4 * qh + 3])}, rw, po + u * 4096 + qh * 512);
                        }
                }
            }
            wait_vm<0>();
            wait_lds();
            bar();
            publish(rw, 6 * layer + 1, blk, tag + 1, w);
            { const unsigned t_ = NOW_(); if (layer == 0) a_tI = t_; else b_tI = t_; };
            const unsigned gu0 = p3_voff(lx, 1, 0), gu1 = p3_voff(lx, 1, 1), vx = lx * 16;
#pragma unroll
            for (int n = 0; n < 8; ++n) dma_p3(1, n, (n & 1) ? gg1 : gg0, lw, g_sb);
#pragma unroll
            for (int n = 0; n < 3; ++n) dma_p3(2, n, (n & 1) ? gu1 : gu0, lw, g_sb);
            poll8(rw, 6 * layer + 1, (blk & ~3) | (lx & 3), tag + 1, dead);
            { const unsigned t_ = NOW_(); if (layer == 0) a_tJ = t_; else b_tJ = t_; };
            bf16x8 pv[3][2];
#pragma unroll
            for (int d = 1; d < 4; ++d)
#pragma unroll
                for (int h = 0; h < 2; ++h) aload(pv[d - 1][h], vx, rw, pgrp + j2 * 32768 + ((j2 + d) & 3) * 8192 + w * 2048, h * 1024);
            f32x4 v[4];
#pragma unroll
            for (int b = 0; b < 4; ++b) v[b] = *(LAS f32x4*)(L + LDS_M + w * 8192 + b * 1024 + vx);
            asm volatile("s_waitcnt vmcnt(0)" : "+v"(pv[0][0]), "+v"(pv[0][1]), "+v"(pv[1][0]), "+v"(pv[1][1]), "+v"(pv[2][0]), "+v"(pv[2][1]) :: "memory");
#pragma unroll
            for (int n = 3; n < 8; ++n) dma_p3(2, n, (n & 1) ? gu1 : gu0, lw, g_sb);
#pragma unroll
            for (int d = 0; d < 3; ++d)
#pragma unroll
                for (int h = 0; h < 2; ++h) {
                    const u32x4 p = __builtin_bit_cast(u32x4, pv[d][h]);
                    v[2 * h] += (f32x4){lo16(p[0]), hi16(p[0]), lo16(p[1]), hi16(p[1])};
                    v[2 * h + 1] += (f32x4){lo16(p[2]), hi16(p[2]), lo16(p[3]), hi16(p[3])};
                }
            finish_strip(v, false, WS_X1 + layer * 2 * MB, WS_ST + layer * ST_BYTES);
            wait_vm<0>();
            bar();
            publish(rw, 6 * layer + 2, blk, tag + 2, w);
            { const unsigned t_ = NOW_(); if (layer == 0) a_tE = t_; else b_tE = t_; };
#pragma unroll
            for (int n = 0; n < 8; ++n) dma_p3(3, n, (n & 1) ? gu1 : gu0, lw, g_sb);
        }

        {
            const int w = opq_s(wid), lane = lane_id(), i16 = lane & 15, q = lane >> 4;
            poll8(rw, 6 * layer + 2, blk_p24(4 * RT + w, lane & 7), tag + 2, dead);
            if (layer == 0 && blk == 0 && w == 0) {
                u64 e2 = epoch; asm volatile("" : "+s"(e2)); e2 += 64;
                __builtin_amdgcn_raw_buffer_store_b64((u32x2){(unsigned)e2, (unsigned)(e2 >> 32)}, rw, 0, 0, 16);
                wait_vm<0>();
            }
            { const unsigned t_ = NOW_(); if (layer == 0) a_tF = t_; else b_tF = t_; };
            const int sb0 = 16 * RT + 4 * w;
            u32x2 sa[4], sb_[4];
            bf16x8 act[P3_LA + 1][4];
            const unsigned vo = lane * 16, xo = WS_X1 + layer * 2 * MB + sb0 * 16384;
            auto load_x = [&](int g, int t) { aload(act[g % (P3_LA + 1)][t], vo, rw, xo, t * 16384 + g * 1024); };
#pragma unroll
            for (int g = 0; g < P3_LA; ++g)
#pragma unroll
                for (int t = 0; t < 4; ++t) load_x(g, t);
            bar();
            const int qq = (lane >> 2) & 3, p4 = lane & 3;
            unsigned tb_[7];
            {
                const unsigned f = 2 * (qq >> 1) + 4 * (q & 1), rowb = (8 * q + qq) * 128 + 8 * (p4 & 1);
#pragma unroll
                for (int b = 0; b < 6; ++b) tb_[b] = (b & 1) * 65536 + rowb + (((2 * (b >> 1) + (p4 >> 1)) ^ f ^ (b & 1)) * 16);
                tb_[6] = (p4 >> 1) * 65536 + rowb + ((6 ^ f ^ (p4 >> 1)) * 16);
            }
            if (layer == 0) {
                f32x4 ac[7];
#pragma unroll
                for (int b = 0; b < 7; ++b) ac[b] = (f32x4){0.f, 0.f, 0.f, 0.f};
#pragma unroll
                for (int k = 0; k < 4; ++k)
#pragma unroll
                    for (int b = 0; b < 7; ++b) ac[b] = mfma(ldsT(L, tb_[b] + w * 16384 + k * 4096), ones, ac[b]);
#pragma unroll
                for (int b = 0; b < 7; ++b) *(LAS f32x4*)(L + T_CS3P + ((w * 7 + b) * 4 + q) * 16) = ac[b];
                wait_lds();
                bar();
                if (w == 0) {
                    const int l = lane < 28 ? lane : 27;
                    f32x4 s = *(LAS f32x4*)(L + T_CS3P + l * 16);
#pragma unroll
                    for (int k = 1; k < 4; ++k) s += *(LAS f32x4*)(L + T_CS3P + (k * 28 + l) * 16);
                    *(LAS f32x4*)(L + T_CS3 + l * 16) = s;
                }
            }
            auto w_lo = [&](int ks, int b) { return __builtin_amdgcn_ds_read_tr16_b64_v4i16((LAS s16x4*)(L + tb_[b] + ks * 4096)); };
            auto w_hi = [&](int ks, int b) { return __builtin_amdgcn_ds_read_tr16_b64_v4i16((LAS s16x4*)(L + tb_[b] + ks * 4096 + 512)); };
            const unsigned sto = WS_ST + layer * ST_BYTES + sb0 * 1024, sv0 = (2 * q * 16 + i16) * 8;
            f32x4 a3[7][4];
#pragma unroll
            for (int b = 0; b < 7; ++b)
#pragma unroll
                for (int t = 0; t < 4; ++t) a3[b][t] = (f32x4){0.f, 0.f, 0.f, 0.f};
            bf16x8 wf[7];
#pragma unroll
            for (int b = 0; b < 6; ++b) wf[b] = ldsT(L, tb_[b]);
            sfor<16>([&](auto KS) {
                constexpr int ks = CI(KS), sl = ks % (P3_LA + 1);
                wait4<p3_wait(ks)>(act[sl][0], act[sl][1], act[sl][2], act[sl][3]);
                if constexpr (ks == 0) {};
                sfor<7>([&](auto B) {
                    constexpr int b = CI(B), pb = b == 0 ? 6 : b - 1, pks = b == 0 ? ks : ks + 1;
                    s16x4 lo, hi;
                    a3[b][0] = mfma(wf[b], act[sl][0], a3[b][0]);
                    SCHED();
                    if constexpr (pks < 16) lo = w_lo(pks, pb);
                    SCHED();
                    a3[b][1] = mfma(wf[b], act[sl][1], a3[b][1]);
                    SCHED();
                    if constexpr (pks < 16) { hi = w_hi(pks, pb); wf[pb] = __builtin_shufflevector(lo, hi, 0, 1, 2, 3, 4, 5, 6, 7); }
                    SCHED();
                    a3[b][2] = mfma(wf[b], act[sl][2], a3[b][2]);
                    SCHED();
                    if constexpr (b >= 1 && b <= 4 && ks + P3_LA < 16) load_x(ks + P3_LA, b - 1);
                    SCHED();
                    a3[b][3] = mfma(wf[b], act[sl][3], a3[b][3]);
                    SCHED();
                    if constexpr (ks == 8 && b == 6) {
#pragma unroll
                        for (int t = 0; t < 4; ++t) { aload2(sa[t], sv0, rw, sto + t * 1024); aload2(sb_[t], sv0 + 128, rw, sto + t * 1024); }
                        SCHED();
                    }
                });
            });
            wait_lds();
            bar();
            { const unsigned t_ = NOW_(); if (layer == 0) a_tG = t_; else b_tG = t_; };

            const int l2 = lane_id();
            const unsigned vE = (l2 >> 3) * 1024 + (((l2 & 7) ^ ((l2 >> 3) & 2)) * 16), vO = vE ^ 64;
            const int ws_ = opq_s(wid);
            const unsigned lw = ldsb + ws_ * 8192, dsb = 448 * ws_ * 1024;
            const rsrc_t rwd = mk(wdn + 128 * ct, F * D * 2 - 128 * ct);
            auto dma_p4 = [&](int c, int p) {
                dma(lw, ((c + 1) & 3) * REG + p * 1024, (p & 1) ? vO : vE, rwd, dsb, p4_row(2 * c + (p >> 2), p & 3) * 1024);
            };
            float mean[4], sig[4], kk_[4];
            asm volatile("" : "+v"(sa[0]), "+v"(sa[1]), "+v"(sa[2]), "+v"(sa[3]), "+v"(sb_[0]), "+v"(sb_[1]), "+v"(sb_[2]), "+v"(sb_[3]));
            {
                float v[4];
                combine_stats<4>(sa, sb_, mean, v);
                float l2e = 1.44269504088896f;
                asm volatile("" : "+s"(l2e));
#pragma unroll
                for (int t = 0; t < 4; ++t) {
                    const float vv = v[t] + EPS, r = __builtin_amdgcn_rsqf(vv);
                    sig[t] = vv * r; kk_[t] = l2e * r;
                }
            }
            auto glu = [&](float G, float U, float cg, float cu, int t) {
                const float e = __builtin_amdgcn_exp2f(kk_[t] * (mean[t] * cg - G));
                return (U - mean[t] * cu) * __builtin_amdgcn_rcpf(sig[t] * e + sig[t]);
            };
            const unsigned ho = WS_H + layer * 7 * MB, hw = 14 * (CT >> 3);
            {
                u32x4 o[4];
                const f32x4 cg0 = *(LAS f32x4*)(L + T_CS3 + (0 * 4 + q) * 16), cu0 = *(LAS f32x4*)(L + T_CS3 + (1 * 4 + q) * 16);
                const f32x4 cg1 = *(LAS f32x4*)(L + T_CS3 + (2 * 4 + q) * 16), cu1 = *(LAS f32x4*)(L + T_CS3 + (3 * 4 + q) * 16);
                sfor<4>([&](auto T) {
                    constexpr int t = CI(T);
                    float h0[4], h1[4];
                    sfor<4>([&](auto R) {
                        constexpr int r = CI(R);
                        h0[r] = glu(a3[0][t][r], a3[1][t][r], cg0[r], cu0[r], t);
                        h1[r] = glu(a3[2][t][r], a3[3][t][r], cg1[r], cu1[r], t);
                        asm volatile("" : "+v"(h0[r]), "+v"(h1[r]));
                        dma_p4(t >> 1, 4 * (t & 1) + r);
                        SCHED();
                    });
                    o[t] = gather8(pk(h0[0], h0[1]), pk(h0[2], h0[3]), pk(h1[0], h1[1]), pk(h1[2], h1[3]));
                });
#pragma unroll
                for (int t = 0; t < 4; ++t) store16(o[t], rw, ho + ((sb0 + t) * 56 + hw + (CT & 7)) * 1024 + vo);
                SCHED();
            }
            {};
            {
                const f32x4 cg = *(LAS f32x4*)(L + T_CS3 + (4 * 4 + q) * 16), cu = *(LAS f32x4*)(L + T_CS3 + (5 * 4 + q) * 16);
                const f32x4 mg = *(LAS f32x4*)(L + T_CS3 + (6 * 4 + (q & 1)) * 16), mu = *(LAS f32x4*)(L + T_CS3 + (6 * 4 + 2 + (q & 1)) * 16);
                const unsigned beta = 3 * (CT & 7) + q;
                const unsigned hb = q < 3 ? ho + (sb0 * 56 + hw + 8 + (beta >> 2)) * 1024 + ((beta & 3) * 16 + i16) * 16 : 0x7ff00000u;
                u32x4 o[4];
                sfor<4>([&](auto T) {
                    constexpr int t = CI(T);
                    float hv[4], hm[4];
                    if constexpr (t == 2) {
                        wait_vm<8>();
                        bar();
                        publish(rw, 6 * layer + 3, blk, tag + 3, w);
                        {};
                        SCHED();
                    }
                    sfor<4>([&](auto R) {
                        constexpr int r = CI(R);
                        hv[r] = glu(a3[4][t][r], a3[5][t][r], cg[r], cu[r], t);
                        auto sw = __builtin_amdgcn_permlane32_swap(__float_as_uint(a3[6][t][r]), __float_as_uint(a3[6][t][r]), false, false);
                        hm[r] = glu(__uint_as_float(sw[0]), __uint_as_float(sw[1]), mg[r], mu[r], t);
                        asm volatile("" : "+v"(hv[r]), "+v"(hm[r]));
                        dma_p4(2 + (t >> 1), 4 * (t & 1) + r);
                        SCHED();
                    });
                    o[t] = gather8(pk(hv[0], hv[1]), pk(hv[2], hv[3]), pk(hm[0], hm[1]), pk(hm[2], hm[3]));
                });
#pragma unroll
                for (int t = 0; t < 4; ++t) store16(o[t], rw, hb + t * 56 * 1024);
                SCHED();
            }
            {};

            poll8(rw, 6 * layer + 3, blk_p3(rt >> 2, 8 * w + (lane & 7)), tag + 3, dead);
            bar();
            publish(rw, 6 * layer + 4, blk, tag + 4, w);
            {};
            unsigned so[4];
#pragma unroll
            for (int d = 0; d < 4; ++d) so[d] = ho + ((4 * rt + ((ws_ + d) & 3)) * 56 + 14 * ws_) * 1024;
            bf16x8 hact[6][4];
            auto load_h = [&](int u, int d) { aload(hact[u % 6][d], vo, rw, so[d], u * 1024); };
#pragma unroll
            for (int u = 0; u < 5; ++u)
#pragma unroll
                for (int d = 0; d < 4; ++d) load_h(u, d);
            f32x4 acc[4][4];
#pragma unroll
            for (int b = 0; b < 4; ++b)
#pragma unroll
                for (int d = 0; d < 4; ++d) acc[b][d] = (f32x4){0.f, 0.f, 0.f, 0.f};
            unsigned wb_[2][4];
#pragma unroll
            for (int b = 0; b < 4; ++b) {
                wb_[0][b] = w * 8192 + (8 * q + qq) * 128 + (((2 * b + (p4 >> 1)) ^ ((qq & 2) + 4 * (q & 1))) * 16) + 8 * (p4 & 1);
                wb_[1][b] = wb_[0][b] + 2 * REG;
            }
            auto wd_off = [&](int u, int b) { return wb_[(((u >> 1) + 1) & 3) >> 1][b] + (((u >> 1) + 1) & 1) * REG + (u & 1) * 4096; };
            bf16x8 wf4[2][4];
#pragma unroll
            for (int b = 0; b < 4; ++b) wf4[0][b] = ldsT(L, wd_off(0, b));
            u32x2 flb;
            sfor<14>([&](auto U) {
                constexpr int u = CI(U);
                wait4<p4_wait(u)>(hact[u % 6][0], hact[u % 6][1], hact[u % 6][2], hact[u % 6][3]);
                if constexpr (u == 1) {
                    aload2(flb, (unsigned)blk_p3(rt >> 2, 8 * w + (lane & 7)) * 256u, rw, WS_FLAGS + (6 * layer + 4) * 65536u);
                    SCHED();
                }
                if constexpr (u == 3) {
                    asm volatile("" : "+v"(flb));
                    if (!__all((((u64)flb[1] << 32) | flb[0]) == tag + 4)) poll8(rw, 6 * layer + 4, blk_p3(rt >> 2, 8 * w + (lane & 7)), tag + 4, dead);
                    {};
                    SCHED();
                }
                sfor<8>([&](auto J) {
                    constexpr int j = CI(J), b = j >> 1, d0 = 2 * (j & 1);
                    acc[b][d0] = mfma(wf4[u & 1][b], hact[u % 6][d0], acc[b][d0]);
                    acc[b][d0 + 1] = mfma(wf4[u & 1][b], hact[u % 6][d0 + 1], acc[b][d0 + 1]);
                    SCHED();
                    if constexpr (j < 4 && u < 13) wf4[(u + 1) & 1][j] = ldsT(L, wd_off(u + 1, j));
                    if constexpr ((j & 1) == 0) {
                        constexpr int p = 4 * ((u + 1) & 1) + (j >> 1);
                        if constexpr (u >= 1 && u <= 6) dma_p4(((u - 1) >> 1) + 4, p);
                    } else if constexpr (u <= 8) load_h(u + 5, j >> 1);
                    SCHED();
                });
            });
            { const unsigned t_ = NOW_(); if (layer == 0) a_tH = t_; else b_tH = t_; };
            if (layer == 0) dma_p1_all();
            LAS char* x3 = L + w * 8192 + lane * 16 + 3 * REG;
            LAS char* xm = L + w * 8192 + lane * 16 + LDS_M;
            asm volatile("" : "+v"(x3), "+v"(xm));
#pragma unroll
            for (int d = 1; d < 4; ++d)
#pragma unroll
                for (int b = 0; b < 4; ++b)
                    *(LAS f32x4*)((d < 3 ? x3 : xm) + (d < 3 ? (d - 1) * 4096 : 0) + b * 1024) = acc[b][d];
            wait_lds();
            bar();
            if (layer == 0) dma_p2n(4, 6);
            f32x4 v[4], t[3][4];
#pragma unroll
            for (int d = 1; d < 4; ++d) {
                LAS char* src = L + ((w - d) & 3) * 8192 + lane * 16 + (d < 3 ? 3 * REG + (d - 1) * 4096 : LDS_M);
                asm volatile("" : "+v"(src));
#pragma unroll
                for (int b = 0; b < 4; ++b) t[d - 1][b] = *(LAS f32x4*)(src + b * 1024);
            }
            SCHED();
#pragma unroll
            for (int b = 0; b < 4; ++b) v[b] = ((acc[b][0] + t[0][b]) + t[1][b]) + t[2][b];
            if (layer == 0) {
                finish_strip(v, false, WS_X2, WS_ST + 2 * ST_BYTES);
                wait_vm<0>();
                bar();
                publish(rw, 5, blk, tag + 5, w);
                a_tK = NOW_();
                dma_p2n(6, 8);
            } else {
                finish_strip(v, true, 0, 0);
                wait_vm<0>();
                b_tK = NOW_();
            }
        }
        if (layer == 1) break;
        phase1(std::false_type{});
    }
    if (wid == 0 && (blk == 0 || blk == 101 || blk == 202 || blk == 255) && lane_id() == 0) {
        printf("blk %d L0: x %u P1end %u P1pub %u P2ok %u P2end %u partpub %u partok %u x1pub %u P3ok %u P3end %u P4end %u x2pub %u\n", blk,
               a_t1 - a_t0, a_tA - a_t0, a_tB - a_t0, a_tC - a_t0, a_tD - a_t0, a_tI - a_t0, a_tJ - a_t0, a_tE - a_t0, a_tF - a_t0, a_tG - a_t0, a_tH - a_t0, a_tK - a_t0);
        printf("blk %d L1: P1ok %u P1end %u P1pub %u P2ok %u P2end %u partpub %u partok %u x1pub %u P3ok %u P3end %u P4end %u end %u t0 %u\n", blk,
               b_t1 - a_t0, b_tA - a_t0, b_tB - a_t0, b_tC - a_t0, b_tD - a_t0, b_tI - a_t0, b_tJ - a_t0, b_tE - a_t0, b_tF - a_t0, b_tG - a_t0, b_tH - a_t0, b_tK - a_t0, a_t0);
    }
}

}

extern "C" void kernel_launch(void* const* d_in, const int* in_sizes, int n_in,
                              void* d_out, int out_size, void* d_ws, size_t ws_size,
                              hipStream_t stream) {
    (void)in_sizes; (void)n_in; (void)out_size; (void)ws_size;
    (void)hipFuncSetAttribute((const void*)fn::fnet_glu_kernel, hipFuncAttributeMaxDynamicSharedMemorySize, fn::LDS_BYTES);
    fn::fnet_glu_kernel<<<256, 256, fn::LDS_BYTES, stream>>>((const char*)d_in[0], (const char*)d_in[1], (const char*)d_in[2],
        (const char*)d_in[3], (const char*)d_in[4], (const char*)d_in[6], (const char*)d_in[7], (const char*)d_in[8],
        (char*)d_out, (char*)d_ws);
}
```

```cpp
#include <hip/hip_runtime.h>
#include <hip/hip_bf16.h>
#include <stdint.h>
#include <type_traits>
#include <utility>

#define DI __device__ __forceinline__
#define LAS __attribute__((address_space(3)))
#define GAS __attribute__((address_space(1)))
typedef short bf16x8 __attribute__((ext_vector_type(8)));
typedef short s16x4 __attribute__((ext_vector_type(4)));
typedef float f32x4 __attribute__((ext_vector_type(4)));
typedef float f32x2 __attribute__((ext_vector_type(2)));
typedef float f32x16 __attribute__((ext_vector_type(16)));
typedef unsigned u32x4 __attribute__((ext_vector_type(4)));
typedef unsigned u32x2 __attribute__((ext_vector_type(2)));
typedef __bf16 bf16x2 __attribute__((ext_vector_type(2)));
typedef unsigned long long u64;
typedef __amdgpu_buffer_rsrc_t rsrc_t;

namespace fn {

constexpr int S = 2048, D = 512, F = 1792;
constexpr unsigned MB = 1u << 20;
constexpr unsigned WS_FLAGS = 4096;
constexpr unsigned WS_ABT = 1 * MB;
constexpr unsigned WS_X1 = 9 * MB;
constexpr unsigned WS_X2 = 13 * MB;
constexpr unsigned WS_H = 15 * MB;
constexpr unsigned WS_ST = 29 * MB;
constexpr unsigned WS_PART = 32 * MB;
constexpr unsigned ST_BYTES = 128 * 1024;

constexpr unsigned REG = 32768;
constexpr unsigned LDS_M = 4 * REG;
constexpr unsigned T_RES = 4096;
constexpr unsigned T_STAT = 6144;
constexpr unsigned T_CS1 = LDS_M + 6400;
constexpr unsigned T_CS3 = LDS_M + 8192 + 6400;
constexpr unsigned T_CS3P = LDS_M + 16384 + 6400;
constexpr unsigned T_DUMMY = LDS_M + 24576 + 6400;
constexpr unsigned LDS_BYTES = 163840;
constexpr unsigned SPIN_LIMIT = 200000;
constexpr float EPS = 1e-5f;

DI rsrc_t mk(const void* p, unsigned bytes) { return __builtin_amdgcn_make_buffer_rsrc((void*)p, 0, bytes, 0x00020000); }
DI void dma(unsigned lds, int LI, unsigned voff, rsrc_t r, unsigned sb, int SI) {
    unsigned t;
    asm volatile("s_add_u32 m0, %1, %5\n\ts_add_u32 %0, %4, %6\n\tbuffer_load_dwordx4 %2, %3, %0 offen lds"
                 : "=&s"(t) : "s"(lds), "v"(voff), "s"(r), "s"(sb), "i"(LI), "i"(SI) : "m0", "scc", "memory");
}
DI void aload(bf16x8& d, unsigned voff, rsrc_t r, unsigned sb, int SI) {
    asm volatile("buffer_load_dwordx4 %0, %1, %2, %3 offen offset:%4 sc1"
                 : "=v"(d) : "v"(voff), "s"(r), "s"(sb + (SI & ~4095)), "i"(SI & 4095) : "memory");
}
DI void aload2(u32x2& d, unsigned voff, rsrc_t r, unsigned sb) {
    asm volatile("buffer_load_dwordx2 %0, %1, %2, %3 offen sc1" : "=v"(d) : "v"(voff), "s"(r), "s"(sb) : "memory");
}
template <int... Is, class Fn> DI void sfor_impl(std::integer_sequence<int, Is...>, Fn&& f) { (f(std::integral_constant<int, Is>{}), ...); }
template <int N, class Fn> DI void sfor(Fn&& f) { sfor_impl(std::make_integer_sequence<int, N>{}, f); }
#define CI(x) decltype(x)::value
DI int lane_id() { int l; asm volatile("v_mbcnt_lo_u32_b32 %0, -1, 0\n\tv_mbcnt_hi_u32_b32 %0, -1, %0" : "=v"(l)); return l; }
DI int opq_s(int v) { asm volatile("" : "+s"(v)); return v; }
template <int N> DI void wait_vm() { asm volatile("s_waitcnt vmcnt(%0)" :: "i"(N) : "memory"); }
template <int N> DI void wait2(bf16x8& a, bf16x8& b) { asm volatile("s_waitcnt vmcnt(%2)" : "+v"(a), "+v"(b) : "i"(N) : "memory"); }
template <int N> DI void wait4(bf16x8& a, bf16x8& b, bf16x8& c, bf16x8& d) {
    asm volatile("s_waitcnt vmcnt(%4)" : "+v"(a), "+v"(b), "+v"(c), "+v"(d) : "i"(N) : "memory");
}
DI void wait_lds() { asm volatile("s_waitcnt lgkmcnt(0)" ::: "memory"); }
DI void bar() { asm volatile("" ::: "memory"); __builtin_amdgcn_s_barrier(); asm volatile("" ::: "memory"); }
#define SCHED() __builtin_amdgcn_sched_barrier(0)
DI f32x4 mfma(bf16x8 a, bf16x8 b, f32x4 c) { return __builtin_amdgcn_mfma_f32_16x16x32_bf16(a, b, c, 0, 0, 0); }
DI f32x16 mfma32(bf16x8 a, bf16x8 b, f32x16 c) { return __builtin_amdgcn_mfma_f32_32x32x16_bf16(a, b, c, 0, 0, 0); }
DI unsigned pk(float lo, float hi) { f32x2 v = {lo, hi}; return __builtin_bit_cast(unsigned, __builtin_convertvector(v, bf16x2)); }
DI float lo16(unsigned u) { return __uint_as_float(u << 16); }
DI float hi16(unsigned u) { return __uint_as_float(u & 0xffff0000u); }
DI float sum16(float v) { auto r = __builtin_amdgcn_permlane16_swap(__float_as_uint(v), __float_as_uint(v), false, false); return __uint_as_float(r[0]) + __uint_as_float(r[1]); }
DI float sum32(float v) { auto r = __builtin_amdgcn_permlane32_swap(__float_as_uint(v), __float_as_uint(v), false, false); return __uint_as_float(r[0]) + __uint_as_float(r[1]); }
DI float sumq(float v) { return sum32(sum16(v)); }
DI u32x4 gather8(unsigned x0, unsigned x1, unsigned y0, unsigned y1) {
    auto a = __builtin_amdgcn_permlane32_swap(x0, y0, false, false);
    auto b = __builtin_amdgcn_permlane32_swap(x1, y1, false, false);
    auto c = __builtin_amdgcn_permlane16_swap(a[0], a[1], false, false);
    auto d = __builtin_amdgcn_permlane16_swap(b[0], b[1], false, false);
    return (u32x4){c[0], d[0], c[1], d[1]};
}
DI void store16(u32x4 v, rsrc_t r, unsigned voff) { __builtin_amdgcn_raw_buffer_store_b128(v, r, voff, 0, 16); }
DI bf16x8 ldsA(LAS char* L, unsigned off) { return *(LAS bf16x8*)(L + off); }
DI bf16x8 ldsT(LAS char* L, unsigned off) {
    s16x4 lo = __builtin_amdgcn_ds_read_tr16_b64_v4i16((LAS s16x4*)(L + off));
    s16x4 hi = __builtin_amdgcn_ds_read_tr16_b64_v4i16((LAS s16x4*)(L + off + 512));
    return __builtin_shufflevector(lo, hi, 0, 1, 2, 3, 4, 5, 6, 7);
}
DI int blk_p24(int rt, int ct) { return 8 * (4 * ((rt >> 1) & 7) + (ct >> 1)) + 4 * (rt >> 4) + 2 * (rt & 1) + (ct & 1); }
DI int blk_p1(int rt1, int et) { return 8 * (8 * (rt1 & 3) + (et & 7)) + 2 * (rt1 >> 2) + (et >> 3); }
DI int blk_p3(int RT, int CT) { return 8 * (8 * (RT & 3) + (CT & 7)) + 2 * (CT >> 3) + (RT >> 2); }

DI void poll8(rsrc_t rw, int phase, int prod, u64 tag, bool& dead) {
    const unsigned voff = (unsigned)prod * 256u, sb = WS_FLAGS + (unsigned)phase * 65536u;
    if (!dead) {
        unsigned spins = 0;
        for (;;) {
            u64 v;
            asm volatile("buffer_load_dwordx2 %0, %1, %2, %3 offen sc1\n\ts_waitcnt vmcnt(0)" : "=v"(v) : "v"(voff), "s"(rw), "s"(sb) : "memory");
            if (__all(v == tag)) break;
            if (++spins > SPIN_LIMIT) { dead = true; break; }
        }
    }
    asm volatile("" ::: "memory");
}
DI void publish(rsrc_t rw, int phase, int blk, u64 tag, int w) {
    if (w == 0)
        __builtin_amdgcn_raw_buffer_store_b64((u32x2){(unsigned)tag, (unsigned)(tag >> 32)}, rw, WS_FLAGS + (unsigned)(phase * 256 + blk) * 256u, 0, 16);
}


constexpr int P1_LA = 12;
constexpr int p1_issued(int ks) { return 2 * P1_LA + 2 * (ks < 16 - P1_LA ? ks : 16 - P1_LA); }
constexpr int p1_wait(int ks) { return p1_issued(ks) - 1 - (ks < P1_LA ? 2 * ks + 1 : p1_issued(ks - P1_LA + 1) - 1); }
constexpr int P2_LA = 22;
constexpr int p2_dma(int v) { return (v >= 8 && v <= 39) || (v >= 43 && v <= 50) ? 1 : 0; }
constexpr int p2_ops(int v) { return p2_dma(v) + (v + P2_LA < 64 ? 1 : 0); }
constexpr int p2_issued(int v) { int n = P2_LA; for (int j = 0; j < v; ++j) n += p2_ops(j); return n; }
constexpr int p2_wait(int v) {
    const int pa = v < P2_LA ? v : p2_issued(v - P2_LA + 1) - 1;
    const int c = (v >> 2) + 1;
    const int pd = (v & 3) == 2 && c >= 8 && c < 16 ? p2_issued(4 * (c - 6) + 3) : -1;
    return p2_issued(v) - 1 - (pa > pd ? pa : pd);
}
constexpr int P3_LA = 3;
constexpr int p3_ops(int g) { return (g + P3_LA < 16 ? 4 : 0) + (g == 8 ? 8 : 0); }
constexpr int p3_issued(int g) { int n = 4 * P3_LA; for (int j = 0; j < g; ++j) n += p3_ops(j); return n; }
constexpr int p3_wait(int g) { return p3_issued(g) - 1 - (g < P3_LA ? 4 * g + 3 : p3_issued(g - P3_LA) + 3); }
constexpr int p4_ops(int u) { return (u == 1 ? 1 : 0) + (u >= 1 && u <= 6 ? 4 : 0) + (u <= 8 ? 4 : 0); }
constexpr int p4_issued(int u) { int n = 20; for (int j = 0; j < u; ++j) n += p4_ops(j); return n; }
constexpr int p4_wait(int u) {
    int p = u < 5 ? 4 * u + 3 : p4_issued(u - 4) - 1;
    if (u == 3 && p < p4_issued(1)) p = p4_issued(1);
    if ((u & 1) && u < 13) {
        const int c = (u + 1) >> 1;
        if (c >= 4 && p4_issued(2 * c - 6) + 6 > p) p = p4_issued(2 * c - 6) + 6;
    }
    return p4_issued(u) - 1 - p;
}
constexpr int p4_row(int u, int qq) { return u < 8 ? 56 * u + 8 * qq : 56 * ((4 * (u - 8) + qq) / 3) + 32 + 8 * ((4 * (u - 8) + qq) % 3); }

template <int N> DI void combine_stats(const u32x2 (&a)[N], const u32x2 (&b)[N], float (&mean)[N], float (&var)[N]) {
    float s[N], t[N], inv = 1.0f / 512.0f;
    asm volatile("" : "+s"(inv));
#pragma unroll
    for (int i = 0; i < N; ++i) { s[i] = __uint_as_float(a[i][0]) + __uint_as_float(b[i][0]); t[i] = __uint_as_float(a[i][1]) + __uint_as_float(b[i][1]); }
#pragma unroll
    for (int i = 0; i < N; ++i) { s[i] = sum16(s[i]); t[i] = sum16(t[i]); }
#pragma unroll
    for (int i = 0; i < N; ++i) { s[i] = sum32(s[i]); t[i] = sum32(t[i]); }
#pragma unroll
    for (int i = 0; i < N; ++i) { mean[i] = s[i] * inv; var[i] = fmaxf(t[i] * inv - mean[i] * mean[i], 0.f); }
}

__global__ __launch_bounds__(256, 2) void fnet_glu_kernel(const char* __restrict__ x, const char* __restrict__ wsre, const char* __restrict__ wsim,
        const char* __restrict__ wdre, const char* __restrict__ wdim_, const char* __restrict__ wg, const char* __restrict__ wu,
        const char* __restrict__ wdn, char* __restrict__ out, char* __restrict__ ws) {
    extern __shared__ __attribute__((aligned(16))) char smem[];
    LAS char* L = (LAS char*)smem;
    int wid;
    asm volatile("s_nop 1\n\tv_readfirstlane_b32 %0, %1\n\ts_nop 4" : "=s"(wid) : "v"(threadIdx.x >> 6));
    const int blk = blockIdx.x, xcd = blk & 7, idx = blk >> 3;

    const unsigned ldsb = (unsigned)(size_t)L;
    const rsrc_t rw = mk(ws, 48 * MB);
    const int T2 = 8 * (xcd >> 2) + (idx >> 2), E2 = idx & 3, j2 = xcd & 3;
    const int rt = 2 * T2 + (j2 >> 1), ct = 2 * E2 + (j2 & 1);
    const int rt1 = 4 * (xcd >> 1) + (idx >> 3), et = 8 * (xcd & 1) + (idx & 7);
    const int RT = 4 * (xcd & 1) + (idx >> 3), CT = 8 * (xcd >> 1) + (idx & 7);
    {
        u64 pc;
        int len;
        asm volatile("s_getpc_b64 %0\n1:\n\ts_mov_b32 %1, fnet_glu_code_end-1b" : "=s"(pc), "=s"(len));
        const rsrc_t rc = mk((const void*)pc, len > 0 ? (unsigned)len : 0u);
        const unsigned o = (unsigned)((idx * 4 + wid) & 7) * 8192u + lane_id() * 128u, l = ldsb + T_DUMMY + 768;
        asm volatile("s_mov_b32 m0, %0\n\ts_nop 0\n\tbuffer_load_dword %1, %2, 0 offen lds" :: "s"(l), "v"(o), "s"(rc) : "m0", "memory");
    }

    auto p1_rsrc = [&](unsigned bytes) { return mk(((et >> 3) ? wdim_ : wdre) + (size_t)(et & 7) * 64 * 1024, bytes); };
    auto dma_p1 = [&](int j, int p, rsrc_t r, unsigned v0, unsigned lw, unsigned sb) {
        dma(lw, (32 * j + p) * 1024, v0 ^ (16 * p), r, sb, (32 * j + p) * 1024);
    };
    auto p3_voff = [&](int ln, int up, int odd) -> unsigned {
        const unsigned c = (ln & 7) ^ ((2 * ((ln >> 4) & 1) + 4 * odd) ^ up);
        return (ln >> 3) * 3584 + 16 * (c > 6 ? 6 : c);
    };
    auto dma_p3 = [&](int j, int p, unsigned v, unsigned lw, unsigned sb) {
        dma(lw, j * REG + p * 1024, v, mk((j >> 1) ? wu : wg, D * F * 2), sb, (256 * (j & 1) + 8 * p) * 3584);
    };
    auto dma_p2n = [&](int c0, int c1) {
        const int w = opq_s(wid), ln = lane_id();
        const rsrc_t r = mk(((j2 >> 1) ? wsim : wsre) + ((size_t)128 * T2 * S + 1024 * (j2 & 1)) * 2, (128 * S - 1024 * (j2 & 1)) * 2);
        const unsigned vE = (ln >> 3) * 4096 + (((ln & 7) ^ (ln >> 4)) * 16), vO = vE ^ 64;
#pragma unroll
        for (int c = c0; c < c1; ++c)
#pragma unroll
            for (int p = 0; p < 4; ++p) dma(ldsb + w * 4096, (c & 7) * 16384 + p * 1024, (p & 1) ? vO : vE, r, 32 * w * 4096, p * 32768 + c * 128);
    };
    auto dma_p1_all = [&]() {
        const int w = opq_s(wid), ln = lane_id();
        const rsrc_t r = p1_rsrc(65536);
        const unsigned v0 = (unsigned)(ln * 16) ^ (128 * (w & 1)), lw = ldsb + w * 8192;
#pragma unroll
        for (int j = 0; j < 2; ++j)
#pragma unroll
            for (int p = 0; p < 8; ++p) dma_p1(j, p, r, v0, lw, w * 8192);
    };
    auto warm_ffn = [&]() {
        const int w = opq_s(wid), ln = lane_id();
        const unsigned o = blk * 7168u + (ln < 56 ? ln : 55) * 128u, l = ldsb + T_DUMMY + w * 256;
        auto touch = [&](rsrc_t r) {
            asm volatile("s_mov_b32 m0, %0\n\ts_nop 0\n\tbuffer_load_dword %1, %2, 0 offen lds" :: "s"(l), "v"(o), "s"(r) : "m0", "memory");
        };
        if (w == 0) touch(mk(wg, D * F * 2));
        if (w == 1) touch(mk(wu, D * F * 2));
        if (w == 2) touch(mk(wdn, D * F * 2));
    };
    dma_p1_all();

    u32x2 ev;
    aload2(ev, 0u, rw, 0u);
    u64 epoch = 0;
    bool dead = false;
    const bf16x8 ones = {0x3F80, 0x3F80, 0x3F80, 0x3F80, 0x3F80, 0x3F80, 0x3F80, 0x3F80};

        auto phase1 = [&](auto first_tag) {
            constexpr bool FIRST = decltype(first_tag)::value;
            constexpr int layer = FIRST ? 0 : 1;
            const int w = opq_s(wid), lane = lane_id(), i16 = lane & 15, q = lane >> 4;
            const int sbg = 8 * rt1 + 2 * w;
            bf16x8 xa[2][16];
            u32x2 sa[2], sb_[2];
            f32x4 acc[2][4];
#pragma unroll
            for (int s = 0; s < 2; ++s)
#pragma unroll
                for (int e = 0; e < 4; ++e) acc[s][e] = (f32x4){0.f, 0.f, 0.f, 0.f};
            unsigned wb[4];
#pragma unroll
            for (int k = 0; k < 4; ++k) wb[k] = i16 * 1024 + (((4 * k + q) ^ i16) * 16);
            LAS f32x2* tst = (LAS f32x2*)(L + LDS_M + w * 8192 + T_STAT);
            const float sgn = __int_as_float(opq_s((et >> 3) ? (int)0xbf800000 : 0x3f800000));
            f32x4 aS[2], aQ[2], aC = {0.f, 0.f, 0.f, 0.f};
#pragma unroll
            for (int s = 0; s < 2; ++s) { aS[s] = (f32x4){0.f, 0.f, 0.f, 0.f}; aQ[s] = aS[s]; }
            const unsigned wown = w * 16384;
            const unsigned vo = lane * 16, sbase = WS_X2 + sbg * 16384;
            if constexpr (FIRST) {
                const rsrc_t rx = mk(x, S * D * 2);
                bf16x8 xr[32];
#pragma unroll
                for (int r = 0; r < 32; ++r) aload(xr[r], vo, rx, sbg * 16384, r * 1024);
                LAS char* T0 = L + 2 * REG + w * 8192;
                sfor<2>([&](auto SB) {
                    constexpr int s = CI(SB);
                    sfor<4>([&](auto G) {
                        constexpr int g = CI(G), r0 = 16 * s + 4 * g;
                        wait4<28 - r0>(xr[r0], xr[r0 + 1], xr[r0 + 2], xr[r0 + 3]);
#pragma unroll
                        for (int r = 0; r < 4; ++r)
                            *(LAS bf16x8*)(T0 + ((4 * g + r) >> 3) * REG + ((4 * g + r) & 7) * 1024 + ((lane * 16) ^ ((4 * g + r) * 16))) = xr[r0 + r];
                    });
#pragma unroll
                    for (int ks = 0; ks < 16; ++ks)
                        xa[s][ks] = *(LAS bf16x8*)(T0 + (i16 >> 3) * REG + (i16 & 7) * 1024 + (((4 * ks + q) ^ i16) * 16));
                    wait_lds();
                });
                asm volatile("" : "+v"(ev));
                epoch = ((u64)(unsigned)__builtin_amdgcn_readfirstlane((int)ev[1]) << 32) | (unsigned)__builtin_amdgcn_readfirstlane((int)ev[0]);
                wait_lds();
                bar();
                dma_p2n(4, 8);
                warm_ffn();
            } else {
                poll8(rw, 5, blk_p24(2 * rt1 + (w >> 1), lane & 7), epoch + 6, dead);
#pragma unroll
                for (int s = 0; s < 2; ++s) {
                    const unsigned so = WS_ST + 2 * ST_BYTES + (sbg + s) * 1024;
                    aload2(sa[s], (2 * q * 16 + i16) * 8, rw, so);
                    aload2(sb_[s], ((2 * q + 1) * 16 + i16) * 8, rw, so);
                }
#pragma unroll
                for (int ks = 0; ks < P1_LA; ++ks)
#pragma unroll
                    for (int s = 0; s < 2; ++s) aload(xa[s][ks], vo, rw, sbase, s * 16384 + ks * 1024);
                wait2<p1_wait(0)>(xa[0][0], xa[1][0]);
                asm volatile("" : "+v"(sa[0]), "+v"(sa[1]), "+v"(sb_[0]), "+v"(sb_[1]));
            }
            bf16x8 wf[2][4], wo;
#pragma unroll
            for (int e = 0; e < 4; ++e) wf[0][e] = ldsA(L, wb[0] + e * 16384);
            sfor<16>([&](auto KS) {
                constexpr int ks = CI(KS);
                if constexpr (!FIRST && ks > 0) wait2<p1_wait(ks)>(xa[0][ks], xa[1][ks]);
                if constexpr (FIRST) wo = ldsA(L, wb[ks & 3] + wown + 256 * (ks >> 2));
                sfor<4>([&](auto E) {
                    constexpr int e = CI(E);
                    acc[0][e] = mfma(xa[0][ks], wf[ks & 1][e], acc[0][e]);
                    acc[1][e] = mfma(xa[1][ks], wf[ks & 1][e], acc[1][e]);
                    SCHED();
                    if constexpr (ks < 15) wf[(ks + 1) & 1][e] = ldsA(L, wb[(ks + 1) & 3] + e * 16384 + 256 * ((ks + 1) >> 2));
                    if constexpr (!FIRST && e >= 2 && ks + P1_LA < 16) aload(xa[e - 2][ks + P1_LA], vo, rw, sbase, (e - 2) * 16384 + (ks + P1_LA) * 1024);
                    SCHED();
                });
                if constexpr (!FIRST && ks == 15) {
                    float mean[2], var[2];
                    combine_stats<2>(sa, sb_, mean, var);
#pragma unroll
                    for (int s = 0; s < 2; ++s) tst[16 * s + i16] = (f32x2){mean[s], sgn * __builtin_amdgcn_rsqf(var[s] + EPS)};
                    SCHED();
                }
                if constexpr (FIRST) {
                    aC = mfma(ones, wo, aC);
#pragma unroll
                    for (int s = 0; s < 2; ++s) { aS[s] = mfma(ones, xa[s][ks], aS[s]); aQ[s] = mfma(xa[s][ks], xa[s][ks], aQ[s]); }
                    SCHED();
                }
            });
            if constexpr (FIRST) {
                ((LAS float*)(L + T_CS1))[16 * w + i16] = aC[0];
#pragma unroll
                for (int s = 0; s < 2; ++s) {
                    const int r3 = i16 & 3;
                    float dq = r3 == 0 ? aQ[s][0] : r3 == 1 ? aQ[s][1] : r3 == 2 ? aQ[s][2] : aQ[s][3];
                    dq = (q == (i16 >> 2)) ? dq : 0.f;
                    const float mean = aS[s][0] * (1.0f / 512.0f);
                    const float var = fmaxf(sumq(dq) * (1.0f / 512.0f) - mean * mean, 0.f);
                    tst[16 * s + i16] = (f32x2){mean, sgn * __builtin_amdgcn_rsqf(var + EPS)};
                }
            }
            wait_lds();
            bar();
            dma_p2n(0, 2);
            f32x4 st0[2], st1[2];
#pragma unroll
            for (int s = 0; s < 2; ++s) {
                const f32x4 a = *(LAS f32x4*)(tst + 16 * s + 4 * q), b = *(LAS f32x4*)(tst + 16 * s + 4 * q + 2);
                st0[s] = (f32x4){a[0], a[2], b[0], b[2]}; st1[s] = (f32x4){a[1], a[3], b[1], b[3]};
            }
#pragma unroll
            for (int E = 0; E < 2; ++E) {
                const float cs0 = ((LAS float*)(L + T_CS1))[32 * E + i16], cs1 = ((LAS float*)(L + T_CS1))[32 * E + 16 + i16];
#pragma unroll
                for (int s = 0; s < 2; ++s) {
                    f32x4 v0, v1;
#pragma unroll
                    for (int r = 0; r < 4; ++r) {
                        v0[r] = (acc[s][2 * E][r] - st0[s][r] * cs0) * st1[s][r];
                        v1[r] = (acc[s][2 * E + 1][r] - st0[s][r] * cs1) * st1[s][r];
                    }
                    auto a = __builtin_amdgcn_permlane16_swap(pk(v0[0], v0[1]), pk(v1[0], v1[1]), false, false);
                    auto b = __builtin_amdgcn_permlane16_swap(pk(v0[2], v0[3]), pk(v1[2], v1[3]), false, false);
                    store16((u32x4){a[0], b[0], a[1], b[1]}, rw,
                            WS_ABT + layer * 4 * MB + (et >> 3) * 2 * MB + ((2 * (et & 7) + E) * 128 + sbg + s) * 1024 + lane * 16);
                }
            }
        };


    phase1(std::true_type{});
    for (int layer = 0; layer < 2; ++layer) {
        const u64 tag = epoch + 1 + 6 * layer;
        auto finish_strip = [&](f32x4 (&v)[4], bool last, unsigned xo, unsigned sto) {
            const int w = opq_s(wid), lane = lane_id(), i16 = lane & 15, q = lane >> 4;
            float sum = 0.f, sq = 0.f;
            u32x2 res[4];
            {
                const u32x4 r0 = *(LAS u32x4*)(L + LDS_M + w * 8192 + T_RES + lane * 16), r1 = *(LAS u32x4*)(L + LDS_M + w * 8192 + T_RES + 1024 + lane * 16);
                res[0] = (u32x2){r0[0], r0[1]}; res[1] = (u32x2){r0[2], r0[3]}; res[2] = (u32x2){r1[0], r1[1]}; res[3] = (u32x2){r1[2], r1[3]};
            }
#pragma unroll
            for (int b = 0; b < 4; ++b) {
                res[b][0] = pk(lo16(res[b][0]) + v[b][0], hi16(res[b][0]) + v[b][1]);
                res[b][1] = pk(lo16(res[b][1]) + v[b][2], hi16(res[b][1]) + v[b][3]);
                const float x0 = lo16(res[b][0]), x1 = hi16(res[b][0]), x2 = lo16(res[b][1]), x3 = hi16(res[b][1]);
                sum += (x0 + x1) + (x2 + x3);
                sq += (x0 * x0 + x1 * x1) + (x2 * x2 + x3 * x3);
            }
            const u32x4 o0 = gather8(res[0][0], res[0][1], res[1][0], res[1][1]);
            const u32x4 o1 = gather8(res[2][0], res[2][1], res[3][0], res[3][1]);
            if (last) {
                const rsrc_t rxo = mk(out, S * D * 2);
                const unsigned a = (64 * rt + 16 * w + i16) * 1024 + (64 * ct + 8 * q) * 2;
                __builtin_amdgcn_raw_buffer_store_b128(o0, rxo, a, 0, 0);
                __builtin_amdgcn_raw_buffer_store_b128(o1, rxo, a + 64, 0, 0);
            } else {
                const unsigned a = xo + ((4 * rt + w) * 16 + 2 * ct) * 1024 + lane * 16;
                store16(o0, rw, a); store16(o1, rw, a + 1024);
                *(LAS u32x4*)(L + LDS_M + w * 8192 + T_RES + lane * 16) = (u32x4){res[0][0], res[0][1], res[1][0], res[1][1]};
                *(LAS u32x4*)(L + LDS_M + w * 8192 + T_RES + 1024 + lane * 16) = (u32x4){res[2][0], res[2][1], res[3][0], res[3][1]};
                sum = sum16(sum); sq = sum16(sq);
                sum = sum32(sum); sq = sum32(sq);
                __builtin_amdgcn_raw_buffer_store_b64((u32x2){__float_as_uint(sum), __float_as_uint(sq)}, rw, sto + (((4 * rt + w) * 8 + ct) * 16 + i16) * 8, 0, 16);
            }
        };

        {
            const int w = opq_s(wid), lane = lane_id(), i16 = lane & 15, q = lane >> 4;
            const int part = j2 >> 1, half = j2 & 1, r32 = lane & 31, hh = lane >> 5;
            const rsrc_t rws = mk((part ? wsim : wsre) + ((size_t)128 * T2 * S + 1024 * half) * 2, (128 * S - 1024 * half) * 2);
            const unsigned vsE = (lane >> 3) * 4096 + (((lane & 7) ^ (lane >> 4)) * 16), vsO = vsE ^ 64;
            const int w4_ = opq_s(wid);
            const unsigned lw4 = ldsb + w4_ * 4096, wsb = 32 * w4_ * 4096;
            auto dma_p2 = [&](int c, int p) { dma(lw4, (c & 7) * 16384 + p * 1024, (p & 1) ? vsO : vsE, rws, wsb, p * 32768 + c * 128); };
            wait_vm<0>();
            bar();
            publish(rw, 6 * layer + 0, blk, tag, w);
            dma_p2n(2, 4);
            poll8(rw, 6 * layer + 0, blk_p1(8 * half + (lane & 7), 8 * part + 2 * E2 + (w >> 1)), tag, dead);
            const unsigned so0 = WS_ABT + layer * 4 * MB + part * 2 * MB + ((4 * E2 + w) * 128 + 64 * half) * 1024, vo = lane * 16;
            bf16x8 act[P2_LA + 2];
            auto load_act = [&](int v) { aload(act[v % (P2_LA + 2)], vo, rw, so0, v * 1024); };
#pragma unroll
            for (int v = 0; v < P2_LA; ++v) load_act(v);
            f32x16 acc[4];
#pragma unroll
            for (int t = 0; t < 4; ++t)
#pragma unroll
                for (int r = 0; r < 16; ++r) acc[t][r] = 0.f;
            unsigned rb[2][4];
#pragma unroll
            for (int k = 0; k < 4; ++k) { rb[0][k] = r32 * 128 + (((2 * k + hh) ^ ((r32 >> 1) & 7)) * 16); rb[1][k] = rb[0][k] + 65536; }
            auto ws_frag = [&](int n) {
                const int v = n >> 2, t = n & 3, c = v >> 2;
                return ldsA(L, rb[(c & 7) >> 2][v & 3] + (c & 3) * 16384 + t * 4096);
            };
            const int ws_ = opq_s(wid);
            const unsigned lw = ldsb + ws_ * 8192, g_sb = 64 * ws_ * 3584 + 112 * CT;
            unsigned gg0 = 0, gg1 = 0;
            bf16x8 wf[8];
#pragma unroll
            for (int n = 0; n < 6; ++n) wf[n] = ws_frag(n);
            sfor<64>([&](auto V) {
                constexpr int v = CI(V);
                asm volatile("s_waitcnt vmcnt(%1)" : "+v"(act[v % (P2_LA + 2)]) : "i"(p2_wait(v)) : "memory");
                if constexpr (v == 43) { const int l2 = lane_id(); gg0 = p3_voff(l2, 0, 0); gg1 = p3_voff(l2, 0, 1); }
                sfor<4>([&](auto TB) {
                    constexpr int t = CI(TB), n = 4 * v + t;
                    acc[t] = mfma32(act[v % (P2_LA + 2)], wf[n & 7], acc[t]);
                    SCHED();
                    if constexpr ((v & 3) == 2 && t == 2 && v < 60) bar();
                    if constexpr (n + 6 < 256) wf[(n + 6) & 7] = ws_frag(n + 6);
                    if constexpr (t == 1 && p2_dma(v)) {
                        if constexpr (v <= 39) dma_p2((v >> 2) + 6, v & 3);
                        else dma_p3(0, v - 43, ((v - 43) & 1) ? gg1 : gg0, lw, g_sb);
                    }
                    if constexpr (t == 3 && v + P2_LA < 64) load_act(v + P2_LA);
                    SCHED();
                });
            });
            const unsigned pgrp = WS_PART + layer * 8 * MB + (T2 * 4 + E2) * 131072;
            const int lx = lane_id(), i16x = lx & 15, tup = (lx >> 4) & 1, hx = lx >> 5;
#pragma unroll
            for (int a = 0; a < 2; ++a) {
                const int owner = 2 * a + (w >> 1);
                if (owner != j2) {
                    const unsigned po = pgrp + owner * 32768 + j2 * 8192 + (w & 1) * 1024 + tup * 2048 + (i16x + 16 * hx) * 16;
#pragma unroll
                    for (int u = 0; u < 2; ++u)
#pragma unroll
                        for (int qh = 0; qh < 2; ++qh) {
                            const f32x16& c = acc[2 * a + u];
                            store16((u32x4){pk(c[4 * qh], c[4 * qh + 1]), pk(c[4 * qh + 2], c[4 * qh + 3]),
                                            pk(c[8 + 4 * qh], c[8 + 4 * qh + 1]), pk(c[8 + 4 * qh + 2], c[8 + 4 * qh + 3])}, rw, po + u * 4096 + qh * 512);
                        }
                }
            }
#pragma unroll
            for (int a = 0; a < 2; ++a) {
                if (2 * a + (w >> 1) == j2) {
#pragma unroll
                    for (int u = 0; u < 2; ++u)
#pragma unroll
                        for (int g = 0; g < 4; ++g) {
                            const f32x16& c = acc[2 * a + u];
                            *(LAS f32x4*)(L + LDS_M + (2 * u + tup) * 8192 + (2 * (w & 1) + (g >> 1)) * 1024 + (i16x + 16 * (2 * (g & 1) + hx)) * 16) =
                                (f32x4){c[4 * g], c[4 * g + 1], c[4 * g + 2], c[4 * g + 3]};
                        }
                }
            }
            wait_vm<0>();
            wait_lds();
            bar();
            publish(rw, 6 * layer + 1, blk, tag + 1, w);
            const unsigned gu0 = p3_voff(lx, 1, 0), gu1 = p3_voff(lx, 1, 1), vx = lx * 16;
#pragma unroll
            for (int n = 0; n < 8; ++n) dma_p3(1, n, (n & 1) ? gg1 : gg0, lw, g_sb);
#pragma unroll
            for (int n = 0; n < 3; ++n) dma_p3(2, n, (n & 1) ? gu1 : gu0, lw, g_sb);
            if (layer == 0) {
                u32x2 res0[4];
                const rsrc_t rx = mk(x, S * D * 2);
                const unsigned vr = (64 * rt + 16 * w + i16x) * 1024 + (64 * ct + 4 * (lx >> 4)) * 2;
#pragma unroll
                for (int b = 0; b < 4; ++b) aload2(res0[b], vr + b * 32, rx, 0u);
                asm volatile("s_waitcnt vmcnt(0)" : "+v"(res0[0]), "+v"(res0[1]), "+v"(res0[2]), "+v"(res0[3]) :: "memory");
                *(LAS u32x4*)(L + LDS_M + w * 8192 + T_RES + vx) = (u32x4){res0[0][0], res0[0][1], res0[1][0], res0[1][1]};
                *(LAS u32x4*)(L + LDS_M + w * 8192 + T_RES + 1024 + vx) = (u32x4){res0[2][0], res0[2][1], res0[3][0], res0[3][1]};
            }
            poll8(rw, 6 * layer + 1, (blk & ~3) | (lx & 3), tag + 1, dead);
            bf16x8 pv[3][2];
#pragma unroll
            for (int d = 1; d < 4; ++d)
#pragma unroll
                for (int h = 0; h < 2; ++h) aload(pv[d - 1][h], vx, rw, pgrp + j2 * 32768 + ((j2 + d) & 3) * 8192 + w * 2048, h * 1024);
            f32x4 v[4];
#pragma unroll
            for (int b = 0; b < 4; ++b) v[b] = *(LAS f32x4*)(L + LDS_M + w * 8192 + b * 1024 + vx);
            asm volatile("s_waitcnt vmcnt(0)" : "+v"(pv[0][0]), "+v"(pv[0][1]), "+v"(pv[1][0]), "+v"(pv[1][1]), "+v"(pv[2][0]), "+v"(pv[2][1]) :: "memory");
#pragma unroll
            for (int n = 3; n < 8; ++n) dma_p3(2, n, (n & 1) ? gu1 : gu0, lw, g_sb);
#pragma unroll
            for (int d = 0; d < 3; ++d)
#pragma unroll
                for (int h = 0; h < 2; ++h) {
                    const u32x4 p = __builtin_bit_cast(u32x4, pv[d][h]);
                    v[2 * h] += (f32x4){lo16(p[0]), hi16(p[0]), lo16(p[1]), hi16(p[1])};
                    v[2 * h + 1] += (f32x4){lo16(p[2]), hi16(p[2]), lo16(p[3]), hi16(p[3])};
                }
            finish_strip(v, false, WS_X1 + layer * 2 * MB, WS_ST + layer * ST_BYTES);
            wait_vm<0>();
            bar();
            publish(rw, 6 * layer + 2, blk, tag + 2, w);
#pragma unroll
            for (int n = 0; n < 8; ++n) dma_p3(3, n, (n & 1) ? gu1 : gu0, lw, g_sb);
        }

        {
            const int w = opq_s(wid), lane = lane_id(), i16 = lane & 15, q = lane >> 4;
            poll8(rw, 6 * layer + 2, blk_p24(4 * RT + w, lane & 7), tag + 2, dead);
            if (layer == 0 && blk == 0 && w == 0) {
                u64 e2 = epoch; asm volatile("" : "+s"(e2)); e2 += 64;
                __builtin_amdgcn_raw_buffer_store_b64((u32x2){(unsigned)e2, (unsigned)(e2 >> 32)}, rw, 0, 0, 16);
                wait_vm<0>();
            }
            const int sb0 = 16 * RT + 4 * w;
            u32x2 sa[4], sb_[4];
            bf16x8 act[P3_LA + 1][4];
            const unsigned vo = lane * 16, xo = WS_X1 + layer * 2 * MB + sb0 * 16384;
            auto load_x = [&](int g, int t) { aload(act[g % (P3_LA + 1)][t], vo, rw, xo, t * 16384 + g * 1024); };
#pragma unroll
            for (int g = 0; g < P3_LA; ++g)
#pragma unroll
                for (int t = 0; t < 4; ++t) load_x(g, t);
            bar();
            const int qq = (lane >> 2) & 3, p4 = lane & 3;
            unsigned tb_[7];
            {
                const unsigned f = 2 * (qq >> 1) + 4 * (q & 1), rowb = (8 * q + qq) * 128 + 8 * (p4 & 1);
#pragma unroll
                for (int b = 0; b < 6; ++b) tb_[b] = (b & 1) * 65536 + rowb + (((2 * (b >> 1) + (p4 >> 1)) ^ f ^ (b & 1)) * 16);
                tb_[6] = (p4 >> 1) * 65536 + rowb + ((6 ^ f ^ (p4 >> 1)) * 16);
            }
            if (layer == 0) {
                f32x4 ac[7];
#pragma unroll
                for (int b = 0; b < 7; ++b) ac[b] = (f32x4){0.f, 0.f, 0.f, 0.f};
#pragma unroll
                for (int k = 0; k < 4; ++k)
#pragma unroll
                    for (int b = 0; b < 7; ++b) ac[b] = mfma(ldsT(L, tb_[b] + w * 16384 + k * 4096), ones, ac[b]);
#pragma unroll
                for (int b = 0; b < 7; ++b) *(LAS f32x4*)(L + T_CS3P + ((w * 7 + b) * 4 + q) * 16) = ac[b];
                wait_lds();
                bar();
                if (w == 0) {
                    const int l = lane < 28 ? lane : 27;
                    f32x4 s = *(LAS f32x4*)(L + T_CS3P + l * 16);
#pragma unroll
                    for (int k = 1; k < 4; ++k) s += *(LAS f32x4*)(L + T_CS3P + (k * 28 + l) * 16);
                    *(LAS f32x4*)(L + T_CS3 + l * 16) = s;
                }
            }
            auto w_lo = [&](int ks, int b) { return __builtin_amdgcn_ds_read_tr16_b64_v4i16((LAS s16x4*)(L + tb_[b] + ks * 4096)); };
            auto w_hi = [&](int ks, int b) { return __builtin_amdgcn_ds_read_tr16_b64_v4i16((LAS s16x4*)(L + tb_[b] + ks * 4096 + 512)); };
            const unsigned sto = WS_ST + layer * ST_BYTES + sb0 * 1024, sv0 = (2 * q * 16 + i16) * 8;
            f32x4 a3[7][4];
#pragma unroll
            for (int b = 0; b < 7; ++b)
#pragma unroll
                for (int t = 0; t < 4; ++t) a3[b][t] = (f32x4){0.f, 0.f, 0.f, 0.f};
            bf16x8 wf[7];
#pragma unroll
            for (int b = 0; b < 6; ++b) wf[b] = ldsT(L, tb_[b]);
            sfor<16>([&](auto KS) {
                constexpr int ks = CI(KS), sl = ks % (P3_LA + 1);
                wait4<p3_wait(ks)>(act[sl][0], act[sl][1], act[sl][2], act[sl][3]);
                sfor<7>([&](auto B) {
                    constexpr int b = CI(B), pb = b == 0 ? 6 : b - 1, pks = b == 0 ? ks : ks + 1;
                    s16x4 lo, hi;
                    a3[b][0] = mfma(wf[b], act[sl][0], a3[b][0]);
                    SCHED();
                    if constexpr (pks < 16) lo = w_lo(pks, pb);
                    SCHED();
                    a3[b][1] = mfma(wf[b], act[sl][1], a3[b][1]);
                    SCHED();
                    if constexpr (pks < 16) { hi = w_hi(pks, pb); wf[pb] = __builtin_shufflevector(lo, hi, 0, 1, 2, 3, 4, 5, 6, 7); }
                    SCHED();
                    a3[b][2] = mfma(wf[b], act[sl][2], a3[b][2]);
                    SCHED();
                    if constexpr (b >= 1 && b <= 4 && ks + P3_LA < 16) load_x(ks + P3_LA, b - 1);
                    SCHED();
                    a3[b][3] = mfma(wf[b], act[sl][3], a3[b][3]);
                    SCHED();
                    if constexpr (ks == 8 && b == 6) {
#pragma unroll
                        for (int t = 0; t < 4; ++t) { aload2(sa[t], sv0, rw, sto + t * 1024); aload2(sb_[t], sv0 + 128, rw, sto + t * 1024); }
                        SCHED();
                    }
                });
            });
            wait_lds();
            bar();

            const int l2 = lane_id();
            const unsigned vE = (l2 >> 3) * 1024 + (((l2 & 7) ^ ((l2 >> 3) & 2)) * 16), vO = vE ^ 64;
            const int ws_ = opq_s(wid);
            const unsigned lw = ldsb + ws_ * 8192, dsb = 448 * ws_ * 1024;
            const rsrc_t rwd = mk(wdn + 128 * ct, F * D * 2 - 128 * ct);
            auto dma_p4 = [&](int c, int p) {
                dma(lw, ((c + 1) & 3) * REG + p * 1024, (p & 1) ? vO : vE, rwd, dsb, p4_row(2 * c + (p >> 2), p & 3) * 1024);
            };
            float mean[4], sig[4], kk_[4];
            asm volatile("" : "+v"(sa[0]), "+v"(sa[1]), "+v"(sa[2]), "+v"(sa[3]), "+v"(sb_[0]), "+v"(sb_[1]), "+v"(sb_[2]), "+v"(sb_[3]));
            {
                float v[4];
                combine_stats<4>(sa, sb_, mean, v);
                float l2e = 1.44269504088896f;
                asm volatile("" : "+s"(l2e));
#pragma unroll
                for (int t = 0; t < 4; ++t) {
                    const float vv = v[t] + EPS, r = __builtin_amdgcn_rsqf(vv);
                    sig[t] = vv * r; kk_[t] = l2e * r;
                }
            }
            auto glu = [&](float G, float U, float cg, float cu, float m, float sg, float kk) {
                const float e = __builtin_amdgcn_exp2f(kk * (m * cg - G));
                return (U - m * cu) * __builtin_amdgcn_rcpf(sg * e + sg);
            };
            const unsigned ho = WS_H + layer * 7 * MB, hw = 14 * (CT >> 3);
            {
                u32x4 o[4];
                const f32x4 cg0 = *(LAS f32x4*)(L + T_CS3 + (0 * 4 + q) * 16), cu0 = *(LAS f32x4*)(L + T_CS3 + (1 * 4 + q) * 16);
                const f32x4 cg1 = *(LAS f32x4*)(L + T_CS3 + (2 * 4 + q) * 16), cu1 = *(LAS f32x4*)(L + T_CS3 + (3 * 4 + q) * 16);
                sfor<4>([&](auto T) {
                    constexpr int t = CI(T);
                    float h0[4], h1[4];
                    sfor<4>([&](auto R) {
                        constexpr int r = CI(R);
                        h0[r] = glu(a3[0][t][r], a3[1][t][r], cg0[r], cu0[r], mean[t], sig[t], kk_[t]);
                        h1[r] = glu(a3[2][t][r], a3[3][t][r], cg1[r], cu1[r], mean[t], sig[t], kk_[t]);
                        asm volatile("" : "+v"(h0[r]), "+v"(h1[r]));
                        dma_p4(t >> 1, 4 * (t & 1) + r);
                        SCHED();
                    });
                    o[t] = gather8(pk(h0[0], h0[1]), pk(h0[2], h0[3]), pk(h1[0], h1[1]), pk(h1[2], h1[3]));
                });
#pragma unroll
                for (int t = 0; t < 4; ++t) store16(o[t], rw, ho + ((sb0 + t) * 56 + hw + (CT & 7)) * 1024 + vo);
                SCHED();
            }
            {
                const f32x4 cg = *(LAS f32x4*)(L + T_CS3 + (4 * 4 + q) * 16), cu = *(LAS f32x4*)(L + T_CS3 + (5 * 4 + q) * 16);
                const f32x4 mg = *(LAS f32x4*)(L + T_CS3 + (6 * 4 + (q & 1)) * 16), mu = *(LAS f32x4*)(L + T_CS3 + (6 * 4 + 2 + (q & 1)) * 16);
                unsigned H[4][2], M[2][2];
                sfor<4>([&](auto T) {
                    constexpr int t = CI(T);
                    float h[4];
                    if constexpr (t == 2) {
                        wait_vm<8>();
                        bar();
                        publish(rw, 6 * layer + 3, blk, tag + 3, w);
                        SCHED();
                    }
#pragma unroll
                    for (int r = 0; r < 4; ++r) h[r] = glu(a3[4][t][r], a3[5][t][r], cg[r], cu[r], mean[t], sig[t], kk_[t]);
                    H[t][0] = pk(h[0], h[1]); H[t][1] = pk(h[2], h[3]);
                    asm volatile("" : "+v"(H[t][0]), "+v"(H[t][1]));
#pragma unroll
                    for (int r = 0; r < 4; ++r) dma_p4(2 + (t >> 1), 4 * (t & 1) + r);
                    SCHED();
                });
#pragma unroll
                for (int p = 0; p < 2; ++p) {
                    const float m = q < 2 ? mean[2 * p] : mean[2 * p + 1], sg = q < 2 ? sig[2 * p] : sig[2 * p + 1], kk = q < 2 ? kk_[2 * p] : kk_[2 * p + 1];
                    float h[4];
#pragma unroll
                    for (int r = 0; r < 4; ++r) {
                        auto sw = __builtin_amdgcn_permlane32_swap(__float_as_uint(a3[6][2 * p][r]), __float_as_uint(a3[6][2 * p + 1][r]), false, false);
                        h[r] = glu(__uint_as_float(sw[0]), __uint_as_float(sw[1]), mg[r], mu[r], m, sg, kk);
                    }
                    M[p][0] = pk(h[0], h[1]); M[p][1] = pk(h[2], h[3]);
                }
                const unsigned be = 3 * (CT & 7) + (q & 1), bm = 3 * (CT & 7) + 2;
                const unsigned hb = ho + ((sb0 + (q >> 1)) * 56 + hw + 8 + (be >> 2)) * 1024 + ((be & 3) * 16 + i16) * 16;
                store16(gather8(H[0][0], H[0][1], H[1][0], H[1][1]), rw, hb);
                store16(gather8(H[2][0], H[2][1], H[3][0], H[3][1]), rw, hb + 2 * 56 * 1024);
                store16(gather8(M[0][0], M[0][1], M[1][0], M[1][1]), rw, ho + ((sb0 + q) * 56 + hw + 8 + (bm >> 2)) * 1024 + ((bm & 3) * 16 + i16) * 16);
                SCHED();
            }

            poll8(rw, 6 * layer + 3, blk_p3(rt >> 2, 8 * w + (lane_id() & 7)), tag + 3, dead);
            bar();
            publish(rw, 6 * layer + 4, blk, tag + 4, w);
            unsigned so[4];
#pragma unroll
            for (int d = 0; d < 4; ++d) so[d] = ho + ((4 * rt + ((ws_ + d) & 3)) * 56 + 14 * ws_) * 1024;
            bf16x8 hact[6][4];
            auto load_h = [&](int u, int d) { aload(hact[u % 6][d], vo, rw, so[d], u * 1024); };
#pragma unroll
            for (int u = 0; u < 5; ++u)
#pragma unroll
                for (int d = 0; d < 4; ++d) load_h(u, d);
            f32x4 acc[4][4];
#pragma unroll
            for (int b = 0; b < 4; ++b)
#pragma unroll
                for (int d = 0; d < 4; ++d) acc[b][d] = (f32x4){0.f, 0.f, 0.f, 0.f};
            unsigned wb_[2][4];
            const int l3 = lane_id(), q3 = l3 >> 4, qq3 = (l3 >> 2) & 3, p3 = l3 & 3;
#pragma unroll
            for (int b = 0; b < 4; ++b) {
                wb_[0][b] = w * 8192 + (8 * q3 + qq3) * 128 + (((2 * b + (p3 >> 1)) ^ ((qq3 & 2) + 4 * (q3 & 1))) * 16) + 8 * (p3 & 1);
                wb_[1][b] = wb_[0][b] + 2 * REG;
            }
            auto wd_off = [&](int u, int b) { return wb_[(((u >> 1) + 1) & 3) >> 1][b] + (((u >> 1) + 1) & 1) * REG + (u & 1) * 4096; };
            bf16x8 wf4[2][4];
#pragma unroll
            for (int b = 0; b < 4; ++b) wf4[0][b] = ldsT(L, wd_off(0, b));
            u32x2 flb;
            sfor<14>([&](auto U) {
                constexpr int u = CI(U);
                wait4<p4_wait(u)>(hact[u % 6][0], hact[u % 6][1], hact[u % 6][2], hact[u % 6][3]);
                if constexpr (u == 1) {
                    aload2(flb, (unsigned)blk_p3(rt >> 2, 8 * w + (lane_id() & 7)) * 256u, rw, WS_FLAGS + (6 * layer + 4) * 65536u);
                    SCHED();
                }
                if constexpr (u == 3) {
                    asm volatile("" : "+v"(flb));
                    if (!__all((((u64)flb[1] << 32) | flb[0]) == tag + 4)) poll8(rw, 6 * layer + 4, blk_p3(rt >> 2, 8 * w + (lane_id() & 7)), tag + 4, dead);
                    SCHED();
                }
                sfor<8>([&](auto J) {
                    constexpr int j = CI(J), b = j >> 1, d0 = 2 * (j & 1);
                    acc[b][d0] = mfma(wf4[u & 1][b], hact[u % 6][d0], acc[b][d0]);
                    acc[b][d0 + 1] = mfma(wf4[u & 1][b], hact[u % 6][d0 + 1], acc[b][d0 + 1]);
                    SCHED();
                    if constexpr (j < 4 && u < 13) wf4[(u + 1) & 1][j] = ldsT(L, wd_off(u + 1, j));
                    if constexpr ((j & 1) == 0) {
                        constexpr int p = 4 * ((u + 1) & 1) + (j >> 1);
                        if constexpr (u >= 1 && u <= 6) dma_p4(((u - 1) >> 1) + 4, p);
                    } else if constexpr (u <= 8) load_h(u + 5, j >> 1);
                    SCHED();
                });
            });
            if (layer == 0) dma_p1_all();
            LAS char* x3 = L + w * 8192 + lane * 16 + 3 * REG;
            LAS char* xm = L + w * 8192 + lane * 16 + LDS_M;
            asm volatile("" : "+v"(x3), "+v"(xm));
#pragma unroll
            for (int d = 1; d < 4; ++d)
#pragma unroll
                for (int b = 0; b < 4; ++b)
                    *(LAS f32x4*)((d < 3 ? x3 : xm) + (d < 3 ? (d - 1) * 4096 : 0) + b * 1024) = acc[b][d];
            wait_lds();
            bar();
            if (layer == 0) dma_p2n(4, 6);
            f32x4 v[4], t[3][4];
#pragma unroll
            for (int d = 1; d < 4; ++d) {
                LAS char* src = L + ((w - d) & 3) * 8192 + lane * 16 + (d < 3 ? 3 * REG + (d - 1) * 4096 : LDS_M);
                asm volatile("" : "+v"(src));
#pragma unroll
                for (int b = 0; b < 4; ++b) t[d - 1][b] = *(LAS f32x4*)(src + b * 1024);
            }
            SCHED();
#pragma unroll
            for (int b = 0; b < 4; ++b) v[b] = ((acc[b][0] + t[0][b]) + t[1][b]) + t[2][b];
            if (layer == 0) {
                finish_strip(v, false, WS_X2, WS_ST + 2 * ST_BYTES);
                wait_vm<0>();
                bar();
                publish(rw, 5, blk, tag + 5, w);
                dma_p2n(6, 8);
            } else {
                finish_strip(v, true, 0, 0);
            }
        }
        if (layer == 1) break;
        phase1(std::false_type{});
    }
    asm volatile("fnet_glu_code_end:");
}

}

extern "C" void kernel_launch(void* const* d_in, const int* in_sizes, int n_in,
                              void* d_out, int out_size, void* d_ws, size_t ws_size,
                              hipStream_t stream) {
    (void)in_sizes; (void)n_in; (void)out_size; (void)ws_size;
    (void)hipFuncSetAttribute((const void*)fn::fnet_glu_kernel, hipFuncAttributeMaxDynamicSharedMemorySize, fn::LDS_BYTES);
    fn::fnet_glu_kernel<<<256, 256, fn::LDS_BYTES, stream>>>((const char*)d_in[0], (const char*)d_in[1], (const char*)d_in[2],
        (const char*)d_in[3], (const char*)d_in[4], (const char*)d_in[6], (const char*)d_in[7], (const char*)d_in[8],
        (char*)d_out, (char*)d_ws);
}
```

```cpp
#include <hip/hip_runtime.h>
#include <hip/hip_bf16.h>
#include <stdint.h>
#include <type_traits>
#include <utility>

#define DI __device__ __forceinline__
#define LAS __attribute__((address_space(3)))
#define GAS __attribute__((address_space(1)))
typedef short bf16x8 __attribute__((ext_vector_type(8)));
typedef short s16x4 __attribute__((ext_vector_type(4)));
typedef float f32x4 __attribute__((ext_vector_type(4)));
typedef float f32x2 __attribute__((ext_vector_type(2)));
typedef float f32x16 __attribute__((ext_vector_type(16)));
typedef unsigned u32x4 __attribute__((ext_vector_type(4)));
typedef unsigned u32x2 __attribute__((ext_vector_type(2)));
typedef __bf16 bf16x2 __attribute__((ext_vector_type(2)));
typedef unsigned long long u64;
typedef __amdgpu_buffer_rsrc_t rsrc_t;

#define D47_AT 1
#define FFN_AT 0
namespace fn {

constexpr int S = 2048, D = 512, F = 1792;
constexpr unsigned MB = 1u << 20;
constexpr unsigned WS_FLAGS = 4096;
constexpr unsigned WS_ABT = 1 * MB;
constexpr unsigned WS_X1 = 9 * MB;
constexpr unsigned WS_X2 = 13 * MB;
constexpr unsigned WS_H = 15 * MB;
constexpr unsigned WS_ST = 29 * MB;
constexpr unsigned WS_PART = 32 * MB;
constexpr unsigned ST_BYTES = 128 * 1024;

constexpr unsigned REG = 32768;
constexpr unsigned LDS_M = 4 * REG;
constexpr unsigned T_RES = 4096;
constexpr unsigned T_STAT = 6144;
constexpr unsigned T_CS1 = LDS_M + 6400;
constexpr unsigned T_CS3 = LDS_M + 8192 + 6400;
constexpr unsigned T_CS3P = LDS_M + 16384 + 6400;
constexpr unsigned T_DUMMY = LDS_M + 24576 + 6400;
constexpr unsigned LDS_BYTES = 163840;
constexpr unsigned SPIN_LIMIT = 200000;
constexpr float EPS = 1e-5f;

DI rsrc_t mk(const void* p, unsigned bytes) { return __builtin_amdgcn_make_buffer_rsrc((void*)p, 0, bytes, 0x00020000); }
DI void dma(unsigned lds, int LI, unsigned voff, rsrc_t r, unsigned sb, int SI) {
    unsigned t;
    asm volatile("s_add_u32 m0, %1, %5\n\ts_add_u32 %0, %4, %6\n\tbuffer_load_dwordx4 %2, %3, %0 offen lds"
                 : "=&s"(t) : "s"(lds), "v"(voff), "s"(r), "s"(sb), "i"(LI), "i"(SI) : "m0", "scc", "memory");
}
DI void aload(bf16x8& d, unsigned voff, rsrc_t r, unsigned sb, int SI) {
    asm volatile("buffer_load_dwordx4 %0, %1, %2, %3 offen offset:%4 sc1"
                 : "=v"(d) : "v"(voff), "s"(r), "s"(sb + (SI & ~4095)), "i"(SI & 4095) : "memory");
}
DI void aload2(u32x2& d, unsigned voff, rsrc_t r, unsigned sb) {
    asm volatile("buffer_load_dwordx2 %0, %1, %2, %3 offen sc1" : "=v"(d) : "v"(voff), "s"(r), "s"(sb) : "memory");
}
template <int... Is, class Fn> DI void sfor_impl(std::integer_sequence<int, Is...>, Fn&& f) { (f(std::integral_constant<int, Is>{}), ...); }
template <int N, class Fn> DI void sfor(Fn&& f) { sfor_impl(std::make_integer_sequence<int, N>{}, f); }
#define CI(x) decltype(x)::value
DI int lane_id() { int l; asm volatile("v_mbcnt_lo_u32_b32 %0, -1, 0\n\tv_mbcnt_hi_u32_b32 %0, -1, %0" : "=v"(l)); return l; }
DI int opq_s(int v) { asm volatile("" : "+s"(v)); return v; }
template <int N> DI void wait_vm() { asm volatile("s_waitcnt vmcnt(%0)" :: "i"(N) : "memory"); }
template <int N> DI void wait2(bf16x8& a, bf16x8& b) { asm volatile("s_waitcnt vmcnt(%2)" : "+v"(a), "+v"(b) : "i"(N) : "memory"); }
template <int N> DI void wait4(bf16x8& a, bf16x8& b, bf16x8& c, bf16x8& d) {
    asm volatile("s_waitcnt vmcnt(%4)" : "+v"(a), "+v"(b), "+v"(c), "+v"(d) : "i"(N) : "memory");
}
DI void wait_lds() { asm volatile("s_waitcnt lgkmcnt(0)" ::: "memory"); }
DI void bar() { asm volatile("" ::: "memory"); __builtin_amdgcn_s_barrier(); asm volatile("" ::: "memory"); }
#define SCHED() __builtin_amdgcn_sched_barrier(0)
DI f32x4 mfma(bf16x8 a, bf16x8 b, f32x4 c) { return __builtin_amdgcn_mfma_f32_16x16x32_bf16(a, b, c, 0, 0, 0); }
DI f32x16 mfma32(bf16x8 a, bf16x8 b, f32x16 c) { return __builtin_amdgcn_mfma_f32_32x32x16_bf16(a, b, c, 0, 0, 0); }
DI unsigned pk(float lo, float hi) { f32x2 v = {lo, hi}; return __builtin_bit_cast(unsigned, __builtin_convertvector(v, bf16x2)); }
DI float lo16(unsigned u) { return __uint_as_float(u << 16); }
DI float hi16(unsigned u) { return __uint_as_float(u & 0xffff0000u); }
DI float sum16(float v) { auto r = __builtin_amdgcn_permlane16_swap(__float_as_uint(v), __float_as_uint(v), false, false); return __uint_as_float(r[0]) + __uint_as_float(r[1]); }
DI float sum32(float v) { auto r = __builtin_amdgcn_permlane32_swap(__float_as_uint(v), __float_as_uint(v), false, false); return __uint_as_float(r[0]) + __uint_as_float(r[1]); }
DI float sumq(float v) { return sum32(sum16(v)); }
DI u32x4 gather8(unsigned x0, unsigned x1, unsigned y0, unsigned y1) {
    auto a = __builtin_amdgcn_permlane32_swap(x0, y0, false, false);
    auto b = __builtin_amdgcn_permlane32_swap(x1, y1, false, false);
    auto c = __builtin_amdgcn_permlane16_swap(a[0], a[1], false, false);
    auto d = __builtin_amdgcn_permlane16_swap(b[0], b[1], false, false);
    return (u32x4){c[0], d[0], c[1], d[1]};
}
DI void store16(u32x4 v, rsrc_t r, unsigned voff) { __builtin_amdgcn_raw_buffer_store_b128(v, r, voff, 0, 16); }
DI bf16x8 ldsA(LAS char* L, unsigned off) { return *(LAS bf16x8*)(L + off); }
DI bf16x8 ldsT(LAS char* L, unsigned off) {
    s16x4 lo = __builtin_amdgcn_ds_read_tr16_b64_v4i16((LAS s16x4*)(L + off));
    s16x4 hi = __builtin_amdgcn_ds_read_tr16_b64_v4i16((LAS s16x4*)(L + off + 512));
    return __builtin_shufflevector(lo, hi, 0, 1, 2, 3, 4, 5, 6, 7);
}
DI int blk_p24(int rt, int ct) { return 8 * (4 * ((rt >> 1) & 7) + (ct >> 1)) + 4 * (rt >> 4) + 2 * (rt & 1) + (ct & 1); }
DI int blk_p1(int rt1, int et) { return 8 * (8 * (rt1 & 3) + (et & 7)) + 2 * (rt1 >> 2) + (et >> 3); }
DI int blk_p3(int RT, int CT) { return 8 * (8 * (RT & 3) + (CT & 7)) + 2 * (CT >> 3) + (RT >> 2); }

DI void poll8(rsrc_t rw, int phase, int prod, u64 tag, bool& dead) {
    const unsigned voff = (unsigned)prod * 256u, sb = WS_FLAGS + (unsigned)phase * 65536u;
    if (!dead) {
        unsigned spins = 0;
        for (;;) {
            u64 v;
            asm volatile("buffer_load_dwordx2 %0, %1, %2, %3 offen sc1\n\ts_waitcnt vmcnt(0)" : "=v"(v) : "v"(voff), "s"(rw), "s"(sb) : "memory");
            if (__all(v == tag)) break;
            if (++spins > SPIN_LIMIT) { dead = true; break; }
        }
    }
    asm volatile("" ::: "memory");
}
DI void publish(rsrc_t rw, int phase, int blk, u64 tag, int w) {
    if (w == 0)
        __builtin_amdgcn_raw_buffer_store_b64((u32x2){(unsigned)tag, (unsigned)(tag >> 32)}, rw, WS_FLAGS + (unsigned)(phase * 256 + blk) * 256u, 0, 16);
}

constexpr int P1_LA = 12;
constexpr int p1_issued(int ks) { return 2 * P1_LA + 2 * (ks < 16 - P1_LA ? ks : 16 - P1_LA); }
constexpr int p1_wait(int ks) { return p1_issued(ks) - 1 - (ks < P1_LA ? 2 * ks + 1 : p1_issued(ks - P1_LA + 1) - 1); }
constexpr int P2_LA = 22;
constexpr int p2_dma(int v) { return (v >= 8 && v <= 39) || (v >= 43 && v <= 50) ? 1 : 0; }
constexpr int p2_ops(int v) { return p2_dma(v) + (v + P2_LA < 64 ? 1 : 0); }
constexpr int p2_issued(int v) { int n = P2_LA; for (int j = 0; j < v; ++j) n += p2_ops(j); return n; }
constexpr int p2_wait(int v) {
    const int pa = v < P2_LA ? v : p2_issued(v - P2_LA + 1) - 1;
    const int c = (v >> 2) + 1;
    const int pd = (v & 3) == 2 && c >= 8 && c < 16 ? p2_issued(4 * (c - 6) + 3) : -1;
    return p2_issued(v) - 1 - (pa > pd ? pa : pd);
}
constexpr int P3_LA = 3;
constexpr int p3_ops(int g) { return (g + P3_LA < 16 ? 4 : 0) + (g == 8 ? 8 : 0); }
constexpr int p3_issued(int g) { int n = 4 * P3_LA; for (int j = 0; j < g; ++j) n += p3_ops(j); return n; }
constexpr int p3_wait(int g) { return p3_issued(g) - 1 - (g < P3_LA ? 4 * g + 3 : p3_issued(g - P3_LA) + 3); }
constexpr int p4_ops(int u) { return (u == 1 ? 1 : 0) + (u >= 1 && u <= 6 ? 4 : 0) + (u <= 8 ? 4 : 0); }
constexpr int p4_issued(int u) { int n = 20; for (int j = 0; j < u; ++j) n += p4_ops(j); return n; }
constexpr int p4_wait(int u) {
    int p = u < 5 ? 4 * u + 3 : p4_issued(u - 4) - 1;
    if (u == 3 && p < p4_issued(1)) p = p4_issued(1);
    if ((u & 1) && u < 13) {
        const int c = (u + 1) >> 1;
        if (c >= 4 && p4_issued(2 * c - 6) + 6 > p) p = p4_issued(2 * c - 6) + 6;
    }
    return p4_issued(u) - 1 - p;
}
constexpr int p4_row(int u, int qq) { return u < 8 ? 56 * u + 8 * qq : 56 * ((4 * (u - 8) + qq) / 3) + 32 + 8 * ((4 * (u - 8) + qq) % 3); }

template <int N> DI void combine_stats(const u32x2 (&a)[N], const u32x2 (&b)[N], float (&mean)[N], float (&var)[N]) {
    float s[N], t[N], inv = 1.0f / 512.0f;
    asm volatile("" : "+s"(inv));
#pragma unroll
    for (int i = 0; i < N; ++i) { s[i] = __uint_as_float(a[i][0]) + __uint_as_float(b[i][0]); t[i] = __uint_as_float(a[i][1]) + __uint_as_float(b[i][1]); }
#pragma unroll
    for (int i = 0; i < N; ++i) { s[i] = sum16(s[i]); t[i] = sum16(t[i]); }
#pragma unroll
    for (int i = 0; i < N; ++i) { s[i] = sum32(s[i]); t[i] = sum32(t[i]); }
#pragma unroll
    for (int i = 0; i < N; ++i) { mean[i] = s[i] * inv; var[i] = fmaxf(t[i] * inv - mean[i] * mean[i], 0.f); }
}

__global__ __launch_bounds__(256, 2) void fnet_glu_kernel(const char* __restrict__ x, const char* __restrict__ wsre, const char* __restrict__ wsim,
        const char* __restrict__ wdre, const char* __restrict__ wdim_, const char* __restrict__ wg, const char* __restrict__ wu,
        const char* __restrict__ wdn, char* __restrict__ out, char* __restrict__ ws) {
    extern __shared__ __attribute__((aligned(16))) char smem[];
    LAS char* L = (LAS char*)smem;
    int wid;
    asm volatile("s_nop 1\n\tv_readfirstlane_b32 %0, %1\n\ts_nop 4" : "=s"(wid) : "v"(threadIdx.x >> 6));
    const int blk = blockIdx.x, xcd = blk & 7, idx = blk >> 3;

    const unsigned ldsb = (unsigned)(size_t)L;
    const rsrc_t rw = mk(ws, 48 * MB);
    const int T2 = 8 * (xcd >> 2) + (idx >> 2), E2 = idx & 3, j2 = xcd & 3;
    const int rt = 2 * T2 + (j2 >> 1), ct = 2 * E2 + (j2 & 1);
    const int rt1 = 4 * (xcd >> 1) + (idx >> 3), et = 8 * (xcd & 1) + (idx & 7);
    const int RT = 4 * (xcd & 1) + (idx >> 3), CT = 8 * (xcd >> 1) + (idx & 7);
    {
        u64 pc;
        int len;
        asm volatile("s_getpc_b64 %0\n1:\n\ts_mov_b32 %1, fnet_glu_code_end-1b" : "=s"(pc), "=s"(len));
        const rsrc_t rc = mk((const void*)pc, len > 0 ? (unsigned)len : 0u);
        const unsigned o = (unsigned)((idx * 4 + wid) & 7) * 8192u + lane_id() * 128u, l = ldsb + T_DUMMY + 768;
        asm volatile("s_mov_b32 m0, %0\n\ts_nop 0\n\tbuffer_load_dword %1, %2, 0 offen lds" :: "s"(l), "v"(o), "s"(rc) : "m0", "memory");
    }

    auto p1_rsrc = [&](unsigned bytes) { return mk(((et >> 3) ? wdim_ : wdre) + (size_t)(et & 7) * 64 * 1024, bytes); };
    auto dma_p1 = [&](int j, int p, rsrc_t r, unsigned v0, unsigned lw, unsigned sb) {
        dma(lw, (32 * j + p) * 1024, v0 ^ (16 * p), r, sb, (32 * j + p) * 1024);
    };
    auto p3_voff = [&](int ln, int up, int odd) -> unsigned {
        const unsigned c = (ln & 7) ^ ((2 * ((ln >> 4) & 1) + 4 * odd) ^ up);
        return (ln >> 3) * 3584 + 16 * (c > 6 ? 6 : c);
    };
    auto dma_p3 = [&](int j, int p, unsigned v, unsigned lw, unsigned sb) {
        dma(lw, j * REG + p * 1024, v, mk((j >> 1) ? wu : wg, D * F * 2), sb, (256 * (j & 1) + 8 * p) * 3584);
    };
    auto dma_p2n = [&](int c0, int c1) {
        const int w = opq_s(wid), ln = lane_id();
        const rsrc_t r = mk(((j2 >> 1) ? wsim : wsre) + ((size_t)128 * T2 * S + 1024 * (j2 & 1)) * 2, (128 * S - 1024 * (j2 & 1)) * 2);
        const unsigned vE = (ln >> 3) * 4096 + (((ln & 7) ^ (ln >> 4)) * 16), vO = vE ^ 64;
#pragma unroll
        for (int c = c0; c < c1; ++c)
#pragma unroll
            for (int p = 0; p < 4; ++p) dma(ldsb + w * 4096, (c & 7) * 16384 + p * 1024, (p & 1) ? vO : vE, r, 32 * w * 4096, p * 32768 + c * 128);
    };
    auto dma_p1_all = [&]() {
        const int w = opq_s(wid), ln = lane_id();
        const rsrc_t r = p1_rsrc(65536);
        const unsigned v0 = (unsigned)(ln * 16) ^ (128 * (w & 1)), lw = ldsb + w * 8192;
#pragma unroll
        for (int j = 0; j < 2; ++j)
#pragma unroll
            for (int p = 0; p < 8; ++p) dma_p1(j, p, r, v0, lw, w * 8192);
    };
    auto warm_ffn = [&]() {
        const int w = opq_s(wid), ln = lane_id();
        const unsigned o = blk * 7168u + (ln < 56 ? ln : 55) * 128u, l = ldsb + T_DUMMY + w * 256;
        auto touch = [&](rsrc_t r) {
            asm volatile("s_mov_b32 m0, %0\n\ts_nop 0\n\tbuffer_load_dword %1, %2, 0 offen lds" :: "s"(l), "v"(o), "s"(r) : "m0", "memory");
        };
        if (w == 0) touch(mk(wg, D * F * 2));
        if (w == 1) touch(mk(wu, D * F * 2));
        if (w == 2) touch(mk(wdn, D * F * 2));
    };
    auto warm_ws = [&]() {
        const int w = opq_s(wid), ln = lane_id();
        const rsrc_t r = mk(((j2 >> 1) ? wsim : wsre) + ((size_t)128 * T2 * S + 1024 * (j2 & 1)) * 2, (128 * S - 1024 * (j2 & 1)) * 2);
        const unsigned o = (32 * E2 + (ln & 31)) * 4096 + (4 * w + (ln >> 5)) * 128, l = ldsb + T_DUMMY + 1024;
        asm volatile("s_mov_b32 m0, %0\n\ts_nop 0\n\tbuffer_load_dword %1, %2, 0 offen lds" :: "s"(l), "v"(o), "s"(r) : "m0", "memory");
        asm volatile("s_mov_b32 m0, %0\n\ts_nop 0\n\tbuffer_load_dword %1, %2, 0 offen lds" :: "s"(l), "v"(o + 256), "s"(r) : "m0", "memory");
    };
    dma_p1_all();

    u32x2 ev;
    aload2(ev, 0u, rw, 0u);
    u64 epoch = 0;
    bool dead = false;
    const bf16x8 ones = {0x3F80, 0x3F80, 0x3F80, 0x3F80, 0x3F80, 0x3F80, 0x3F80, 0x3F80};

        auto phase1 = [&](auto first_tag) {
            constexpr bool FIRST = decltype(first_tag)::value;
            constexpr int layer = FIRST ? 0 : 1;
            const int w = opq_s(wid), lane = lane_id(), i16 = lane & 15, q = lane >> 4;
            const int sbg = 8 * rt1 + 2 * w;
            bf16x8 xa[2][16];
            u32x2 sa[2], sb_[2];
            f32x4 acc[2][4];
#pragma unroll
            for (int s = 0; s < 2; ++s)
#pragma unroll
                for (int e = 0; e < 4; ++e) acc[s][e] = (f32x4){0.f, 0.f, 0.f, 0.f};
            unsigned wb[4];
#pragma unroll
            for (int k = 0; k < 4; ++k) wb[k] = i16 * 1024 + (((4 * k + q) ^ i16) * 16);
            LAS f32x2* tst = (LAS f32x2*)(L + LDS_M + w * 8192 + T_STAT);
            const float sgn = __int_as_float(opq_s((et >> 3) ? (int)0xbf800000 : 0x3f800000));
            f32x4 aS[2], aQ[2], aC = {0.f, 0.f, 0.f, 0.f};
#pragma unroll
            for (int s = 0; s < 2; ++s) { aS[s] = (f32x4){0.f, 0.f, 0.f, 0.f}; aQ[s] = aS[s]; }
            const unsigned wown = w * 16384;
            const unsigned vo = lane * 16, sbase = WS_X2 + sbg * 16384;
            if constexpr (FIRST) {
                const rsrc_t rx = mk(x, S * D * 2);
                bf16x8 xr[32];
#pragma unroll
                for (int r = 0; r < 32; ++r) aload(xr[r], vo, rx, sbg * 16384, r * 1024);
                LAS char* T0 = L + 2 * REG + w * 8192;
                sfor<2>([&](auto SB) {
                    constexpr int s = CI(SB);
                    sfor<4>([&](auto G) {
                        constexpr int g = CI(G), r0 = 16 * s + 4 * g;
                        wait4<28 - r0 + (s == 1 ? 2 : 0)>(xr[r0], xr[r0 + 1], xr[r0 + 2], xr[r0 + 3]);
#pragma unroll
                        for (int r = 0; r < 4; ++r)
                            *(LAS bf16x8*)(T0 + ((4 * g + r) >> 3) * REG + ((4 * g + r) & 7) * 1024 + ((lane * 16) ^ ((4 * g + r) * 16))) = xr[r0 + r];
                    });
#pragma unroll
                    for (int ks = 0; ks < 16; ++ks)
                        xa[s][ks] = *(LAS bf16x8*)(T0 + (i16 >> 3) * REG + (i16 & 7) * 1024 + (((4 * ks + q) ^ i16) * 16));
                    wait_lds();
                    if constexpr (s == 0) warm_ws();
                });
                if (FFN_AT == 0) warm_ffn();
                asm volatile("" : "+v"(ev));
                epoch = ((u64)(unsigned)__builtin_amdgcn_readfirstlane((int)ev[1]) << 32) | (unsigned)__builtin_amdgcn_readfirstlane((int)ev[0]);
                wait_lds();
                bar();
                if (D47_AT == 0) dma_p2n(4, 8);
                if (FFN_AT == 1) warm_ffn();
            } else {
                poll8(rw, 5, blk_p24(2 * rt1 + (w >> 1), lane & 7), epoch + 6, dead);
#pragma unroll
                for (int s = 0; s < 2; ++s) {
                    const unsigned so = WS_ST + 2 * ST_BYTES + (sbg + s) * 1024;
                    aload2(sa[s], (2 * q * 16 + i16) * 8, rw, so);
                    aload2(sb_[s], ((2 * q + 1) * 16 + i16) * 8, rw, so);
                }
#pragma unroll
                for (int ks = 0; ks < P1_LA; ++ks)
#pragma unroll
                    for (int s = 0; s < 2; ++s) aload(xa[s][ks], vo, rw, sbase, s * 16384 + ks * 1024);
                wait2<p1_wait(0)>(xa[0][0], xa[1][0]);
                asm volatile("" : "+v"(sa[0]), "+v"(sa[1]), "+v"(sb_[0]), "+v"(sb_[1]));
            }
            bf16x8 wf[2][4], wo;
#pragma unroll
            for (int e = 0; e < 4; ++e) wf[0][e] = ldsA(L, wb[0] + e * 16384);
            sfor<16>([&](auto KS) {
                constexpr int ks = CI(KS);
                if constexpr (!FIRST && ks > 0) wait2<p1_wait(ks)>(xa[0][ks], xa[1][ks]);
                if constexpr (FIRST) wo = ldsA(L, wb[ks & 3] + wown + 256 * (ks >> 2));
                sfor<4>([&](auto E) {
                    constexpr int e = CI(E);
                    acc[0][e] = mfma(xa[0][ks], wf[ks & 1][e], acc[0][e]);
                    acc[1][e] = mfma(xa[1][ks], wf[ks & 1][e], acc[1][e]);
                    SCHED();
                    if constexpr (ks < 15) wf[(ks + 1) & 1][e] = ldsA(L, wb[(ks + 1) & 3] + e * 16384 + 256 * ((ks + 1) >> 2));
                    if constexpr (!FIRST && e >= 2 && ks + P1_LA < 16) aload(xa[e - 2][ks + P1_LA], vo, rw, sbase, (e - 2) * 16384 + (ks + P1_LA) * 1024);
                    SCHED();
                });
                if constexpr (!FIRST && ks == 15) {
                    float mean[2], var[2];
                    combine_stats<2>(sa, sb_, mean, var);
#pragma unroll
                    for (int s = 0; s < 2; ++s) tst[16 * s + i16] = (f32x2){mean[s], sgn * __builtin_amdgcn_rsqf(var[s] + EPS)};
                    SCHED();
                }
                if constexpr (FIRST) {
                    aC = mfma(ones, wo, aC);
#pragma unroll
                    for (int s = 0; s < 2; ++s) { aS[s] = mfma(ones, xa[s][ks], aS[s]); aQ[s] = mfma(xa[s][ks], xa[s][ks], aQ[s]); }
                    SCHED();
                }
            });
            if constexpr (FIRST) {
                ((LAS float*)(L + T_CS1))[16 * w + i16] = aC[0];
#pragma unroll
                for (int s = 0; s < 2; ++s) {
                    const int r3 = i16 & 3;
                    float dq = r3 == 0 ? aQ[s][0] : r3 == 1 ? aQ[s][1] : r3 == 2 ? aQ[s][2] : aQ[s][3];
                    dq = (q == (i16 >> 2)) ? dq : 0.f;
                    const float mean = aS[s][0] * (1.0f / 512.0f);
                    const float var = fmaxf(sumq(dq) * (1.0f / 512.0f) - mean * mean, 0.f);
                    tst[16 * s + i16] = (f32x2){mean, sgn * __builtin_amdgcn_rsqf(var + EPS)};
                }
            }
            wait_lds();
            bar();
            dma_p2n(0, 2);
            f32x4 st0[2], st1[2];
#pragma unroll
            for (int s = 0; s < 2; ++s) {
                const f32x4 a = *(LAS f32x4*)(tst + 16 * s + 4 * q), b = *(LAS f32x4*)(tst + 16 * s + 4 * q + 2);
                st0[s] = (f32x4){a[0], a[2], b[0], b[2]}; st1[s] = (f32x4){a[1], a[3], b[1], b[3]};
            }
#pragma unroll
            for (int E = 0; E < 2; ++E) {
                const float cs0 = ((LAS float*)(L + T_CS1))[32 * E + i16], cs1 = ((LAS float*)(L + T_CS1))[32 * E + 16 + i16];
#pragma unroll
                for (int s = 0; s < 2; ++s) {
                    f32x4 v0, v1;
#pragma unroll
                    for (int r = 0; r < 4; ++r) {
                        v0[r] = (acc[s][2 * E][r] - st0[s][r] * cs0) * st1[s][r];
                        v1[r] = (acc[s][2 * E + 1][r] - st0[s][r] * cs1) * st1[s][r];
                    }
                    auto a = __builtin_amdgcn_permlane16_swap(pk(v0[0], v0[1]), pk(v1[0], v1[1]), false, false);
                    auto b = __builtin_amdgcn_permlane16_swap(pk(v0[2], v0[3]), pk(v1[2], v1[3]), false, false);
                    store16((u32x4){a[0], b[0], a[1], b[1]}, rw,
                            WS_ABT + layer * 4 * MB + (et >> 3) * 2 * MB + ((2 * (et & 7) + E) * 128 + sbg + s) * 1024 + lane * 16);
                }
            }
        };

    phase1(std::true_type{});
    for (int layer = 0; layer < 2; ++layer) {
        const u64 tag = epoch + 1 + 6 * layer;
        auto finish_strip = [&](f32x4 (&v)[4], bool last, unsigned xo, unsigned sto) {
            const int w = opq_s(wid), lane = lane_id(), i16 = lane & 15, q = lane >> 4;
            float sum = 0.f, sq = 0.f;
            u32x2 res[4];
            {
                const u32x4 r0 = *(LAS u32x4*)(L + LDS_M + w * 8192 + T_RES + lane * 16), r1 = *(LAS u32x4*)(L + LDS_M + w * 8192 + T_RES + 1024 + lane * 16);
                res[0] = (u32x2){r0[0], r0[1]}; res[1] = (u32x2){r0[2], r0[3]}; res[2] = (u32x2){r1[0], r1[1]}; res[3] = (u32x2){r1[2], r1[3]};
            }
#pragma unroll
            for (int b = 0; b < 4; ++b) {
                res[b][0] = pk(lo16(res[b][0]) + v[b][0], hi16(res[b][0]) + v[b][1]);
                res[b][1] = pk(lo16(res[b][1]) + v[b][2], hi16(res[b][1]) + v[b][3]);
                const float x0 = lo16(res[b][0]), x1 = hi16(res[b][0]), x2 = lo16(res[b][1]), x3 = hi16(res[b][1]);
                sum += (x0 + x1) + (x2 + x3);
                sq += (x0 * x0 + x1 * x1) + (x2 * x2 + x3 * x3);
            }
            const u32x4 o0 = gather8(res[0][0], res[0][1], res[1][0], res[1][1]);
            const u32x4 o1 = gather8(res[2][0], res[2][1], res[3][0], res[3][1]);
            if (last) {
                const rsrc_t rxo = mk(out, S * D * 2);
                const unsigned a = (64 * rt + 16 * w + i16) * 1024 + (64 * ct + 8 * q) * 2;
                __builtin_amdgcn_raw_buffer_store_b128(o0, rxo, a, 0, 0);
                __builtin_amdgcn_raw_buffer_store_b128(o1, rxo, a + 64, 0, 0);
            } else {
                const unsigned a = xo + ((4 * rt + w) * 16 + 2 * ct) * 1024 + lane * 16;
                store16(o0, rw, a); store16(o1, rw, a + 1024);
                *(LAS u32x4*)(L + LDS_M + w * 8192 + T_RES + lane * 16) = (u32x4){res[0][0], res[0][1], res[1][0], res[1][1]};
                *(LAS u32x4*)(L + LDS_M + w * 8192 + T_RES + 1024 + lane * 16) = (u32x4){res[2][0], res[2][1], res[3][0], res[3][1]};
                sum = sum16(sum); sq = sum16(sq);
                sum = sum32(sum); sq = sum32(sq);
                __builtin_amdgcn_raw_buffer_store_b64((u32x2){__float_as_uint(sum), __float_as_uint(sq)}, rw, sto + (((4 * rt + w) * 8 + ct) * 16 + i16) * 8, 0, 16);
            }
        };

        {
            const int w = opq_s(wid), lane = lane_id(), i16 = lane & 15, q = lane >> 4;
            const int part = j2 >> 1, half = j2 & 1, r32 = lane & 31, hh = lane >> 5;
            const rsrc_t rws = mk((part ? wsim : wsre) + ((size_t)128 * T2 * S + 1024 * half) * 2, (128 * S - 1024 * half) * 2);
            const unsigned vsE = (lane >> 3) * 4096 + (((lane & 7) ^ (lane >> 4)) * 16), vsO = vsE ^ 64;
            const int w4_ = opq_s(wid);
            const unsigned lw4 = ldsb + w4_ * 4096, wsb = 32 * w4_ * 4096;
            auto dma_p2 = [&](int c, int p) { dma(lw4, (c & 7) * 16384 + p * 1024, (p & 1) ? vsO : vsE, rws, wsb, p * 32768 + c * 128); };
            wait_vm<0>();
            bar();
            publish(rw, 6 * layer + 0, blk, tag, w);
            dma_p2n(2, 4);
            if (D47_AT == 1 && layer == 0) dma_p2n(4, 8);
            poll8(rw, 6 * layer + 0, blk_p1(8 * half + (lane & 7), 8 * part + 2 * E2 + (w >> 1)), tag, dead);
            const unsigned so0 = WS_ABT + layer * 4 * MB + part * 2 * MB + ((4 * E2 + w) * 128 + 64 * half) * 1024, vo = lane * 16;
            bf16x8 act[P2_LA + 2];
            auto load_act = [&](int v) { aload(act[v % (P2_LA + 2)], vo, rw, so0, v * 1024); };
#pragma unroll
            for (int v = 0; v < P2_LA; ++v) load_act(v);
            f32x16 acc[4];
#pragma unroll
            for (int t = 0; t < 4; ++t)
#pragma unroll
                for (int r = 0; r < 16; ++r) acc[t][r] = 0.f;
            unsigned rb[2][4];
#pragma unroll
            for (int k = 0; k < 4; ++k) { rb[0][k] = r32 * 128 + (((2 * k + hh) ^ ((r32 >> 1) & 7)) * 16); rb[1][k] = rb[0][k] + 65536; }
            auto ws_frag = [&](int n) {
                const int v = n >> 2, t = n & 3, c = v >> 2;
                return ldsA(L, rb[(c & 7) >> 2][v & 3] + (c & 3) * 16384 + t * 4096);
            };
            const int ws_ = opq_s(wid);
            const unsigned lw = ldsb + ws_ * 8192, g_sb = 64 * ws_ * 3584 + 112 * CT;
            unsigned gg0 = 0, gg1 = 0;
            bf16x8 wf[8];
#pragma unroll
            for (int n = 0; n < 6; ++n) wf[n] = ws_frag(n);
            sfor<64>([&](auto V) {
                constexpr int v = CI(V);
                asm volatile("s_waitcnt vmcnt(%1)" : "+v"(act[v % (P2_LA + 2)]) : "i"(p2_wait(v)) : "memory");
                if constexpr (v == 43) { const int l2 = lane_id(); gg0 = p3_voff(l2, 0, 0); gg1 = p3_voff(l2, 0, 1); }
                sfor<4>([&](auto TB) {
                    constexpr int t = CI(TB), n = 4 * v + t;
                    acc[t] = mfma32(act[v % (P2_LA + 2)], wf[n & 7], acc[t]);
                    SCHED();
                    if constexpr ((v & 3) == 2 && t == 2 && v < 60) bar();
                    if constexpr (n + 6 < 256) wf[(n + 6) & 7] = ws_frag(n + 6);
                    if constexpr (t == 1 && p2_dma(v)) {
                        if constexpr (v <= 39) dma_p2((v >> 2) + 6, v & 3);
                        else dma_p3(0, v - 43, ((v - 43) & 1) ? gg1 : gg0, lw, g_sb);
                    }
                    if constexpr (t == 3 && v + P2_LA < 64) load_act(v + P2_LA);
                    SCHED();
                });
            });
            const unsigned pgrp = WS_PART + layer * 8 * MB + (T2 * 4 + E2) * 131072;
            const int lx = lane_id(), i16x = lx & 15, tup = (lx >> 4) & 1, hx = lx >> 5;
#pragma unroll
            for (int a = 0; a < 2; ++a) {
                const int owner = 2 * a + (w >> 1);
                if (owner != j2) {
                    const unsigned po = pgrp + owner * 32768 + j2 * 8192 + (w & 1) * 1024 + tup * 2048 + (i16x + 16 * hx) * 16;
#pragma unroll
                    for (int u = 0; u < 2; ++u)
#pragma unroll
                        for (int qh = 0; qh < 2; ++qh) {
                            const f32x16& c = acc[2 * a + u];
                            store16((u32x4){pk(c[4 * qh], c[4 * qh + 1]), pk(c[4 * qh + 2], c[4 * qh + 3]),
                                            pk(c[8 + 4 * qh], c[8 + 4 * qh + 1]), pk(c[8 + 4 * qh + 2], c[8 + 4 * qh + 3])}, rw, po + u * 4096 + qh * 512);
                        }
                }
            }
#pragma unroll
            for (int a = 0; a < 2; ++a) {
                if (2 * a + (w >> 1) == j2) {
#pragma unroll
                    for (int u = 0; u < 2; ++u)
#pragma unroll
                        for (int g = 0; g < 4; ++g) {
                            const f32x16& c = acc[2 * a + u];
                            *(LAS f32x4*)(L + LDS_M + (2 * u + tup) * 8192 + (2 * (w & 1) + (g >> 1)) * 1024 + (i16x + 16 * (2 * (g & 1) + hx)) * 16) =
                                (f32x4){c[4 * g], c[4 * g + 1], c[4 * g + 2], c[4 * g + 3]};
                        }
                }
            }
            wait_vm<0>();
            wait_lds();
            bar();
            publish(rw, 6 * layer + 1, blk, tag + 1, w);
            const unsigned gu0 = p3_voff(lx, 1, 0), gu1 = p3_voff(lx, 1, 1), vx = lx * 16;
#pragma unroll
            for (int n = 0; n < 8; ++n) dma_p3(1, n, (n & 1) ? gg1 : gg0, lw, g_sb);
#pragma unroll
            for (int n = 0; n < 3; ++n) dma_p3(2, n, (n & 1) ? gu1 : gu0, lw, g_sb);
            if (layer == 0) {
                u32x2 res0[4];
                const rsrc_t rx = mk(x, S * D * 2);
                const unsigned vr = (64 * rt + 16 * w + i16x) * 1024 + (64 * ct + 4 * (lx >> 4)) * 2;
#pragma unroll
                for (int b = 0; b < 4; ++b) aload2(res0[b], vr + b * 32, rx, 0u);
                asm volatile("s_waitcnt vmcnt(0)" : "+v"(res0[0]), "+v"(res0[1]), "+v"(res0[2]), "+v"(res0[3]) :: "memory");
                *(LAS u32x4*)(L + LDS_M + w * 8192 + T_RES + vx) = (u32x4){res0[0][0], res0[0][1], res0[1][0], res0[1][1]};
                *(LAS u32x4*)(L + LDS_M + w * 8192 + T_RES + 1024 + vx) = (u32x4){res0[2][0], res0[2][1], res0[3][0], res0[3][1]};
            }
            poll8(rw, 6 * layer + 1, (blk & ~3) | (lx & 3), tag + 1, dead);
            bf16x8 pv[3][2];
#pragma unroll
            for (int d = 1; d < 4; ++d)
#pragma unroll
                for (int h = 0; h < 2; ++h) aload(pv[d - 1][h], vx, rw, pgrp + j2 * 32768 + ((j2 + d) & 3) * 8192 + w * 2048, h * 1024);
            f32x4 v[4];
#pragma unroll
            for (int b = 0; b < 4; ++b) v[b] = *(LAS f32x4*)(L + LDS_M + w * 8192 + b * 1024 + vx);
            asm volatile("s_waitcnt vmcnt(0)" : "+v"(pv[0][0]), "+v"(pv[0][1]), "+v"(pv[1][0]), "+v"(pv[1][1]), "+v"(pv[2][0]), "+v"(pv[2][1]) :: "memory");
#pragma unroll
            for (int n = 3; n < 8; ++n) dma_p3(2, n, (n & 1) ? gu1 : gu0, lw, g_sb);
#pragma unroll
            for (int d = 0; d < 3; ++d)
#pragma unroll
                for (int h = 0; h < 2; ++h) {
                    const u32x4 p = __builtin_bit_cast(u32x4, pv[d][h]);
                    v[2 * h] += (f32x4){lo16(p[0]), hi16(p[0]), lo16(p[1]), hi16(p[1])};
                    v[2 * h + 1] += (f32x4){lo16(p[2]), hi16(p[2]), lo16(p[3]), hi16(p[3])};
                }
            finish_strip(v, false, WS_X1 + layer * 2 * MB, WS_ST + layer * ST_BYTES);
            wait_vm<0>();
            bar();
            publish(rw, 6 * layer + 2, blk, tag + 2, w);
#pragma unroll
            for (int n = 0; n < 8; ++n) dma_p3(3, n, (n & 1) ? gu1 : gu0, lw, g_sb);
        }

        {
            const int w = opq_s(wid), lane = lane_id(), i16 = lane & 15, q = lane >> 4;
            poll8(rw, 6 * layer + 2, blk_p24(4 * RT + w, lane & 7), tag + 2, dead);
            if (layer == 0 && blk == 0 && w == 0) {
                u64 e2 = epoch; asm volatile("" : "+s"(e2)); e2 += 64;
                __builtin_amdgcn_raw_buffer_store_b64((u32x2){(unsigned)e2, (unsigned)(e2 >> 32)}, rw, 0, 0, 16);
                wait_vm<0>();
            }
            const int sb0 = 16 * RT + 4 * w;
            u32x2 sa[4], sb_[4];
            bf16x8 act[P3_LA + 1][4];
            const unsigned vo = lane * 16, xo = WS_X1 + layer * 2 * MB + sb0 * 16384;
            auto load_x = [&](int g, int t) { aload(act[g % (P3_LA + 1)][t], vo, rw, xo, t * 16384 + g * 1024); };
#pragma unroll
            for (int g = 0; g < P3_LA; ++g)
#pragma unroll
                for (int t = 0; t < 4; ++t) load_x(g, t);
            bar();
            const int qq = (lane >> 2) & 3, p4 = lane & 3;
            unsigned tb_[7];
            {
                const unsigned f = 2 * (qq >> 1) + 4 * (q & 1), rowb = (8 * q + qq) * 128 + 8 * (p4 & 1);
#pragma unroll
                for (int b = 0; b < 6; ++b) tb_[b] = (b & 1) * 65536 + rowb + (((2 * (b >> 1) + (p4 >> 1)) ^ f ^ (b & 1)) * 16);
                tb_[6] = (p4 >> 1) * 65536 + rowb + ((6 ^ f ^ (p4 >> 1)) * 16);
            }
            if (layer == 0) {
                f32x4 ac[7];
#pragma unroll
                for (int b = 0; b < 7; ++b) ac[b] = (f32x4){0.f, 0.f, 0.f, 0.f};
#pragma unroll
                for (int k = 0; k < 4; ++k)
#pragma unroll
                    for (int b = 0; b < 7; ++b) ac[b] = mfma(ldsT(L, tb_[b] + w * 16384 + k * 4096), ones, ac[b]);
#pragma unroll
                for (int b = 0; b < 7; ++b) *(LAS f32x4*)(L + T_CS3P + ((w * 7 + b) * 4 + q) * 16) = ac[b];
                wait_lds();
                bar();
                if (w == 0) {
                    const int l = lane < 28 ? lane : 27;
                    f32x4 s = *(LAS f32x4*)(L + T_CS3P + l * 16);
#pragma unroll
                    for (int k = 1; k < 4; ++k) s += *(LAS f32x4*)(L + T_CS3P + (k * 28 + l) * 16);
                    *(LAS f32x4*)(L + T_CS3 + l * 16) = s;
                }
            }
            auto w_lo = [&](int ks, int b) { return __builtin_amdgcn_ds_read_tr16_b64_v4i16((LAS s16x4*)(L + tb_[b] + ks * 4096)); };
            auto w_hi = [&](int ks, int b) { return __builtin_amdgcn_ds_read_tr16_b64_v4i16((LAS s16x4*)(L + tb_[b] + ks * 4096 + 512)); };
            const unsigned sto = WS_ST + layer * ST_BYTES + sb0 * 1024, sv0 = (2 * q * 16 + i16) * 8;
            f32x4 a3[7][4];
#pragma unroll
            for (int b = 0; b < 7; ++b)
#pragma unroll
                for (int t = 0; t < 4; ++t) a3[b][t] = (f32x4){0.f, 0.f, 0.f, 0.f};
            bf16x8 wf[7];
#pragma unroll
            for (int b = 0; b < 6; ++b) wf[b] = ldsT(L, tb_[b]);
            sfor<16>([&](auto KS) {
                constexpr int ks = CI(KS), sl = ks % (P3_LA + 1);
                wait4<p3_wait(ks)>(act[sl][0], act[sl][1], act[sl][2], act[sl][3]);
                sfor<7>([&](auto B) {
                    constexpr int b = CI(B), pb = b == 0 ? 6 : b - 1, pks = b == 0 ? ks : ks + 1;
                    s16x4 lo, hi;
                    a3[b][0] = mfma(wf[b], act[sl][0], a3[b][0]);
                    SCHED();
                    if constexpr (pks < 16) lo = w_lo(pks, pb);
                    SCHED();
                    a3[b][1] = mfma(wf[b], act[sl][1], a3[b][1]);
                    SCHED();
                    if constexpr (pks < 16) { hi = w_hi(pks, pb); wf[pb] = __builtin_shufflevector(lo, hi, 0, 1, 2, 3, 4, 5, 6, 7); }
                    SCHED();
                    a3[b][2] = mfma(wf[b], act[sl][2], a3[b][2]);
                    SCHED();
                    if constexpr (b >= 1 && b <= 4 && ks + P3_LA < 16) load_x(ks + P3_LA, b - 1);
                    SCHED();
                    a3[b][3] = mfma(wf[b], act[sl][3], a3[b][3]);
                    SCHED();
                    if constexpr (ks == 8 && b == 6) {
#pragma unroll
                        for (int t = 0; t < 4; ++t) { aload2(sa[t], sv0, rw, sto + t * 1024); aload2(sb_[t], sv0 + 128, rw, sto + t * 1024); }
                        SCHED();
                    }
                });
            });
            wait_lds();
            bar();

            const int l2 = lane_id();
            const unsigned vE = (l2 >> 3) * 1024 + (((l2 & 7) ^ ((l2 >> 3) & 2)) * 16), vO = vE ^ 64;
            const int ws_ = opq_s(wid);
            const unsigned lw = ldsb + ws_ * 8192, dsb = 448 * ws_ * 1024;
            const rsrc_t rwd = mk(wdn + 128 * ct, F * D * 2 - 128 * ct);
            auto dma_p4 = [&](int c, int p) {
                dma(lw, ((c + 1) & 3) * REG + p * 1024, (p & 1) ? vO : vE, rwd, dsb, p4_row(2 * c + (p >> 2), p & 3) * 1024);
            };
            float mean[4], sig[4], kk_[4];
            asm volatile("" : "+v"(sa[0]), "+v"(sa[1]), "+v"(sa[2]), "+v"(sa[3]), "+v"(sb_[0]), "+v"(sb_[1]), "+v"(sb_[2]), "+v"(sb_[3]));
            {
                float v[4];
                combine_stats<4>(sa, sb_, mean, v);
                float l2e = 1.44269504088896f;
                asm volatile("" : "+s"(l2e));
#pragma unroll
                for (int t = 0; t < 4; ++t) {
                    const float vv = v[t] + EPS, r = __builtin_amdgcn_rsqf(vv);
                    sig[t] = vv * r; kk_[t] = l2e * r;
                }
            }
            auto glu = [&](float G, float U, float cg, float cu, float m, float sg, float kk) {
                const float e = __builtin_amdgcn_exp2f(kk * (m * cg - G));
                return (U - m * cu) * __builtin_amdgcn_rcpf(sg * e + sg);
            };
            const unsigned ho = WS_H + layer * 7 * MB, hw = 14 * (CT >> 3);
            {
                u32x4 o[4];
                const f32x4 cg0 = *(LAS f32x4*)(L + T_CS3 + (0 * 4 + q) * 16), cu0 = *(LAS f32x4*)(L + T_CS3 + (1 * 4 + q) * 16);
                const f32x4 cg1 = *(LAS f32x4*)(L + T_CS3 + (2 * 4 + q) * 16), cu1 = *(LAS f32x4*)(L + T_CS3 + (3 * 4 + q) * 16);
                sfor<4>([&](auto T) {
                    constexpr int t = CI(T);
                    float h0[4], h1[4];
                    sfor<4>([&](auto R) {
                        constexpr int r = CI(R);
                        h0[r] = glu(a3[0][t][r], a3[1][t][r], cg0[r], cu0[r], mean[t], sig[t], kk_[t]);
                        h1[r] = glu(a3[2][t][r], a3[3][t][r], cg1[r], cu1[r], mean[t], sig[t], kk_[t]);
                        asm volatile("" : "+v"(h0[r]), "+v"(h1[r]));
                        dma_p4(t >> 1, 4 * (t & 1) + r);
                        SCHED();
                    });
                    o[t] = gather8(pk(h0[0], h0[1]), pk(h0[2], h0[3]), pk(h1[0], h1[1]), pk(h1[2], h1[3]));
                });
#pragma unroll
                for (int t = 0; t < 4; ++t) store16(o[t], rw, ho + ((sb0 + t) * 56 + hw + (CT & 7)) * 1024 + vo);
                SCHED();
            }
            {
                const f32x4 cg = *(LAS f32x4*)(L + T_CS3 + (4 * 4 + q) * 16), cu = *(LAS f32x4*)(L + T_CS3 + (5 * 4 + q) * 16);
                const f32x4 mg = *(LAS f32x4*)(L + T_CS3 + (6 * 4 + (q & 1)) * 16), mu = *(LAS f32x4*)(L + T_CS3 + (6 * 4 + 2 + (q & 1)) * 16);
                unsigned H[4][2], M[2][2];
                sfor<4>([&](auto T) {
                    constexpr int t = CI(T);
                    float h[4];
                    if constexpr (t == 2) {
                        wait_vm<8>();
                        bar();
                        publish(rw, 6 * layer + 3, blk, tag + 3, w);
                        SCHED();
                    }
#pragma unroll
                    for (int r = 0; r < 4; ++r) h[r] = glu(a3[4][t][r], a3[5][t][r], cg[r], cu[r], mean[t], sig[t], kk_[t]);
                    H[t][0] = pk(h[0], h[1]); H[t][1] = pk(h[2], h[3]);
                    asm volatile("" : "+v"(H[t][0]), "+v"(H[t][1]));
#pragma unroll
                    for (int r = 0; r < 4; ++r) dma_p4(2 + (t >> 1), 4 * (t & 1) + r);
                    SCHED();
                });
#pragma unroll
                for (int p = 0; p < 2; ++p) {
                    const float m = q < 2 ? mean[2 * p] : mean[2 * p + 1], sg = q < 2 ? sig[2 * p] : sig[2 * p + 1], kk = q < 2 ? kk_[2 * p] : kk_[2 * p + 1];
                    float h[4];
#pragma unroll
                    for (int r = 0; r < 4; ++r) {
                        auto sw = __builtin_amdgcn_permlane32_swap(__float_as_uint(a3[6][2 * p][r]), __float_as_uint(a3[6][2 * p + 1][r]), false, false);
                        h[r] = glu(__uint_as_float(sw[0]), __uint_as_float(sw[1]), mg[r], mu[r], m, sg, kk);
                    }
                    M[p][0] = pk(h[0], h[1]); M[p][1] = pk(h[2], h[3]);
                }
                const unsigned be = 3 * (CT & 7) + (q & 1), bm = 3 * (CT & 7) + 2;
                const unsigned hb = ho + ((sb0 + (q >> 1)) * 56 + hw + 8 + (be >> 2)) * 1024 + ((be & 3) * 16 + i16) * 16;
                store16(gather8(H[0][0], H[0][1], H[1][0], H[1][1]), rw, hb);
                store16(gather8(H[2][0], H[2][1], H[3][0], H[3][1]), rw, hb + 2 * 56 * 1024);
                store16(gather8(M[0][0], M[0][1], M[1][0], M[1][1]), rw, ho + ((sb0 + q) * 56 + hw + 8 + (bm >> 2)) * 1024 + ((bm & 3) * 16 + i16) * 16);
                SCHED();
            }

            poll8(rw, 6 * layer + 3, blk_p3(rt >> 2, 8 * w + (lane_id() & 7)), tag + 3, dead);
            bar();
            publish(rw, 6 * layer + 4, blk, tag + 4, w);
            unsigned so[4];
#pragma unroll
            for (int d = 0; d < 4; ++d) so[d] = ho + ((4 * rt + ((ws_ + d) & 3)) * 56 + 14 * ws_) * 1024;
            bf16x8 hact[6][4];
            auto load_h = [&](int u, int d) { aload(hact[u % 6][d], vo, rw, so[d], u * 1024); };
#pragma unroll
            for (int u = 0; u < 5; ++u)
#pragma unroll
                for (int d = 0; d < 4; ++d) load_h(u, d);
            f32x4 acc[4][4];
#pragma unroll
            for (int b = 0; b < 4; ++b)
#pragma unroll
                for (int d = 0; d < 4; ++d) acc[b][d] = (f32x4){0.f, 0.f, 0.f, 0.f};
            unsigned wb_[2][4];
            const int l3 = lane_id(), q3 = l3 >> 4, qq3 = (l3 >> 2) & 3, p3 = l3 & 3;
#pragma unroll
            for (int b = 0; b < 4; ++b) {
                wb_[0][b] = w * 8192 + (8 * q3 + qq3) * 128 + (((2 * b + (p3 >> 1)) ^ ((qq3 & 2) + 4 * (q3 & 1))) * 16) + 8 * (p3 & 1);
                wb_[1][b] = wb_[0][b] + 2 * REG;
            }
            auto wd_off = [&](int u, int b) { return wb_[(((u >> 1) + 1) & 3) >> 1][b] + (((u >> 1) + 1) & 1) * REG + (u & 1) * 4096; };
            bf16x8 wf4[2][4];
#pragma unroll
            for (int b = 0; b < 4; ++b) wf4[0][b] = ldsT(L, wd_off(0, b));
            u32x2 flb;
            sfor<14>([&](auto U) {
                constexpr int u = CI(U);
                wait4<p4_wait(u)>(hact[u % 6][0], hact[u % 6][1], hact[u % 6][2], hact[u % 6][3]);
                if constexpr (u == 1) {
                    aload2(flb, (unsigned)blk_p3(rt >> 2, 8 * w + (lane_id() & 7)) * 256u, rw, WS_FLAGS + (6 * layer + 4) * 65536u);
                    SCHED();
                }
                if constexpr (u == 3) {
                    asm volatile("" : "+v"(flb));
                    if (!__all((((u64)flb[1] << 32) | flb[0]) == tag + 4)) poll8(rw, 6 * layer + 4, blk_p3(rt >> 2, 8 * w + (lane_id() & 7)), tag + 4, dead);
                    SCHED();
                }
                sfor<8>([&](auto J) {
                    constexpr int j = CI(J), b = j >> 1, d0 = 2 * (j & 1);
                    acc[b][d0] = mfma(wf4[u & 1][b], hact[u % 6][d0], acc[b][d0]);
                    acc[b][d0 + 1] = mfma(wf4[u & 1][b], hact[u % 6][d0 + 1], acc[b][d0 + 1]);
                    SCHED();
                    if constexpr (j < 4 && u < 13) wf4[(u + 1) & 1][j] = ldsT(L, wd_off(u + 1, j));
                    if constexpr ((j & 1) == 0) {
                        constexpr int p = 4 * ((u + 1) & 1) + (j >> 1);
                        if constexpr (u >= 1 && u <= 6) dma_p4(((u - 1) >> 1) + 4, p);
                    } else if constexpr (u <= 8) load_h(u + 5, j >> 1);
                    SCHED();
                });
            });
            if (layer == 0) dma_p1_all();
            LAS char* x3 = L + w * 8192 + lane * 16 + 3 * REG;
            LAS char* xm = L + w * 8192 + lane * 16 + LDS_M;
            asm volatile("" : "+v"(x3), "+v"(xm));
#pragma unroll
            for (int d = 1; d < 4; ++d)
#pragma unroll
                for (int b = 0; b < 4; ++b)
                    *(LAS f32x4*)((d < 3 ? x3 : xm) + (d < 3 ? (d - 1) * 4096 : 0) + b * 1024) = acc[b][d];
            wait_lds();
            bar();
            if (layer == 0) dma_p2n(4, 6);
            f32x4 v[4], t[3][4];
#pragma unroll
            for (int d = 1; d < 4; ++d) {
                LAS char* src = L + ((w - d) & 3) * 8192 + lane * 16 + (d < 3 ? 3 * REG + (d - 1) * 4096 : LDS_M);
                asm volatile("" : "+v"(src));
#pragma unroll
                for (int b = 0; b < 4; ++b) t[d - 1][b] = *(LAS f32x4*)(src + b * 1024);
            }
            SCHED();
#pragma unroll
            for (int b = 0; b < 4; ++b) v[b] = ((acc[b][0] + t[0][b]) + t[1][b]) + t[2][b];
            if (layer == 0) {
                finish_strip(v, false, WS_X2, WS_ST + 2 * ST_BYTES);
                wait_vm<0>();
                bar();
                publish(rw, 5, blk, tag + 5, w);
                dma_p2n(6, 8);
            } else {
                finish_strip(v, true, 0, 0);
            }
        }
        if (layer == 1) break;
        phase1(std::false_type{});
    }
    asm volatile("fnet_glu_code_end:");
}

}

extern "C" void kernel_launch(void* const* d_in, const int* in_sizes, int n_in,
                              void* d_out, int out_size, void* d_ws, size_t ws_size,
                              hipStream_t stream) {
    (void)in_sizes; (void)n_in; (void)out_size; (void)ws_size;
    (void)hipFuncSetAttribute((const void*)fn::fnet_glu_kernel, hipFuncAttributeMaxDynamicSharedMemorySize, fn::LDS_BYTES);
    fn::fnet_glu_kernel<<<256, 256, fn::LDS_BYTES, stream>>>((const char*)d_in[0], (const char*)d_in[1], (const char*)d_in[2],
        (const char*)d_in[3], (const char*)d_in[4], (const char*)d_in[6], (const char*)d_in[7], (const char*)d_in[8],
        (char*)d_out, (char*)d_ws);
}
```

```cpp
#include <hip/hip_runtime.h>
#include <hip/hip_bf16.h>
#include <stdint.h>
#include <type_traits>
#include <utility>

#define DI __device__ __forceinline__
#define LAS __attribute__((address_space(3)))
#define GAS __attribute__((address_space(1)))
typedef short bf16x8 __attribute__((ext_vector_type(8)));
typedef short s16x4 __attribute__((ext_vector_type(4)));
typedef float f32x4 __attribute__((ext_vector_type(4)));
typedef float f32x2 __attribute__((ext_vector_type(2)));
typedef float f32x16 __attribute__((ext_vector_type(16)));
typedef unsigned u32x4 __attribute__((ext_vector_type(4)));
typedef unsigned u32x2 __attribute__((ext_vector_type(2)));
typedef __bf16 bf16x2 __attribute__((ext_vector_type(2)));
typedef unsigned long long u64;
typedef __amdgpu_buffer_rsrc_t rsrc_t;

namespace fn {

constexpr int S = 2048, D = 512, F = 1792;
constexpr unsigned MB = 1u << 20;
constexpr unsigned WS_FLAGS = 4096;
constexpr unsigned WS_ABT = 1 * MB;
constexpr unsigned WS_X1 = 9 * MB;
constexpr unsigned WS_X2 = 13 * MB;
constexpr unsigned WS_H = 15 * MB;
constexpr unsigned WS_ST = 29 * MB;
constexpr unsigned WS_PART = 32 * MB;
constexpr unsigned ST_BYTES = 128 * 1024;

constexpr unsigned REG = 32768;
constexpr unsigned LDS_M = 4 * REG;
constexpr unsigned T_RES = 4096;
constexpr unsigned T_STAT = 6144;
constexpr unsigned T_CS1 = LDS_M + 6400;
constexpr unsigned T_CS3 = LDS_M + 8192 + 6400;
constexpr unsigned T_CS3P = LDS_M + 16384 + 6400;
constexpr unsigned T_DUMMY = LDS_M + 24576 + 6400;
constexpr unsigned LDS_BYTES = 163840;
constexpr unsigned SPIN_LIMIT = 200000;
constexpr float EPS = 1e-5f;

DI rsrc_t mk(const void* p, unsigned bytes) { return __builtin_amdgcn_make_buffer_rsrc((void*)p, 0, bytes, 0x00020000); }
DI void dma(unsigned lds, int LI, unsigned voff, rsrc_t r, unsigned sb, int SI) {
    unsigned t;
    asm volatile("s_add_u32 m0, %1, %5\n\ts_add_u32 %0, %4, %6\n\tbuffer_load_dwordx4 %2, %3, %0 offen lds"
                 : "=&s"(t) : "s"(lds), "v"(voff), "s"(r), "s"(sb), "i"(LI), "i"(SI) : "m0", "scc", "memory");
}
DI void aload(bf16x8& d, unsigned voff, rsrc_t r, unsigned sb, int SI) {
    asm volatile("buffer_load_dwordx4 %0, %1, %2, %3 offen offset:%4 sc1"
                 : "=v"(d) : "v"(voff), "s"(r), "s"(sb + (SI & ~4095)), "i"(SI & 4095) : "memory");
}
DI void aload2(u32x2& d, unsigned voff, rsrc_t r, unsigned sb) {
    asm volatile("buffer_load_dwordx2 %0, %1, %2, %3 offen sc1" : "=v"(d) : "v"(voff), "s"(r), "s"(sb) : "memory");
}
template <int... Is, class Fn> DI void sfor_impl(std::integer_sequence<int, Is...>, Fn&& f) { (f(std::integral_constant<int, Is>{}), ...); }
template <int N, class Fn> DI void sfor(Fn&& f) { sfor_impl(std::make_integer_sequence<int, N>{}, f); }
#define CI(x) decltype(x)::value
DI int lane_id() { unsigned z = 0; asm volatile("" : "+s"(z)); return (int)__builtin_amdgcn_mbcnt_hi(~0u, __builtin_amdgcn_mbcnt_lo(~0u, z)); }
DI int opq_s(int v) { asm volatile("" : "+s"(v)); return v; }
template <int N> DI void wait_vm() { asm volatile("s_waitcnt vmcnt(%0)" :: "i"(N) : "memory"); }
template <int N> DI void wait2(bf16x8& a, bf16x8& b) { asm volatile("s_waitcnt vmcnt(%2)" : "+v"(a), "+v"(b) : "i"(N) : "memory"); }
template <int N> DI void wait4(bf16x8& a, bf16x8& b, bf16x8& c, bf16x8& d) {
    asm volatile("s_waitcnt vmcnt(%4)" : "+v"(a), "+v"(b), "+v"(c), "+v"(d) : "i"(N) : "memory");
}
DI void wait_lds() { asm volatile("s_waitcnt lgkmcnt(0)" ::: "memory"); }
DI void bar() { asm volatile("" ::: "memory"); __builtin_amdgcn_s_barrier(); asm volatile("" ::: "memory"); }
#define SCHED() __builtin_amdgcn_sched_barrier(0)
DI f32x4 mfma(bf16x8 a, bf16x8 b, f32x4 c) { return __builtin_amdgcn_mfma_f32_16x16x32_bf16(a, b, c, 0, 0, 0); }
DI f32x16 mfma32(bf16x8 a, bf16x8 b, f32x16 c) { return __builtin_amdgcn_mfma_f32_32x32x16_bf16(a, b, c, 0, 0, 0); }
DI unsigned pk(float lo, float hi) { f32x2 v = {lo, hi}; return __builtin_bit_cast(unsigned, __builtin_convertvector(v, bf16x2)); }
DI float lo16(unsigned u) { return __uint_as_float(u << 16); }
DI float hi16(unsigned u) { return __uint_as_float(u & 0xffff0000u); }
DI float sum16(float v) { auto r = __builtin_amdgcn_permlane16_swap(__float_as_uint(v), __float_as_uint(v), false, false); return __uint_as_float(r[0]) + __uint_as_float(r[1]); }
DI float sum32(float v) { auto r = __builtin_amdgcn_permlane32_swap(__float_as_uint(v), __float_as_uint(v), false, false); return __uint_as_float(r[0]) + __uint_as_float(r[1]); }
DI float sumq(float v) { return sum32(sum16(v)); }
DI u32x4 gather8(unsigned x0, unsigned x1, unsigned y0, unsigned y1) {
    auto a = __builtin_amdgcn_permlane32_swap(x0, y0, false, false);
    auto b = __builtin_amdgcn_permlane32_swap(x1, y1, false, false);
    auto c = __builtin_amdgcn_permlane16_swap(a[0], a[1], false, false);
    auto d = __builtin_amdgcn_permlane16_swap(b[0], b[1], false, false);
    return (u32x4){c[0], d[0], c[1], d[1]};
}
DI void store16(u32x4 v, rsrc_t r, unsigned voff) { __builtin_amdgcn_raw_buffer_store_b128(v, r, voff, 0, 16); }
DI bf16x8 ldsA(LAS char* L, unsigned off) { return *(LAS bf16x8*)(L + off); }
DI bf16x8 ldsT(LAS char* L, unsigned off) {
    s16x4 lo = __builtin_amdgcn_ds_read_tr16_b64_v4i16((LAS s16x4*)(L + off));
    s16x4 hi = __builtin_amdgcn_ds_read_tr16_b64_v4i16((LAS s16x4*)(L + off + 512));
    return __builtin_shufflevector(lo, hi, 0, 1, 2, 3, 4, 5, 6, 7);
}
DI int blk_p24(int rt, int ct) { return 8 * (4 * ((rt >> 1) & 7) + (ct >> 1)) + 4 * (rt >> 4) + 2 * (rt & 1) + (ct & 1); }
DI int blk_p1(int rt1, int et) { return 8 * (8 * (rt1 & 3) + (et & 7)) + 2 * (rt1 >> 2) + (et >> 3); }
DI int blk_p3(int RT, int CT) { return 8 * (8 * (RT & 3) + (CT & 7)) + 2 * (CT >> 3) + (RT >> 2); }

DI void poll8(rsrc_t rw, int phase, int prod, u64 tag, bool& dead) {
    const unsigned voff = (unsigned)prod * 256u, sb = WS_FLAGS + (unsigned)phase * 65536u;
    if (!dead) {
        unsigned spins = 0;
        for (;;) {
            u64 v;
            asm volatile("buffer_load_dwordx2 %0, %1, %2, %3 offen sc1\n\ts_waitcnt vmcnt(0)" : "=v"(v) : "v"(voff), "s"(rw), "s"(sb) : "memory");
            if (__all(v == tag)) break;
            if (++spins > SPIN_LIMIT) { dead = true; break; }
        }
    }
    asm volatile("" ::: "memory");
}
DI void publish(rsrc_t rw, int phase, int blk, u64 tag, int w) {
    if (w == 0)
        __builtin_amdgcn_raw_buffer_store_b64((u32x2){(unsigned)tag, (unsigned)(tag >> 32)}, rw, WS_FLAGS + (unsigned)(phase * 256 + blk) * 256u, 0, 16);
}


constexpr int P1_LA = 12;
constexpr int p1_issued(int ks) { return 2 * P1_LA + 2 * (ks < 16 - P1_LA ? ks : 16 - P1_LA); }
constexpr int p1_wait(int ks) { return p1_issued(ks) - 1 - (ks < P1_LA ? 2 * ks + 1 : p1_issued(ks - P1_LA + 1) - 1); }
constexpr int P2_LA = 22;
constexpr int p2_dma(int v) { return (v >= 8 && v <= 39) || (v >= 43 && v <= 50) ? 1 : 0; }
constexpr int p2_ops(int v) { return p2_dma(v) + (v + P2_LA < 64 ? 1 : 0); }
constexpr int p2_issued(int v) { int n = P2_LA; for (int j = 0; j < v; ++j) n += p2_ops(j); return n; }
constexpr int p2_wait(int v) {
    const int pa = v < P2_LA ? v : p2_issued(v - P2_LA + 1) - 1;
    const int c = (v >> 2) + 1;
    const int pd = (v & 3) == 2 && c >= 8 && c < 16 ? p2_issued(4 * (c - 6) + 3) : -1;
    return p2_issued(v) - 1 - (pa > pd ? pa : pd);
}
constexpr int P3_LA = 3;
constexpr int p3_ops(int g) { return (g + P3_LA < 16 ? 4 : 0) + (g == 8 ? 8 : 0); }
constexpr int p3_issued(int g) { int n = 4 * P3_LA; for (int j = 0; j < g; ++j) n += p3_ops(j); return n; }
constexpr int p3_wait(int g) { return p3_issued(g) - 1 - (g < P3_LA ? 4 * g + 3 : p3_issued(g - P3_LA) + 3); }
constexpr int p4_ops(int u) { return (u == 1 ? 1 : 0) + (u >= 1 && u <= 6 ? 4 : 0) + (u <= 8 ? 4 : 0); }
constexpr int p4_issued(int u) { int n = 20; for (int j = 0; j < u; ++j) n += p4_ops(j); return n; }
constexpr int p4_wait(int u) {
    int p = u < 5 ? 4 * u + 3 : p4_issued(u - 4) - 1;
    if (u == 3 && p < p4_issued(1)) p = p4_issued(1);
    if ((u & 1) && u < 13) {
        const int c = (u + 1) >> 1;
        if (c >= 4 && p4_issued(2 * c - 6) + 6 > p) p = p4_issued(2 * c - 6) + 6;
    }
    return p4_issued(u) - 1 - p;
}
constexpr int p4_row(int u, int qq) { return u < 8 ? 56 * u + 8 * qq : 56 * ((4 * (u - 8) + qq) / 3) + 32 + 8 * ((4 * (u - 8) + qq) % 3); }

template <int N> DI void combine_stats(const u32x2 (&a)[N], const u32x2 (&b)[N], float (&mean)[N], float (&var)[N]) {
    float s[N], t[N], inv = 1.0f / 512.0f;
    asm volatile("" : "+s"(inv));
#pragma unroll
    for (int i = 0; i < N; ++i) { s[i] = __uint_as_float(a[i][0]) + __uint_as_float(b[i][0]); t[i] = __uint_as_float(a[i][1]) + __uint_as_float(b[i][1]); }
#pragma unroll
    for (int i = 0; i < N; ++i) { s[i] = sum16(s[i]); t[i] = sum16(t[i]); }
#pragma unroll
    for (int i = 0; i < N; ++i) { s[i] = sum32(s[i]); t[i] = sum32(t[i]); }
#pragma unroll
    for (int i = 0; i < N; ++i) { mean[i] = s[i] * inv; var[i] = fmaxf(t[i] * inv - mean[i] * mean[i], 0.f); }
}

__global__ __launch_bounds__(256, 2) void fnet_glu_kernel(const char* __restrict__ x, const char* __restrict__ wsre, const char* __restrict__ wsim,
        const char* __restrict__ wdre, const char* __restrict__ wdim_, const char* __restrict__ wg, const char* __restrict__ wu,
        const char* __restrict__ wdn, char* __restrict__ out, char* __restrict__ ws) {
    extern __shared__ __attribute__((aligned(16))) char smem[];
    LAS char* L = (LAS char*)smem;
    int wid;
    asm volatile("s_nop 1\n\tv_readfirstlane_b32 %0, %1\n\ts_nop 4" : "=s"(wid) : "v"(threadIdx.x >> 6));
    const int blk = blockIdx.x, xcd = blk & 7, idx = blk >> 3;

    const unsigned ldsb = (unsigned)(size_t)L;
    const rsrc_t rw = mk(ws, 48 * MB);
    const int T2 = 8 * (xcd >> 2) + (idx >> 2), E2 = idx & 3, j2 = xcd & 3;
    const int rt = 2 * T2 + (j2 >> 1), ct = 2 * E2 + (j2 & 1);
    const int rt1 = 4 * (xcd >> 1) + (idx >> 3), et = 8 * (xcd & 1) + (idx & 7);
    const int RT = 4 * (xcd & 1) + (idx >> 3), CT = 8 * (xcd >> 1) + (idx & 7);
    {
        u64 pc;
        int len;
        asm volatile("s_getpc_b64 %0\n1:\n\ts_mov_b32 %1, fnet_glu_code_end-1b" : "=s"(pc), "=s"(len));
        const rsrc_t rc = mk((const void*)pc, len > 0 ? (unsigned)len : 0u);
        const unsigned o = (unsigned)((idx * 4 + wid) & 7) * 8192u + lane_id() * 128u, l = ldsb + T_DUMMY + 768;
        asm volatile("s_mov_b32 m0, %0\n\ts_nop 0\n\tbuffer_load_dword %1, %2, 0 offen lds" :: "s"(l), "v"(o), "s"(rc) : "m0", "memory");
    }

    auto p1_rsrc = [&](unsigned bytes) { return mk(((et >> 3) ? wdim_ : wdre) + (size_t)(et & 7) * 64 * 1024, bytes); };
    auto dma_p1 = [&](int j, int p, rsrc_t r, unsigned v0, unsigned lw, unsigned sb) {
        dma(lw, (32 * j + p) * 1024, v0 ^ (16 * p), r, sb, (32 * j + p) * 1024);
    };
    const unsigned p3_slot = (CT & 3) == 0 ? 0x6543210u : (CT & 3) == 1 ? 0x2165470u : (CT & 3) == 2 ? 0x2765410u : 0x6745210u;
    const unsigned p3_chunk = (CT & 3) == 0 ? 0x66543210u : (CT & 3) == 1 ? 0x14326650u : (CT & 3) == 2 ? 0x54326610u : 0x56342210u;
    auto p3_voff = [&](int ln, int up, int odd) -> unsigned {
        const unsigned p = (ln & 7) ^ ((2 * ((ln >> 4) & 1) + 4 * odd) ^ up);
        return (ln >> 3) * 3584 + 16 * ((p3_chunk >> (4 * p)) & 7);
    };
    auto dma_p3 = [&](int j, int p, unsigned v, unsigned lw, unsigned sb) {
        dma(lw, j * REG + p * 1024, v, mk((j >> 1) ? wu : wg, D * F * 2), sb, (256 * (j & 1) + 8 * p) * 3584);
    };
    auto dma_p2n = [&](int c0, int c1) {
        const int w = opq_s(wid), ln = lane_id();
        const rsrc_t r = mk(((j2 >> 1) ? wsim : wsre) + ((size_t)128 * T2 * S + 1024 * (j2 & 1)) * 2, (128 * S - 1024 * (j2 & 1)) * 2);
        const unsigned vE = (ln >> 3) * 4096 + (((ln & 7) ^ (ln >> 4)) * 16), vO = vE ^ 64;
#pragma unroll
        for (int c = c0; c < c1; ++c)
#pragma unroll
            for (int p = 0; p < 4; ++p) dma(ldsb + w * 4096, (c & 7) * 16384 + p * 1024, (p & 1) ? vO : vE, r, 32 * w * 4096, p * 32768 + c * 128);
    };
    auto dma_p1_all = [&]() {
        const int w = opq_s(wid), ln = lane_id();
        const rsrc_t r = p1_rsrc(65536);
        const unsigned v0 = (unsigned)(ln * 16) ^ (128 * (w & 1)), lw = ldsb + w * 8192;
#pragma unroll
        for (int j = 0; j < 2; ++j)
#pragma unroll
            for (int p = 0; p < 8; ++p) dma_p1(j, p, r, v0, lw, w * 8192);
    };
    auto warm_ffn = [&]() {
        const int w = opq_s(wid), ln = lane_id();
        const unsigned o = blk * 7168u + (ln < 56 ? ln : 55) * 128u, l = ldsb + T_DUMMY + w * 256;
        auto touch = [&](rsrc_t r) {
            asm volatile("s_mov_b32 m0, %0\n\ts_nop 0\n\tbuffer_load_dword %1, %2, 0 offen lds" :: "s"(l), "v"(o), "s"(r) : "m0", "memory");
        };
        if (w == 0) touch(mk(wg, D * F * 2));
        if (w == 1) touch(mk(wu, D * F * 2));
        if (w == 2) touch(mk(wdn, D * F * 2));
    };
    dma_p1_all();

    u32x2 ev;
    aload2(ev, 0u, rw, 0u);
    u64 epoch = 0;
    bool dead = false;
    const bf16x8 ones = {0x3F80, 0x3F80, 0x3F80, 0x3F80, 0x3F80, 0x3F80, 0x3F80, 0x3F80};

        auto phase1 = [&](auto first_tag) {
            constexpr bool FIRST = decltype(first_tag)::value;
            constexpr int layer = FIRST ? 0 : 1;
            const int w = opq_s(wid), lane = lane_id(), i16 = lane & 15, q = lane >> 4;
            const int sbg = 8 * rt1 + 2 * w;
            bf16x8 xa[2][16];
            u32x2 sa[2], sb_[2];
            f32x4 acc[2][4];
#pragma unroll
            for (int s = 0; s < 2; ++s)
#pragma unroll
                for (int e = 0; e < 4; ++e) acc[s][e] = (f32x4){0.f, 0.f, 0.f, 0.f};
            unsigned wb[4];
#pragma unroll
            for (int k = 0; k < 4; ++k) wb[k] = i16 * 1024 + (((4 * k + q) ^ i16) * 16);
            LAS f32x2* tst = (LAS f32x2*)(L + LDS_M + w * 8192 + T_STAT);
            const float sgn = __int_as_float(opq_s((et >> 3) ? (int)0xbf800000 : 0x3f800000));
            f32x4 aS[2], aQ[2], aC = {0.f, 0.f, 0.f, 0.f};
#pragma unroll
            for (int s = 0; s < 2; ++s) { aS[s] = (f32x4){0.f, 0.f, 0.f, 0.f}; aQ[s] = aS[s]; }
            const unsigned wown = w * 16384;
            const unsigned vo = lane * 16, sbase = WS_X2 + sbg * 16384;
            if constexpr (FIRST) {
                const rsrc_t rx = mk(x, S * D * 2);
                bf16x8 xr[32];
#pragma unroll
                for (int r = 0; r < 32; ++r) aload(xr[r], vo, rx, sbg * 16384, r * 1024);
                LAS char* T0 = L + 2 * REG + w * 8192;
                sfor<2>([&](auto SB) {
                    constexpr int s = CI(SB);
                    sfor<4>([&](auto G) {
                        constexpr int g = CI(G), r0 = 16 * s + 4 * g;
                        wait4<28 - r0>(xr[r0], xr[r0 + 1], xr[r0 + 2], xr[r0 + 3]);
#pragma unroll
                        for (int r = 0; r < 4; ++r)
                            *(LAS bf16x8*)(T0 + ((4 * g + r) >> 3) * REG + ((4 * g + r) & 7) * 1024 + ((lane * 16) ^ ((4 * g + r) * 16))) = xr[r0 + r];
                    });
#pragma unroll
                    for (int ks = 0; ks < 16; ++ks)
                        xa[s][ks] = *(LAS bf16x8*)(T0 + (i16 >> 3) * REG + (i16 & 7) * 1024 + (((4 * ks + q) ^ i16) * 16));
                    wait_lds();
                });
                asm volatile("" : "+v"(ev));
                epoch = ((u64)(unsigned)__builtin_amdgcn_readfirstlane((int)ev[1]) << 32) | (unsigned)__builtin_amdgcn_readfirstlane((int)ev[0]);
                wait_lds();
                bar();
                dma_p2n(4, 8);
                warm_ffn();
            } else {
                poll8(rw, 5, blk_p24(2 * rt1 + (w >> 1), lane & 7), epoch + 6, dead);
#pragma unroll
                for (int s = 0; s < 2; ++s) {
                    const unsigned so = WS_ST + 2 * ST_BYTES + (sbg + s) * 1024;
                    aload2(sa[s], (2 * q * 16 + i16) * 8, rw, so);
                    aload2(sb_[s], ((2 * q + 1) * 16 + i16) * 8, rw, so);
                }
#pragma unroll
                for (int ks = 0; ks < P1_LA; ++ks)
#pragma unroll
                    for (int s = 0; s < 2; ++s) aload(xa[s][ks], vo, rw, sbase, s * 16384 + ks * 1024);
                wait2<p1_wait(0)>(xa[0][0], xa[1][0]);
                asm volatile("" : "+v"(sa[0]), "+v"(sa[1]), "+v"(sb_[0]), "+v"(sb_[1]));
            }
            bf16x8 wf[2][4], wo;
#pragma unroll
            for (int e = 0; e < 4; ++e) wf[0][e] = ldsA(L, wb[0] + e * 16384);
            sfor<16>([&](auto KS) {
                constexpr int ks = CI(KS);
                if constexpr (!FIRST && ks > 0) wait2<p1_wait(ks)>(xa[0][ks], xa[1][ks]);
                if constexpr (FIRST) wo = ldsA(L, wb[ks & 3] + wown + 256 * (ks >> 2));
                sfor<4>([&](auto E) {
                    constexpr int e = CI(E);
                    acc[0][e] = mfma(xa[0][ks], wf[ks & 1][e], acc[0][e]);
                    acc[1][e] = mfma(xa[1][ks], wf[ks & 1][e], acc[1][e]);
                    SCHED();
                    if constexpr (ks < 15) wf[(ks + 1) & 1][e] = ldsA(L, wb[(ks + 1) & 3] + e * 16384 + 256 * ((ks + 1) >> 2));
                    if constexpr (!FIRST && e >= 2 && ks + P1_LA < 16) aload(xa[e - 2][ks + P1_LA], vo, rw, sbase, (e - 2) * 16384 + (ks + P1_LA) * 1024);
                    SCHED();
                });
                if constexpr (!FIRST && ks == 15) {
                    float mean[2], var[2];
                    combine_stats<2>(sa, sb_, mean, var);
#pragma unroll
                    for (int s = 0; s < 2; ++s) tst[16 * s + i16] = (f32x2){mean[s], sgn * __builtin_amdgcn_rsqf(var[s] + EPS)};
                    SCHED();
                }
                if constexpr (FIRST) {
                    aC = mfma(ones, wo, aC);
#pragma unroll
                    for (int s = 0; s < 2; ++s) { aS[s] = mfma(ones, xa[s][ks], aS[s]); aQ[s] = mfma(xa[s][ks], xa[s][ks], aQ[s]); }
                    SCHED();
                }
            });
            if constexpr (FIRST) {
                ((LAS float*)(L + T_CS1))[16 * w + i16] = aC[0];
#pragma unroll
                for (int s = 0; s < 2; ++s) {
                    const int r3 = i16 & 3;
                    float dq = r3 == 0 ? aQ[s][0] : r3 == 1 ? aQ[s][1] : r3 == 2 ? aQ[s][2] : aQ[s][3];
                    dq = (q == (i16 >> 2)) ? dq : 0.f;
                    const float mean = aS[s][0] * (1.0f / 512.0f);
                    const float var = fmaxf(sumq(dq) * (1.0f / 512.0f) - mean * mean, 0.f);
                    tst[16 * s + i16] = (f32x2){mean, sgn * __builtin_amdgcn_rsqf(var + EPS)};
                }
            }
            wait_lds();
            bar();
            dma_p2n(0, 2);
            f32x4 st0[2], st1[2];
#pragma unroll
            for (int s = 0; s < 2; ++s) {
                const f32x4 a = *(LAS f32x4*)(tst + 16 * s + 4 * q), b = *(LAS f32x4*)(tst + 16 * s + 4 * q + 2);
                st0[s] = (f32x4){a[0], a[2], b[0], b[2]}; st1[s] = (f32x4){a[1], a[3], b[1], b[3]};
            }
#pragma unroll
            for (int E = 0; E < 2; ++E) {
                const float cs0 = ((LAS float*)(L + T_CS1))[32 * E + i16], cs1 = ((LAS float*)(L + T_CS1))[32 * E + 16 + i16];
#pragma unroll
                for (int s = 0; s < 2; ++s) {
                    f32x4 v0, v1;
#pragma unroll
                    for (int r = 0; r < 4; ++r) {
                        v0[r] = (acc[s][2 * E][r] - st0[s][r] * cs0) * st1[s][r];
                        v1[r] = (acc[s][2 * E + 1][r] - st0[s][r] * cs1) * st1[s][r];
                    }
                    auto a = __builtin_amdgcn_permlane16_swap(pk(v0[0], v0[1]), pk(v1[0], v1[1]), false, false);
                    auto b = __builtin_amdgcn_permlane16_swap(pk(v0[2], v0[3]), pk(v1[2], v1[3]), false, false);
                    store16((u32x4){a[0], b[0], a[1], b[1]}, rw,
                            WS_ABT + layer * 4 * MB + (et >> 3) * 2 * MB + ((2 * (et & 7) + E) * 128 + sbg + s) * 1024 + lane * 16);
                }
            }
        };


    phase1(std::true_type{});
    for (int layer = 0; layer < 2; ++layer) {
        const u64 tag = epoch + 1 + 6 * layer;
        auto finish_strip = [&](f32x4 (&v)[4], bool last, unsigned xo, unsigned sto) {
            const int w = opq_s(wid), lane = lane_id(), i16 = lane & 15, q = lane >> 4;
            float sum = 0.f, sq = 0.f;
            u32x2 res[4];
            {
                const u32x4 r0 = *(LAS u32x4*)(L + LDS_M + w * 8192 + T_RES + lane * 16), r1 = *(LAS u32x4*)(L + LDS_M + w * 8192 + T_RES + 1024 + lane * 16);
                res[0] = (u32x2){r0[0], r0[1]}; res[1] = (u32x2){r0[2], r0[3]}; res[2] = (u32x2){r1[0], r1[1]}; res[3] = (u32x2){r1[2], r1[3]};
            }
#pragma unroll
            for (int b = 0; b < 4; ++b) {
                res[b][0] = pk(lo16(res[b][0]) + v[b][0], hi16(res[b][0]) + v[b][1]);
                res[b][1] = pk(lo16(res[b][1]) + v[b][2], hi16(res[b][1]) + v[b][3]);
                const float x0 = lo16(res[b][0]), x1 = hi16(res[b][0]), x2 = lo16(res[b][1]), x3 = hi16(res[b][1]);
                sum += (x0 + x1) + (x2 + x3);
                sq += (x0 * x0 + x1 * x1) + (x2 * x2 + x3 * x3);
            }
            const u32x4 o0 = gather8(res[0][0], res[0][1], res[1][0], res[1][1]);
            const u32x4 o1 = gather8(res[2][0], res[2][1], res[3][0], res[3][1]);
            if (last) {
                const rsrc_t rxo = mk(out, S * D * 2);
                const unsigned a = (64 * rt + 16 * w + i16) * 1024 + (64 * ct + 8 * q) * 2;
                __builtin_amdgcn_raw_buffer_store_b128(o0, rxo, a, 0, 0);
                __builtin_amdgcn_raw_buffer_store_b128(o1, rxo, a + 64, 0, 0);
            } else {
                const unsigned a = xo + ((4 * rt + w) * 16 + 2 * ct) * 1024 + lane * 16;
                store16(o0, rw, a); store16(o1, rw, a + 1024);
                *(LAS u32x4*)(L + LDS_M + w * 8192 + T_RES + lane * 16) = (u32x4){res[0][0], res[0][1], res[1][0], res[1][1]};
                *(LAS u32x4*)(L + LDS_M + w * 8192 + T_RES + 1024 + lane * 16) = (u32x4){res[2][0], res[2][1], res[3][0], res[3][1]};
                sum = sum16(sum); sq = sum16(sq);
                sum = sum32(sum); sq = sum32(sq);
                __builtin_amdgcn_raw_buffer_store_b64((u32x2){__float_as_uint(sum), __float_as_uint(sq)}, rw, sto + (((4 * rt + w) * 8 + ct) * 16 + i16) * 8, 0, 16);
            }
        };

        {
            const int w = opq_s(wid), lane = lane_id(), i16 = lane & 15, q = lane >> 4;
            const int part = j2 >> 1, half = j2 & 1, r32 = lane & 31, hh = lane >> 5;
            const rsrc_t rws = mk((part ? wsim : wsre) + ((size_t)128 * T2 * S + 1024 * half) * 2, (128 * S - 1024 * half) * 2);
            const unsigned vsE = (lane >> 3) * 4096 + (((lane & 7) ^ (lane >> 4)) * 16), vsO = vsE ^ 64;
            const int w4_ = opq_s(wid);
            const unsigned lw4 = ldsb + w4_ * 4096, wsb = 32 * w4_ * 4096;
            auto dma_p2 = [&](int c, int p) { dma(lw4, (c & 7) * 16384 + p * 1024, (p & 1) ? vsO : vsE, rws, wsb, p * 32768 + c * 128); };
            wait_vm<0>();
            bar();
            publish(rw, 6 * layer + 0, blk, tag, w);
            dma_p2n(2, 4);
            poll8(rw, 6 * layer + 0, blk_p1(8 * half + (lane & 7), 8 * part + 2 * E2 + (w >> 1)), tag, dead);
            const unsigned so0 = WS_ABT + layer * 4 * MB + part * 2 * MB + ((4 * E2 + w) * 128 + 64 * half) * 1024, vo = lane * 16;
            bf16x8 act[P2_LA + 2];
            auto load_act = [&](int v) { aload(act[v % (P2_LA + 2)], vo, rw, so0, v * 1024); };
#pragma unroll
            for (int v = 0; v < P2_LA; ++v) load_act(v);
            f32x16 acc[4];
#pragma unroll
            for (int t = 0; t < 4; ++t)
#pragma unroll
                for (int r = 0; r < 16; ++r) acc[t][r] = 0.f;
            unsigned rb[2][4];
#pragma unroll
            for (int k = 0; k < 4; ++k) { rb[0][k] = r32 * 128 + (((2 * k + hh) ^ ((r32 >> 1) & 7)) * 16); rb[1][k] = rb[0][k] + 65536; }
            auto ws_frag = [&](int n) {
                const int v = n >> 2, t = n & 3, c = v >> 2;
                return ldsA(L, rb[(c & 7) >> 2][v & 3] + (c & 3) * 16384 + t * 4096);
            };
            const int ws_ = opq_s(wid);
            const unsigned lw = ldsb + ws_ * 8192, g_sb = 64 * ws_ * 3584 + 112 * CT;
            unsigned gg0 = 0, gg1 = 0;
            bf16x8 wf[8];
#pragma unroll
            for (int n = 0; n < 6; ++n) wf[n] = ws_frag(n);
            sfor<64>([&](auto V) {
                constexpr int v = CI(V);
                asm volatile("s_waitcnt vmcnt(%1)" : "+v"(act[v % (P2_LA + 2)]) : "i"(p2_wait(v)) : "memory");
                if constexpr (v == 43) { const int l2 = lane_id(); gg0 = p3_voff(l2, 0, 0); gg1 = p3_voff(l2, 0, 1); }
                sfor<4>([&](auto TB) {
                    constexpr int t = CI(TB), n = 4 * v + t;
                    acc[t] = mfma32(act[v % (P2_LA + 2)], wf[n & 7], acc[t]);
                    SCHED();
                    if constexpr ((v & 3) == 2 && t == 2 && v < 60) bar();
                    if constexpr (n + 6 < 256) wf[(n + 6) & 7] = ws_frag(n + 6);
                    if constexpr (t == 1 && p2_dma(v)) {
                        if constexpr (v <= 39) dma_p2((v >> 2) + 6, v & 3);
                        else dma_p3(0, v - 43, ((v - 43) & 1) ? gg1 : gg0, lw, g_sb);
                    }
                    if constexpr (t == 3 && v + P2_LA < 64) load_act(v + P2_LA);
                    SCHED();
                });
            });
            const unsigned pgrp = WS_PART + layer * 8 * MB + (T2 * 4 + E2) * 131072;
            const int lx = lane_id(), i16x = lx & 15, tup = (lx >> 4) & 1, hx = lx >> 5;
#pragma unroll
            for (int a = 0; a < 2; ++a) {
                const int owner = 2 * a + (w >> 1);
                if (owner != j2) {
                    const unsigned po = pgrp + owner * 32768 + j2 * 8192 + (w & 1) * 1024 + tup * 2048 + (i16x + 16 * hx) * 16;
#pragma unroll
                    for (int u = 0; u < 2; ++u)
#pragma unroll
                        for (int qh = 0; qh < 2; ++qh) {
                            const f32x16& c = acc[2 * a + u];
                            store16((u32x4){pk(c[4 * qh], c[4 * qh + 1]), pk(c[4 * qh + 2], c[4 * qh + 3]),
                                            pk(c[8 + 4 * qh], c[8 + 4 * qh + 1]), pk(c[8 + 4 * qh + 2], c[8 + 4 * qh + 3])}, rw, po + u * 4096 + qh * 512);
                        }
                }
            }
#pragma unroll
            for (int a = 0; a < 2; ++a) {
                if (2 * a + (w >> 1) == j2) {
#pragma unroll
                    for (int u = 0; u < 2; ++u)
#pragma unroll
                        for (int g = 0; g < 4; ++g) {
                            const f32x16& c = acc[2 * a + u];
                            *(LAS f32x4*)(L + LDS_M + (2 * u + tup) * 8192 + (2 * (w & 1) + (g >> 1)) * 1024 + (i16x + 16 * (2 * (g & 1) + hx)) * 16) =
                                (f32x4){c[4 * g], c[4 * g + 1], c[4 * g + 2], c[4 * g + 3]};
                        }
                }
            }
            wait_vm<0>();
            wait_lds();
            bar();
            publish(rw, 6 * layer + 1, blk, tag + 1, w);
            const unsigned gu0 = p3_voff(lx, 1, 0), gu1 = p3_voff(lx, 1, 1), vx = lx * 16;
#pragma unroll
            for (int n = 0; n < 8; ++n) dma_p3(1, n, (n & 1) ? gg1 : gg0, lw, g_sb);
#pragma unroll
            for (int n = 0; n < 3; ++n) dma_p3(2, n, (n & 1) ? gu1 : gu0, lw, g_sb);
            if (layer == 0) {
                u32x2 res0[4];
                const rsrc_t rx = mk(x, S * D * 2);
                const unsigned vr = (64 * rt + 16 * w + i16x) * 1024 + (64 * ct + 4 * (lx >> 4)) * 2;
#pragma unroll
                for (int b = 0; b < 4; ++b) aload2(res0[b], vr + b * 32, rx, 0u);
                asm volatile("s_waitcnt vmcnt(0)" : "+v"(res0[0]), "+v"(res0[1]), "+v"(res0[2]), "+v"(res0[3]) :: "memory");
                *(LAS u32x4*)(L + LDS_M + w * 8192 + T_RES + vx) = (u32x4){res0[0][0], res0[0][1], res0[1][0], res0[1][1]};
                *(LAS u32x4*)(L + LDS_M + w * 8192 + T_RES + 1024 + vx) = (u32x4){res0[2][0], res0[2][1], res0[3][0], res0[3][1]};
            }
            poll8(rw, 6 * layer + 1, (blk & ~3) | (lx & 3), tag + 1, dead);
            bf16x8 pv[3][2];
#pragma unroll
            for (int d = 1; d < 4; ++d)
#pragma unroll
                for (int h = 0; h < 2; ++h) aload(pv[d - 1][h], vx, rw, pgrp + j2 * 32768 + ((j2 + d) & 3) * 8192 + w * 2048, h * 1024);
            f32x4 v[4];
#pragma unroll
            for (int b = 0; b < 4; ++b) v[b] = *(LAS f32x4*)(L + LDS_M + w * 8192 + b * 1024 + vx);
            asm volatile("s_waitcnt vmcnt(0)" : "+v"(pv[0][0]), "+v"(pv[0][1]), "+v"(pv[1][0]), "+v"(pv[1][1]), "+v"(pv[2][0]), "+v"(pv[2][1]) :: "memory");
#pragma unroll
            for (int n = 3; n < 8; ++n) dma_p3(2, n, (n & 1) ? gu1 : gu0, lw, g_sb);
#pragma unroll
            for (int d = 0; d < 3; ++d)
#pragma unroll
                for (int h = 0; h < 2; ++h) {
                    const u32x4 p = __builtin_bit_cast(u32x4, pv[d][h]);
                    v[2 * h] += (f32x4){lo16(p[0]), hi16(p[0]), lo16(p[1]), hi16(p[1])};
                    v[2 * h + 1] += (f32x4){lo16(p[2]), hi16(p[2]), lo16(p[3]), hi16(p[3])};
                }
            finish_strip(v, false, WS_X1 + layer * 2 * MB, WS_ST + layer * ST_BYTES);
            wait_vm<0>();
            bar();
            publish(rw, 6 * layer + 2, blk, tag + 2, w);
#pragma unroll
            for (int n = 0; n < 8; ++n) dma_p3(3, n, (n & 1) ? gu1 : gu0, lw, g_sb);
        }

        {
            const int w = opq_s(wid), lane = lane_id(), i16 = lane & 15, q = lane >> 4;
            poll8(rw, 6 * layer + 2, blk_p24(4 * RT + w, lane & 7), tag + 2, dead);
            if (layer == 0 && blk == 0 && w == 0) {
                u64 e2 = epoch; asm volatile("" : "+s"(e2)); e2 += 64;
                __builtin_amdgcn_raw_buffer_store_b64((u32x2){(unsigned)e2, (unsigned)(e2 >> 32)}, rw, 0, 0, 16);
                wait_vm<0>();
            }
            const int sb0 = 16 * RT + 4 * w;
            u32x2 sa[4], sb_[4];
            bf16x8 act[P3_LA + 1][4];
            const unsigned vo = lane * 16, xo = WS_X1 + layer * 2 * MB + sb0 * 16384;
            auto load_x = [&](int g, int t) { aload(act[g % (P3_LA + 1)][t], vo, rw, xo, t * 16384 + g * 1024); };
#pragma unroll
            for (int g = 0; g < P3_LA; ++g)
#pragma unroll
                for (int t = 0; t < 4; ++t) load_x(g, t);
            bar();
            const int qq = (lane >> 2) & 3, p4 = lane & 3;
            unsigned tb_[7];
            {
                const unsigned f = 2 * (qq >> 1) + 4 * (q & 1), rowb = (8 * q + qq) * 128 + 8 * (p4 & 1);
#pragma unroll
                for (int b = 0; b < 6; ++b) tb_[b] = (b & 1) * 65536 + rowb + ((((p3_slot >> (4 * (2 * (b >> 1) + (p4 >> 1)))) & 7) ^ f ^ (b & 1)) * 16);
                tb_[6] = (p4 >> 1) * 65536 + rowb + ((((p3_slot >> 24) & 7) ^ f ^ (p4 >> 1)) * 16);
            }
            if (layer == 0) {
                f32x4 ac[7];
#pragma unroll
                for (int b = 0; b < 7; ++b) ac[b] = (f32x4){0.f, 0.f, 0.f, 0.f};
#pragma unroll
                for (int k = 0; k < 4; ++k)
#pragma unroll
                    for (int b = 0; b < 7; ++b) ac[b] = mfma(ldsT(L, tb_[b] + w * 16384 + k * 4096), ones, ac[b]);
#pragma unroll
                for (int b = 0; b < 7; ++b) *(LAS f32x4*)(L + T_CS3P + ((w * 7 + b) * 4 + q) * 16) = ac[b];
                wait_lds();
                bar();
                if (w == 0) {
                    const int l = lane < 28 ? lane : 27;
                    f32x4 s = *(LAS f32x4*)(L + T_CS3P + l * 16);
#pragma unroll
                    for (int k = 1; k < 4; ++k) s += *(LAS f32x4*)(L + T_CS3P + (k * 28 + l) * 16);
                    *(LAS f32x4*)(L + T_CS3 + l * 16) = s;
                }
            }
            auto w_lo = [&](int ks, int b) { return __builtin_amdgcn_ds_read_tr16_b64_v4i16((LAS s16x4*)(L + tb_[b] + ks * 4096)); };
            auto w_hi = [&](int ks, int b) { return __builtin_amdgcn_ds_read_tr16_b64_v4i16((LAS s16x4*)(L + tb_[b] + ks * 4096 + 512)); };
            const unsigned sto = WS_ST + layer * ST_BYTES + sb0 * 1024, sv0 = (2 * q * 16 + i16) * 8;
            f32x4 a3[7][4];
#pragma unroll
            for (int b = 0; b < 7; ++b)
#pragma unroll
                for (int t = 0; t < 4; ++t) a3[b][t] = (f32x4){0.f, 0.f, 0.f, 0.f};
            bf16x8 wf[7];
#pragma unroll
            for (int b = 0; b < 6; ++b) wf[b] = ldsT(L, tb_[b]);
            sfor<16>([&](auto KS) {
                constexpr int ks = CI(KS), sl = ks % (P3_LA + 1);
                wait4<p3_wait(ks)>(act[sl][0], act[sl][1], act[sl][2], act[sl][3]);
                sfor<7>([&](auto B) {
                    constexpr int b = CI(B), pb = b == 0 ? 6 : b - 1, pks = b == 0 ? ks : ks + 1;
                    s16x4 lo, hi;
                    a3[b][0] = mfma(wf[b], act[sl][0], a3[b][0]);
                    SCHED();
                    if constexpr (pks < 16) lo = w_lo(pks, pb);
                    SCHED();
                    a3[b][1] = mfma(wf[b], act[sl][1], a3[b][1]);
                    SCHED();
                    if constexpr (pks < 16) { hi = w_hi(pks, pb); wf[pb] = __builtin_shufflevector(lo, hi, 0, 1, 2, 3, 4, 5, 6, 7); }
                    SCHED();
                    a3[b][2] = mfma(wf[b], act[sl][2], a3[b][2]);
                    SCHED();
                    if constexpr (b >= 1 && b <= 4 && ks + P3_LA < 16) load_x(ks + P3_LA, b - 1);
                    SCHED();
                    a3[b][3] = mfma(wf[b], act[sl][3], a3[b][3]);
                    SCHED();
                    if constexpr (ks == 8 && b == 6) {
#pragma unroll
                        for (int t = 0; t < 4; ++t) { aload2(sa[t], sv0, rw, sto + t * 1024); aload2(sb_[t], sv0 + 128, rw, sto + t * 1024); }
                        SCHED();
                    }
                });
            });
            wait_lds();
            bar();

            const int l2 = lane_id();
            const unsigned vE = (l2 >> 3) * 1024 + (((l2 & 7) ^ ((l2 >> 3) & 2)) * 16), vO = vE ^ 64;
            const int ws_ = opq_s(wid);
            const unsigned lw = ldsb + ws_ * 8192, dsb = 448 * ws_ * 1024;
            const rsrc_t rwd = mk(wdn + 128 * ct, F * D * 2 - 128 * ct);
            auto dma_p4 = [&](int c, int p) {
                dma(lw, ((c + 1) & 3) * REG + p * 1024, (p & 1) ? vO : vE, rwd, dsb, p4_row(2 * c + (p >> 2), p & 3) * 1024);
            };
            float mean[4], sig[4], kk_[4];
            asm volatile("" : "+v"(sa[0]), "+v"(sa[1]), "+v"(sa[2]), "+v"(sa[3]), "+v"(sb_[0]), "+v"(sb_[1]), "+v"(sb_[2]), "+v"(sb_[3]));
            {
                float v[4];
                combine_stats<4>(sa, sb_, mean, v);
                float l2e = 1.44269504088896f;
                asm volatile("" : "+s"(l2e));
#pragma unroll
                for (int t = 0; t < 4; ++t) {
                    const float vv = v[t] + EPS, r = __builtin_amdgcn_rsqf(vv);
                    sig[t] = vv * r; kk_[t] = l2e * r;
                }
            }
            auto glu = [&](float G, float U, float cg, float cu, float m, float sg, float kk) {
                const float e = __builtin_amdgcn_exp2f(kk * (m * cg - G));
                return (U - m * cu) * __builtin_amdgcn_rcpf(sg * e + sg);
            };
            const unsigned ho = WS_H + layer * 7 * MB, hw = 14 * (CT >> 3);
            {
                u32x4 o[4];
                const f32x4 cg0 = *(LAS f32x4*)(L + T_CS3 + (0 * 4 + q) * 16), cu0 = *(LAS f32x4*)(L + T_CS3 + (1 * 4 + q) * 16);
                const f32x4 cg1 = *(LAS f32x4*)(L + T_CS3 + (2 * 4 + q) * 16), cu1 = *(LAS f32x4*)(L + T_CS3 + (3 * 4 + q) * 16);
                sfor<4>([&](auto T) {
                    constexpr int t = CI(T);
                    float h0[4], h1[4];
                    sfor<4>([&](auto R) {
                        constexpr int r = CI(R);
                        h0[r] = glu(a3[0][t][r], a3[1][t][r], cg0[r], cu0[r], mean[t], sig[t], kk_[t]);
                        h1[r] = glu(a3[2][t][r], a3[3][t][r], cg1[r], cu1[r], mean[t], sig[t], kk_[t]);
                        asm volatile("" : "+v"(h0[r]), "+v"(h1[r]));
                        dma_p4(t >> 1, 4 * (t & 1) + r);
                        SCHED();
                    });
                    o[t] = gather8(pk(h0[0], h0[1]), pk(h0[2], h0[3]), pk(h1[0], h1[1]), pk(h1[2], h1[3]));
                });
#pragma unroll
                for (int t = 0; t < 4; ++t) store16(o[t], rw, ho + ((sb0 + t) * 56 + hw + (CT & 7)) * 1024 + vo);
                SCHED();
            }
            {
                const f32x4 cg = *(LAS f32x4*)(L + T_CS3 + (4 * 4 + q) * 16), cu = *(LAS f32x4*)(L + T_CS3 + (5 * 4 + q) * 16);
                const f32x4 mg = *(LAS f32x4*)(L + T_CS3 + (6 * 4 + (q & 1)) * 16), mu = *(LAS f32x4*)(L + T_CS3 + (6 * 4 + 2 + (q & 1)) * 16);
                unsigned H[4][2], M[2][2];
                sfor<4>([&](auto T) {
                    constexpr int t = CI(T);
                    float h[4];
                    if constexpr (t == 2) {
                        wait_vm<8>();
                        bar();
                        publish(rw, 6 * layer + 3, blk, tag + 3, w);
                        SCHED();
                    }
#pragma unroll
                    for (int r = 0; r < 4; ++r) h[r] = glu(a3[4][t][r], a3[5][t][r], cg[r], cu[r], mean[t], sig[t], kk_[t]);
                    H[t][0] = pk(h[0], h[1]); H[t][1] = pk(h[2], h[3]);
                    asm volatile("" : "+v"(H[t][0]), "+v"(H[t][1]));
#pragma unroll
                    for (int r = 0; r < 4; ++r) dma_p4(2 + (t >> 1), 4 * (t & 1) + r);
                    SCHED();
                });
#pragma unroll
                for (int p = 0; p < 2; ++p) {
                    const float m = q < 2 ? mean[2 * p] : mean[2 * p + 1], sg = q < 2 ? sig[2 * p] : sig[2 * p + 1], kk = q < 2 ? kk_[2 * p] : kk_[2 * p + 1];
                    float h[4];
#pragma unroll
                    for (int r = 0; r < 4; ++r) {
                        auto sw = __builtin_amdgcn_permlane32_swap(__float_as_uint(a3[6][2 * p][r]), __float_as_uint(a3[6][2 * p + 1][r]), false, false);
                        h[r] = glu(__uint_as_float(sw[0]), __uint_as_float(sw[1]), mg[r], mu[r], m, sg, kk);
                    }
                    M[p][0] = pk(h[0], h[1]); M[p][1] = pk(h[2], h[3]);
                }
                const unsigned be = 3 * (CT & 7) + (q & 1), bm = 3 * (CT & 7) + 2;
                const unsigned hb = ho + ((sb0 + (q >> 1)) * 56 + hw + 8 + (be >> 2)) * 1024 + ((be & 3) * 16 + i16) * 16;
                store16(gather8(H[0][0], H[0][1], H[1][0], H[1][1]), rw, hb);
                store16(gather8(H[2][0], H[2][1], H[3][0], H[3][1]), rw, hb + 2 * 56 * 1024);
                store16(gather8(M[0][0], M[0][1], M[1][0], M[1][1]), rw, ho + ((sb0 + q) * 56 + hw + 8 + (bm >> 2)) * 1024 + ((bm & 3) * 16 + i16) * 16);
                SCHED();
            }

            poll8(rw, 6 * layer + 3, blk_p3(rt >> 2, 8 * w + (lane_id() & 7)), tag + 3, dead);
            bar();
            publish(rw, 6 * layer + 4, blk, tag + 4, w);
            unsigned so[4];
#pragma unroll
            for (int d = 0; d < 4; ++d) so[d] = ho + ((4 * rt + ((ws_ + d) & 3)) * 56 + 14 * ws_) * 1024;
            bf16x8 hact[6][4];
            auto load_h = [&](int u, int d) { aload(hact[u % 6][d], vo, rw, so[d], u * 1024); };
#pragma unroll
            for (int u = 0; u < 5; ++u)
#pragma unroll
                for (int d = 0; d < 4; ++d) load_h(u, d);
            f32x4 acc[4][4];
#pragma unroll
            for (int b = 0; b < 4; ++b)
#pragma unroll
                for (int d = 0; d < 4; ++d) acc[b][d] = (f32x4){0.f, 0.f, 0.f, 0.f};
            unsigned wb_[2][4];
            const int l3 = lane_id(), q3 = l3 >> 4, qq3 = (l3 >> 2) & 3, p3 = l3 & 3;
#pragma unroll
            for (int b = 0; b < 4; ++b) {
                wb_[0][b] = w * 8192 + (8 * q3 + qq3) * 128 + (((2 * b + (p3 >> 1)) ^ ((qq3 & 2) + 4 * (q3 & 1))) * 16) + 8 * (p3 & 1);
                wb_[1][b] = wb_[0][b] + 2 * REG;
            }
            auto wd_off = [&](int u, int b) { return wb_[(((u >> 1) + 1) & 3) >> 1][b] + (((u >> 1) + 1) & 1) * REG + (u & 1) * 4096; };
            bf16x8 wf4[2][4];
#pragma unroll
            for (int b = 0; b < 4; ++b) wf4[0][b] = ldsT(L, wd_off(0, b));
            u32x2 flb;
            sfor<14>([&](auto U) {
                constexpr int u = CI(U);
                wait4<p4_wait(u)>(hact[u % 6][0], hact[u % 6][1], hact[u % 6][2], hact[u % 6][3]);
                if constexpr (u == 1) {
                    aload2(flb, (unsigned)blk_p3(rt >> 2, 8 * w + (lane_id() & 7)) * 256u, rw, WS_FLAGS + (6 * layer + 4) * 65536u);
                    SCHED();
                }
                if constexpr (u == 3) {
                    asm volatile("" : "+v"(flb));
                    if (!__all((((u64)flb[1] << 32) | flb[0]) == tag + 4)) poll8(rw, 6 * layer + 4, blk_p3(rt >> 2, 8 * w + (lane_id() & 7)), tag + 4, dead);
                    SCHED();
                }
                sfor<8>([&](auto J) {
                    constexpr int j = CI(J), b = j >> 1, d0 = 2 * (j & 1);
                    acc[b][d0] = mfma(wf4[u & 1][b], hact[u % 6][d0], acc[b][d0]);
                    acc[b][d0 + 1] = mfma(wf4[u & 1][b], hact[u % 6][d0 + 1], acc[b][d0 + 1]);
                    SCHED();
                    if constexpr (j < 4 && u < 13) wf4[(u + 1) & 1][j] = ldsT(L, wd_off(u + 1, j));
                    if constexpr ((j & 1) == 0) {
                        constexpr int p = 4 * ((u + 1) & 1) + (j >> 1);
                        if constexpr (u >= 1 && u <= 6) dma_p4(((u - 1) >> 1) + 4, p);
                    } else if constexpr (u <= 8) load_h(u + 5, j >> 1);
                    SCHED();
                });
            });
            if (layer == 0) dma_p1_all();
            LAS char* x3 = L + w * 8192 + lane * 16 + 3 * REG;
            LAS char* xm = L + w * 8192 + lane * 16 + LDS_M;
            asm volatile("" : "+v"(x3), "+v"(xm));
#pragma unroll
            for (int d = 1; d < 4; ++d)
#pragma unroll
                for (int b = 0; b < 4; ++b)
                    *(LAS f32x4*)((d < 3 ? x3 : xm) + (d < 3 ? (d - 1) * 4096 : 0) + b * 1024) = acc[b][d];
            wait_lds();
            bar();
            if (layer == 0) dma_p2n(4, 6);
            f32x4 v[4], t[3][4];
#pragma unroll
            for (int d = 1; d < 4; ++d) {
                LAS char* src = L + ((w - d) & 3) * 8192 + lane * 16 + (d < 3 ? 3 * REG + (d - 1) * 4096 : LDS_M);
                asm volatile("" : "+v"(src));
#pragma unroll
                for (int b = 0; b < 4; ++b) t[d - 1][b] = *(LAS f32x4*)(src + b * 1024);
            }
            SCHED();
#pragma unroll
            for (int b = 0; b < 4; ++b) v[b] = ((acc[b][0] + t[0][b]) + t[1][b]) + t[2][b];
            if (layer == 0) {
                finish_strip(v, false, WS_X2, WS_ST + 2 * ST_BYTES);
                wait_vm<0>();
                bar();
                publish(rw, 5, blk, tag + 5, w);
                dma_p2n(6, 8);
            } else {
                finish_strip(v, true, 0, 0);
            }
        }
        if (layer == 1) break;
        phase1(std::false_type{});
    }
    asm volatile("fnet_glu_code_end:");
}

}

extern "C" void kernel_launch(void* const* d_in, const int* in_sizes, int n_in,
                              void* d_out, int out_size, void* d_ws, size_t ws_size,
                              hipStream_t stream) {
    (void)in_sizes; (void)n_in; (void)out_size; (void)ws_size;
    (void)hipFuncSetAttribute((const void*)fn::fnet_glu_kernel, hipFuncAttributeMaxDynamicSharedMemorySize, fn::LDS_BYTES);
    fn::fnet_glu_kernel<<<256, 256, fn::LDS_BYTES, stream>>>((const char*)d_in[0], (const char*)d_in[1], (const char*)d_in[2],
        (const char*)d_in[3], (const char*)d_in[4], (const char*)d_in[6], (const char*)d_in[7], (const char*)d_in[8],
        (char*)d_out, (char*)d_ws);
}
```

```cpp
#include <hip/hip_runtime.h>
#include <hip/hip_bf16.h>
#include <stdint.h>
#include <type_traits>
#include <utility>

#define DI __device__ __forceinline__
#define LAS __attribute__((address_space(3)))
#define GAS __attribute__((address_space(1)))
typedef short bf16x8 __attribute__((ext_vector_type(8)));
typedef short s16x4 __attribute__((ext_vector_type(4)));
typedef float f32x4 __attribute__((ext_vector_type(4)));
typedef float f32x2 __attribute__((ext_vector_type(2)));
typedef float f32x16 __attribute__((ext_vector_type(16)));
typedef unsigned u32x4 __attribute__((ext_vector_type(4)));
typedef unsigned u32x2 __attribute__((ext_vector_type(2)));
typedef __bf16 bf16x2 __attribute__((ext_vector_type(2)));
typedef unsigned long long u64;
typedef __amdgpu_buffer_rsrc_t rsrc_t;

namespace fn {

constexpr int S = 2048, D = 512, F = 1792;
constexpr unsigned MB = 1u << 20;
constexpr unsigned WS_FLAGS = 4096;
constexpr unsigned WS_ABT = 1 * MB;
constexpr unsigned WS_X1 = 9 * MB;
constexpr unsigned WS_X2 = 13 * MB;
constexpr unsigned WS_H = 15 * MB;
constexpr unsigned WS_ST = 29 * MB;
constexpr unsigned WS_PART = 32 * MB;
constexpr unsigned ST_BYTES = 128 * 1024;

constexpr unsigned REG = 32768;
constexpr unsigned LDS_M = 4 * REG;
constexpr unsigned T_RES = 4096;
constexpr unsigned T_STAT = 6144;
constexpr unsigned T_CS1 = LDS_M + 6400;
constexpr unsigned T_CS3 = LDS_M + 8192 + 6400;
constexpr unsigned T_CS3P = LDS_M + 16384 + 6400;
constexpr unsigned T_DUMMY = LDS_M + 24576 + 6400;
constexpr unsigned LDS_BYTES = 163840;
constexpr unsigned SPIN_LIMIT = 200000;
constexpr float EPS = 1e-5f;

DI rsrc_t mk(const void* p, unsigned bytes) { return __builtin_amdgcn_make_buffer_rsrc((void*)p, 0, bytes, 0x00020000); }
DI void dma(unsigned lds, int LI, unsigned voff, rsrc_t r, unsigned sb, int SI) {
    unsigned t;
    asm volatile("s_add_u32 m0, %1, %5\n\ts_add_u32 %0, %4, %6\n\tbuffer_load_dwordx4 %2, %3, %0 offen lds"
                 : "=&s"(t) : "s"(lds), "v"(voff), "s"(r), "s"(sb), "i"(LI), "i"(SI) : "m0", "scc", "memory");
}
DI void aload(bf16x8& d, unsigned voff, rsrc_t r, unsigned sb, int SI) {
    asm volatile("buffer_load_dwordx4 %0, %1, %2, %3 offen offset:%4 sc1"
                 : "=v"(d) : "v"(voff), "s"(r), "s"(sb + (SI & ~4095)), "i"(SI & 4095) : "memory");
}
DI void aload2(u32x2& d, unsigned voff, rsrc_t r, unsigned sb) {
    asm volatile("buffer_load_dwordx2 %0, %1, %2, %3 offen sc1" : "=v"(d) : "v"(voff), "s"(r), "s"(sb) : "memory");
}
template <int... Is, class Fn> DI void sfor_impl(std::integer_sequence<int, Is...>, Fn&& f) { (f(std::integral_constant<int, Is>{}), ...); }
template <int N, class Fn> DI void sfor(Fn&& f) { sfor_impl(std::make_integer_sequence<int, N>{}, f); }
#define CI(x) decltype(x)::value
DI int lane_id() { unsigned z = 0; asm volatile("" : "+s"(z)); return (int)__builtin_amdgcn_mbcnt_hi(~0u, __builtin_amdgcn_mbcnt_lo(~0u, z)); }
DI int opq_s(int v) { asm volatile("" : "+s"(v)); return v; }
template <int N> DI void wait_vm() { asm volatile("s_waitcnt vmcnt(%0)" :: "i"(N) : "memory"); }
template <int N> DI void wait2(bf16x8& a, bf16x8& b) { asm volatile("s_waitcnt vmcnt(%2)" : "+v"(a), "+v"(b) : "i"(N) : "memory"); }
template <int N> DI void wait4(bf16x8& a, bf16x8& b, bf16x8& c, bf16x8& d) {
    asm volatile("s_waitcnt vmcnt(%4)" : "+v"(a), "+v"(b), "+v"(c), "+v"(d) : "i"(N) : "memory");
}
DI void wait_lds() { asm volatile("s_waitcnt lgkmcnt(0)" ::: "memory"); }
DI void bar() { asm volatile("" ::: "memory"); __builtin_amdgcn_s_barrier(); asm volatile("" ::: "memory"); }
#define SCHED() __builtin_amdgcn_sched_barrier(0)
DI f32x4 mfma(bf16x8 a, bf16x8 b, f32x4 c) { return __builtin_amdgcn_mfma_f32_16x16x32_bf16(a, b, c, 0, 0, 0); }
DI f32x16 mfma32(bf16x8 a, bf16x8 b, f32x16 c) { return __builtin_amdgcn_mfma_f32_32x32x16_bf16(a, b, c, 0, 0, 0); }
DI unsigned pk(float lo, float hi) { f32x2 v = {lo, hi}; return __builtin_bit_cast(unsigned, __builtin_convertvector(v, bf16x2)); }
DI float lo16(unsigned u) { return __uint_as_float(u << 16); }
DI float hi16(unsigned u) { return __uint_as_float(u & 0xffff0000u); }
DI float sum16(float v) { auto r = __builtin_amdgcn_permlane16_swap(__float_as_uint(v), __float_as_uint(v), false, false); return __uint_as_float(r[0]) + __uint_as_float(r[1]); }
DI float sum32(float v) { auto r = __builtin_amdgcn_permlane32_swap(__float_as_uint(v), __float_as_uint(v), false, false); return __uint_as_float(r[0]) + __uint_as_float(r[1]); }
DI float sumq(float v) { return sum32(sum16(v)); }
DI u32x4 gather8(unsigned x0, unsigned x1, unsigned y0, unsigned y1) {
    auto a = __builtin_amdgcn_permlane32_swap(x0, y0, false, false);
    auto b = __builtin_amdgcn_permlane32_swap(x1, y1, false, false);
    auto c = __builtin_amdgcn_permlane16_swap(a[0], a[1], false, false);
    auto d = __builtin_amdgcn_permlane16_swap(b[0], b[1], false, false);
    return (u32x4){c[0], d[0], c[1], d[1]};
}
DI void store16(u32x4 v, rsrc_t r, unsigned voff) { __builtin_amdgcn_raw_buffer_store_b128(v, r, voff, 0, 16); }
DI bf16x8 ldsA(LAS char* L, unsigned off) { return *(LAS bf16x8*)(L + off); }
DI bf16x8 ldsT(LAS char* L, unsigned off) {
    s16x4 lo = __builtin_amdgcn_ds_read_tr16_b64_v4i16((LAS s16x4*)(L + off));
    s16x4 hi = __builtin_amdgcn_ds_read_tr16_b64_v4i16((LAS s16x4*)(L + off + 512));
    return __builtin_shufflevector(lo, hi, 0, 1, 2, 3, 4, 5, 6, 7);
}
DI int blk_p24(int rt, int ct) { return 8 * (4 * ((rt >> 1) & 7) + (ct >> 1)) + 4 * (rt >> 4) + 2 * (rt & 1) + (ct & 1); }
DI int blk_p1(int rt1, int et) { return 8 * (8 * (rt1 & 3) + (et & 7)) + 2 * (rt1 >> 2) + (et >> 3); }
DI int blk_p3(int RT, int CT) { return 8 * (8 * (RT & 3) + (CT & 7)) + 2 * (CT >> 3) + (RT >> 2); }

DI void poll8(rsrc_t rw, int phase, int prod, u64 tag, bool& dead) {
    const unsigned voff = (unsigned)prod * 256u, sb = WS_FLAGS + (unsigned)phase * 65536u;
    if (!dead) {
        unsigned spins = 0;
        for (;;) {
            u64 v;
            asm volatile("buffer_load_dwordx2 %0, %1, %2, %3 offen sc1\n\ts_waitcnt vmcnt(0)" : "=v"(v) : "v"(voff), "s"(rw), "s"(sb) : "memory");
            if (__all(v == tag)) break;
            if (++spins > SPIN_LIMIT) { dead = true; break; }
        }
    }
    asm volatile("" ::: "memory");
}
DI void publish(rsrc_t rw, int phase, int blk, u64 tag, int w) {
    if (w == 0)
        __builtin_amdgcn_raw_buffer_store_b64((u32x2){(unsigned)tag, (unsigned)(tag >> 32)}, rw, WS_FLAGS + (unsigned)(phase * 256 + blk) * 256u, 0, 16);
}


constexpr int P1_LA = 12;
constexpr int p1_issued(int ks) { return 2 * P1_LA + 2 * (ks < 16 - P1_LA ? ks : 16 - P1_LA); }
constexpr int p1_wait(int ks) { return p1_issued(ks) - 1 - (ks < P1_LA ? 2 * ks + 1 : p1_issued(ks - P1_LA + 1) - 1); }
constexpr int P2_LA = 22;
constexpr int p2_dma(int v) { return (v >= 8 && v <= 39) || (v >= 43 && v <= 50) ? 1 : 0; }
constexpr int p2_ops(int v) { return p2_dma(v) + (v + P2_LA < 64 ? 1 : 0); }
constexpr int p2_issued(int v) { int n = P2_LA; for (int j = 0; j < v; ++j) n += p2_ops(j); return n; }
constexpr int p2_wait(int v) {
    const int pa = v < P2_LA ? v : p2_issued(v - P2_LA + 1) - 1;
    const int c = (v >> 2) + 1;
    const int pd = (v & 3) == 2 && c >= 8 && c < 16 ? p2_issued(4 * (c - 6) + 3) : -1;
    return p2_issued(v) - 1 - (pa > pd ? pa : pd);
}
constexpr int P3_LA = 3;
constexpr int p3_ops(int g) { return (g + P3_LA < 16 ? 4 : 0) + (g == 8 ? 8 : 0); }
constexpr int p3_issued(int g) { int n = 4 * P3_LA; for (int j = 0; j < g; ++j) n += p3_ops(j); return n; }
constexpr int p3_wait(int g) { return p3_issued(g) - 1 - (g < P3_LA ? 4 * g + 3 : p3_issued(g - P3_LA) + 3); }
constexpr int p4_ops(int u) { return (u == 1 ? 1 : 0) + (u >= 1 && u <= 6 ? 4 : 0) + (u <= 8 ? 4 : 0); }
constexpr int p4_issued(int u) { int n = 20; for (int j = 0; j < u; ++j) n += p4_ops(j); return n; }
constexpr int p4_wait(int u) {
    int p = u < 5 ? 4 * u + 3 : p4_issued(u - 4) - 1;
    if (u == 3 && p < p4_issued(1)) p = p4_issued(1);
    if ((u & 1) && u < 13) {
        const int c = (u + 1) >> 1;
        if (c >= 4 && p4_issued(2 * c - 6) + 6 > p) p = p4_issued(2 * c - 6) + 6;
    }
    return p4_issued(u) - 1 - p;
}
constexpr int p4_row(int u, int qq) { return u < 8 ? 56 * u + 8 * qq : 56 * ((4 * (u - 8) + qq) / 3) + 32 + 8 * ((4 * (u - 8) + qq) % 3); }

template <int N> DI void combine_stats(const u32x2 (&a)[N], const u32x2 (&b)[N], float (&mean)[N], float (&var)[N]) {
    float s[N], t[N], inv = 1.0f / 512.0f;
    asm volatile("" : "+s"(inv));
#pragma unroll
    for (int i = 0; i < N; ++i) { s[i] = __uint_as_float(a[i][0]) + __uint_as_float(b[i][0]); t[i] = __uint_as_float(a[i][1]) + __uint_as_float(b[i][1]); }
#pragma unroll
    for (int i = 0; i < N; ++i) { s[i] = sum16(s[i]); t[i] = sum16(t[i]); }
#pragma unroll
    for (int i = 0; i < N; ++i) { s[i] = sum32(s[i]); t[i] = sum32(t[i]); }
#pragma unroll
    for (int i = 0; i < N; ++i) { mean[i] = s[i] * inv; var[i] = fmaxf(t[i] * inv - mean[i] * mean[i], 0.f); }
}

__global__ __launch_bounds__(256, 2) void fnet_glu_kernel(const char* __restrict__ x, const char* __restrict__ wsre, const char* __restrict__ wsim,
        const char* __restrict__ wdre, const char* __restrict__ wdim_, const char* __restrict__ wg, const char* __restrict__ wu,
        const char* __restrict__ wdn, char* __restrict__ out, char* __restrict__ ws) {
    extern __shared__ __attribute__((aligned(16))) char smem[];
    LAS char* L = (LAS char*)smem;
    int wid;
    asm volatile("s_nop 1\n\tv_readfirstlane_b32 %0, %1\n\ts_nop 4" : "=s"(wid) : "v"(threadIdx.x >> 6));
    const int blk = blockIdx.x, xcd = blk & 7, idx = blk >> 3;

    const unsigned ldsb = (unsigned)(size_t)L;
    const rsrc_t rw = mk(ws, 48 * MB);
    const int T2 = 8 * (xcd >> 2) + (idx >> 2), E2 = idx & 3, j2 = xcd & 3;
    const int rt = 2 * T2 + (j2 >> 1), ct = 2 * E2 + (j2 & 1);
    const int rt1 = 4 * (xcd >> 1) + (idx >> 3), et = 8 * (xcd & 1) + (idx & 7);
    const int RT = 4 * (xcd & 1) + (idx >> 3), CT = 8 * (xcd >> 1) + (idx & 7);
    {
        u64 pc;
        int len;
        asm volatile("s_getpc_b64 %0\n1:\n\ts_mov_b32 %1, fnet_glu_code_end-1b" : "=s"(pc), "=s"(len));
        const rsrc_t rc = mk((const void*)pc, len > 0 ? (unsigned)len : 0u);
        const unsigned o = (unsigned)((idx * 4 + wid) & 7) * 8192u + lane_id() * 128u, l = ldsb + T_DUMMY + 768;
        asm volatile("s_mov_b32 m0, %0\n\ts_nop 0\n\tbuffer_load_dword %1, %2, 0 offen lds" :: "s"(l), "v"(o), "s"(rc) : "m0", "memory");
    }

    auto p1_rsrc = [&](unsigned bytes) { return mk(((et >> 3) ? wdim_ : wdre) + (size_t)(et & 7) * 64 * 1024, bytes); };
    auto dma_p1 = [&](int j, int p, rsrc_t r, unsigned v0, unsigned lw, unsigned sb) {
        dma(lw, (32 * j + p) * 1024, v0 ^ (16 * p), r, sb, (32 * j + p) * 1024);
    };
    auto p3_voff = [&](int ln, int up, int odd) -> unsigned {
        const unsigned c = (ln & 7) ^ ((2 * ((ln >> 4) & 1) + 4 * odd) ^ up);
        return (ln >> 3) * 3584 + 16 * (c > 6 ? 6 : c);
    };
    auto dma_p3 = [&](int j, int p, unsigned v, unsigned lw, unsigned sb) {
        dma(lw, j * REG + p * 1024, v, mk((j >> 1) ? wu : wg, D * F * 2), sb, (256 * (j & 1) + 8 * p) * 3584);
    };
    auto dma_p2n = [&](int c0, int c1) {
        const int w = opq_s(wid), ln = lane_id();
        const rsrc_t r = mk(((j2 >> 1) ? wsim : wsre) + ((size_t)128 * T2 * S + 1024 * (j2 & 1)) * 2, (128 * S - 1024 * (j2 & 1)) * 2);
        const unsigned vE = (ln >> 3) * 4096 + (((ln & 7) ^ (ln >> 4)) * 16), vO = vE ^ 64;
#pragma unroll
        for (int c = c0; c < c1; ++c)
#pragma unroll
            for (int p = 0; p < 4; ++p) dma(ldsb + w * 4096, (c & 7) * 16384 + p * 1024, (p & 1) ? vO : vE, r, 32 * w * 4096, p * 32768 + c * 128);
    };
    auto dma_p1_all = [&]() {
        const int w = opq_s(wid), ln = lane_id();
        const rsrc_t r = p1_rsrc(65536);
        const unsigned v0 = (unsigned)(ln * 16) ^ (128 * (w & 1)), lw = ldsb + w * 8192;
#pragma unroll
        for (int j = 0; j < 2; ++j)
#pragma unroll
            for (int p = 0; p < 8; ++p) dma_p1(j, p, r, v0, lw, w * 8192);
    };
    auto warm_ffn = [&]() {
        const int w = opq_s(wid), ln = lane_id();
        const unsigned o = blk * 7168u + (ln < 56 ? ln : 55) * 128u, l = ldsb + T_DUMMY + w * 256;
        auto touch = [&](rsrc_t r) {
            asm volatile("s_mov_b32 m0, %0\n\ts_nop 0\n\tbuffer_load_dword %1, %2, 0 offen lds" :: "s"(l), "v"(o), "s"(r) : "m0", "memory");
        };
        if (w == 0) touch(mk(wg, D * F * 2));
        if (w == 1) touch(mk(wu, D * F * 2));
        if (w == 2) touch(mk(wdn, D * F * 2));
    };
    dma_p1_all();

    u32x2 ev;
    aload2(ev, 0u, rw, 0u);
    u64 epoch = 0;
    bool dead = false;
    const bf16x8 ones = {0x3F80, 0x3F80, 0x3F80, 0x3F80, 0x3F80, 0x3F80, 0x3F80, 0x3F80};

        auto phase1 = [&](auto first_tag) {
            constexpr bool FIRST = decltype(first_tag)::value;
            constexpr int layer = FIRST ? 0 : 1;
            const int w = opq_s(wid), lane = lane_id(), i16 = lane & 15, q = lane >> 4;
            const int sbg = 8 * rt1 + 2 * w;
            bf16x8 xa[2][16];
            u32x2 sa[2], sb_[2];
            f32x4 acc[2][4];
#pragma unroll
            for (int s = 0; s < 2; ++s)
#pragma unroll
                for (int e = 0; e < 4; ++e) acc[s][e] = (f32x4){0.f, 0.f, 0.f, 0.f};
            unsigned wb[4];
#pragma unroll
            for (int k = 0; k < 4; ++k) wb[k] = i16 * 1024 + (((4 * k + q) ^ i16) * 16);
            LAS f32x2* tst = (LAS f32x2*)(L + LDS_M + w * 8192 + T_STAT);
            const float sgn = __int_as_float(opq_s((et >> 3) ? (int)0xbf800000 : 0x3f800000));
            f32x4 aS[2], aQ[2], aC = {0.f, 0.f, 0.f, 0.f};
#pragma unroll
            for (int s = 0; s < 2; ++s) { aS[s] = (f32x4){0.f, 0.f, 0.f, 0.f}; aQ[s] = aS[s]; }
            const unsigned wown = w * 16384;
            const unsigned vo = lane * 16, sbase = WS_X2 + sbg * 16384;
            if constexpr (FIRST) {
                const rsrc_t rx = mk(x, S * D * 2);
                bf16x8 xr[32];
#pragma unroll
                for (int r = 0; r < 32; ++r) aload(xr[r], vo, rx, sbg * 16384, r * 1024);
                LAS char* T0 = L + 2 * REG + w * 8192;
                sfor<2>([&](auto SB) {
                    constexpr int s = CI(SB);
                    sfor<4>([&](auto G) {
                        constexpr int g = CI(G), r0 = 16 * s + 4 * g;
                        wait4<28 - r0>(xr[r0], xr[r0 + 1], xr[r0 + 2], xr[r0 + 3]);
#pragma unroll
                        for (int r = 0; r < 4; ++r)
                            *(LAS bf16x8*)(T0 + ((4 * g + r) >> 3) * REG + ((4 * g + r) & 7) * 1024 + ((lane * 16) ^ ((4 * g + r) * 16))) = xr[r0 + r];
                    });
#pragma unroll
                    for (int ks = 0; ks < 16; ++ks)
                        xa[s][ks] = *(LAS bf16x8*)(T0 + (i16 >> 3) * REG + (i16 & 7) * 1024 + (((4 * ks + q) ^ i16) * 16));
                    wait_lds();
                });
                asm volatile("" : "+v"(ev));
                epoch = ((u64)(unsigned)__builtin_amdgcn_readfirstlane((int)ev[1]) << 32) | (unsigned)__builtin_amdgcn_readfirstlane((int)ev[0]);
                wait_lds();
                bar();
                dma_p2n(4, 8);
                warm_ffn();
            } else {
                poll8(rw, 5, blk_p24(2 * rt1 + (w >> 1), lane & 7), epoch + 6, dead);
#pragma unroll
                for (int s = 0; s < 2; ++s) {
                    const unsigned so = WS_ST + 2 * ST_BYTES + (sbg + s) * 1024;
                    aload2(sa[s], (2 * q * 16 + i16) * 8, rw, so);
                    aload2(sb_[s], ((2 * q + 1) * 16 + i16) * 8, rw, so);
                }
#pragma unroll
                for (int ks = 0; ks < P1_LA; ++ks)
#pragma unroll
                    for (int s = 0; s < 2; ++s) aload(xa[s][ks], vo, rw, sbase, s * 16384 + ks * 1024);
                wait2<p1_wait(0)>(xa[0][0], xa[1][0]);
                asm volatile("" : "+v"(sa[0]), "+v"(sa[1]), "+v"(sb_[0]), "+v"(sb_[1]));
            }
            bf16x8 wf[2][4], wo;
#pragma unroll
            for (int e = 0; e < 4; ++e) wf[0][e] = ldsA(L, wb[0] + e * 16384);
            sfor<16>([&](auto KS) {
                constexpr int ks = CI(KS);
                if constexpr (!FIRST && ks > 0) wait2<p1_wait(ks)>(xa[0][ks], xa[1][ks]);
                if constexpr (FIRST) wo = ldsA(L, wb[ks & 3] + wown + 256 * (ks >> 2));
                sfor<4>([&](auto E) {
                    constexpr int e = CI(E);
                    acc[0][e] = mfma(xa[0][ks], wf[ks & 1][e], acc[0][e]);
                    acc[1][e] = mfma(xa[1][ks], wf[ks & 1][e], acc[1][e]);
                    SCHED();
                    if constexpr (ks < 15) wf[(ks + 1) & 1][e] = ldsA(L, wb[(ks + 1) & 3] + e * 16384 + 256 * ((ks + 1) >> 2));
                    if constexpr (!FIRST && e >= 2 && ks + P1_LA < 16) aload(xa[e - 2][ks + P1_LA], vo, rw, sbase, (e - 2) * 16384 + (ks + P1_LA) * 1024);
                    SCHED();
                });
                if constexpr (!FIRST && ks == 15) {
                    float mean[2], var[2];
                    combine_stats<2>(sa, sb_, mean, var);
#pragma unroll
                    for (int s = 0; s < 2; ++s) tst[16 * s + i16] = (f32x2){mean[s], sgn * __builtin_amdgcn_rsqf(var[s] + EPS)};
                    SCHED();
                }
                if constexpr (FIRST) {
                    aC = mfma(ones, wo, aC);
#pragma unroll
                    for (int s = 0; s < 2; ++s) { aS[s] = mfma(ones, xa[s][ks], aS[s]); aQ[s] = mfma(xa[s][ks], xa[s][ks], aQ[s]); }
                    SCHED();
                }
            });
            if constexpr (FIRST) {
                ((LAS float*)(L + T_CS1))[16 * w + i16] = aC[0];
#pragma unroll
                for (int s = 0; s < 2; ++s) {
                    const int r3 = i16 & 3;
                    float dq = r3 == 0 ? aQ[s][0] : r3 == 1 ? aQ[s][1] : r3 == 2 ? aQ[s][2] : aQ[s][3];
                    dq = (q == (i16 >> 2)) ? dq : 0.f;
                    const float mean = aS[s][0] * (1.0f / 512.0f);
                    const float var = fmaxf(sumq(dq) * (1.0f / 512.0f) - mean * mean, 0.f);
                    tst[16 * s + i16] = (f32x2){mean, sgn * __builtin_amdgcn_rsqf(var + EPS)};
                }
            }
            wait_lds();
            bar();
            dma_p2n(0, 2);
            f32x4 st0[2], st1[2];
#pragma unroll
            for (int s = 0; s < 2; ++s) {
                const f32x4 a = *(LAS f32x4*)(tst + 16 * s + 4 * q), b = *(LAS f32x4*)(tst + 16 * s + 4 * q + 2);
                st0[s] = (f32x4){a[0], a[2], b[0], b[2]}; st1[s] = (f32x4){a[1], a[3], b[1], b[3]};
            }
#pragma unroll
            for (int E = 0; E < 2; ++E) {
                const float cs0 = ((LAS float*)(L + T_CS1))[32 * E + i16], cs1 = ((LAS float*)(L + T_CS1))[32 * E + 16 + i16];
#pragma unroll
                for (int s = 0; s < 2; ++s) {
                    f32x4 v0, v1;
#pragma unroll
                    for (int r = 0; r < 4; ++r) {
                        v0[r] = (acc[s][2 * E][r] - st0[s][r] * cs0) * st1[s][r];
                        v1[r] = (acc[s][2 * E + 1][r] - st0[s][r] * cs1) * st1[s][r];
                    }
                    auto a = __builtin_amdgcn_permlane16_swap(pk(v0[0], v0[1]), pk(v1[0], v1[1]), false, false);
                    auto b = __builtin_amdgcn_permlane16_swap(pk(v0[2], v0[3]), pk(v1[2], v1[3]), false, false);
                    store16((u32x4){a[0], b[0], a[1], b[1]}, rw,
                            WS_ABT + layer * 4 * MB + (et >> 3) * 2 * MB + ((2 * (et & 7) + E) * 128 + sbg + s) * 1024 + lane * 16);
                }
            }
        };


    phase1(std::true_type{});
    for (int layer = 0; layer < 2; ++layer) {
        const u64 tag = epoch + 1 + 6 * layer;
        auto finish_strip = [&](f32x4 (&v)[4], bool last, unsigned xo, unsigned sto) {
            const int w = opq_s(wid), lane = lane_id(), i16 = lane & 15, q = lane >> 4;
            float sum = 0.f, sq = 0.f;
            u32x2 res[4];
            {
                const u32x4 r0 = *(LAS u32x4*)(L + LDS_M + w * 8192 + T_RES + lane * 16), r1 = *(LAS u32x4*)(L + LDS_M + w * 8192 + T_RES + 1024 + lane * 16);
                res[0] = (u32x2){r0[0], r0[1]}; res[1] = (u32x2){r0[2], r0[3]}; res[2] = (u32x2){r1[0], r1[1]}; res[3] = (u32x2){r1[2], r1[3]};
            }
#pragma unroll
            for (int b = 0; b < 4; ++b) {
                res[b][0] = pk(lo16(res[b][0]) + v[b][0], hi16(res[b][0]) + v[b][1]);
                res[b][1] = pk(lo16(res[b][1]) + v[b][2], hi16(res[b][1]) + v[b][3]);
                const float x0 = lo16(res[b][0]), x1 = hi16(res[b][0]), x2 = lo16(res[b][1]), x3 = hi16(res[b][1]);
                sum += (x0 + x1) + (x2 + x3);
                sq += (x0 * x0 + x1 * x1) + (x2 * x2 + x3 * x3);
            }
            const u32x4 o0 = gather8(res[0][0], res[0][1], res[1][0], res[1][1]);
            const u32x4 o1 = gather8(res[2][0], res[2][1], res[3][0], res[3][1]);
            if (last) {
                const rsrc_t rxo = mk(out, S * D * 2);
                const unsigned a = (64 * rt + 16 * w + i16) * 1024 + (64 * ct + 8 * q) * 2;
                __builtin_amdgcn_raw_buffer_store_b128(o0, rxo, a, 0, 0);
                __builtin_amdgcn_raw_buffer_store_b128(o1, rxo, a + 64, 0, 0);
            } else {
                const unsigned a = xo + ((4 * rt + w) * 16 + 2 * ct) * 1024 + lane * 16;
                store16(o0, rw, a); store16(o1, rw, a + 1024);
                *(LAS u32x4*)(L + LDS_M + w * 8192 + T_RES + lane * 16) = (u32x4){res[0][0], res[0][1], res[1][0], res[1][1]};
                *(LAS u32x4*)(L + LDS_M + w * 8192 + T_RES + 1024 + lane * 16) = (u32x4){res[2][0], res[2][1], res[3][0], res[3][1]};
                sum = sum16(sum); sq = sum16(sq);
                sum = sum32(sum); sq = sum32(sq);
                __builtin_amdgcn_raw_buffer_store_b64((u32x2){__float_as_uint(sum), __float_as_uint(sq)}, rw, sto + (((4 * rt + w) * 8 + ct) * 16 + i16) * 8, 0, 16);
            }
        };

        {
            const int w = opq_s(wid), lane = lane_id(), i16 = lane & 15, q = lane >> 4;
            const int part = j2 >> 1, half = j2 & 1, r32 = lane & 31, hh = lane >> 5;
            const rsrc_t rws = mk((part ? wsim : wsre) + ((size_t)128 * T2 * S + 1024 * half) * 2, (128 * S - 1024 * half) * 2);
            const unsigned vsE = (lane >> 3) * 4096 + (((lane & 7) ^ (lane >> 4)) * 16), vsO = vsE ^ 64;
            const int w4_ = opq_s(wid);
            const unsigned lw4 = ldsb + w4_ * 4096, wsb = 32 * w4_ * 4096;
            auto dma_p2 = [&](int c, int p) { dma(lw4, (c & 7) * 16384 + p * 1024, (p & 1) ? vsO : vsE, rws, wsb, p * 32768 + c * 128); };
            wait_vm<0>();
            bar();
            publish(rw, 6 * layer + 0, blk, tag, w);
            dma_p2n(2, 4);
            poll8(rw, 6 * layer + 0, blk_p1(8 * half + (lane & 7), 8 * part + 2 * E2 + (w >> 1)), tag, dead);
            const unsigned so0 = WS_ABT + layer * 4 * MB + part * 2 * MB + ((4 * E2 + w) * 128 + 64 * half) * 1024, vo = lane * 16;
            bf16x8 act[P2_LA + 2];
            auto load_act = [&](int v) { aload(act[v % (P2_LA + 2)], vo, rw, so0, v * 1024); };
#pragma unroll
            for (int v = 0; v < P2_LA; ++v) load_act(v);
            f32x16 acc[4];
#pragma unroll
            for (int t = 0; t < 4; ++t)
#pragma unroll
                for (int r = 0; r < 16; ++r) acc[t][r] = 0.f;
            unsigned rb[2][4];
#pragma unroll
            for (int k = 0; k < 4; ++k) { rb[0][k] = r32 * 128 + (((2 * k + hh) ^ ((r32 >> 1) & 7)) * 16); rb[1][k] = rb[0][k] + 65536; }
            auto ws_frag = [&](int n) {
                const int v = n >> 2, t = n & 3, c = v >> 2;
                return ldsA(L, rb[(c & 7) >> 2][v & 3] + (c & 3) * 16384 + t * 4096);
            };
            const int ws_ = opq_s(wid);
            const unsigned lw = ldsb + ws_ * 8192, g_sb = 64 * ws_ * 3584 + 112 * CT;
            unsigned gg0 = 0, gg1 = 0;
            bf16x8 wf[8];
#pragma unroll
            for (int n = 0; n < 6; ++n) wf[n] = ws_frag(n);
            sfor<64>([&](auto V) {
                constexpr int v = CI(V);
                asm volatile("s_waitcnt vmcnt(%1)" : "+v"(act[v % (P2_LA + 2)]) : "i"(p2_wait(v)) : "memory");
                if constexpr (v == 43) { const int l2 = lane_id(); gg0 = p3_voff(l2, 0, 0); gg1 = p3_voff(l2, 0, 1); }
                sfor<4>([&](auto TB) {
                    constexpr int t = CI(TB), n = 4 * v + t;
                    acc[t] = mfma32(act[v % (P2_LA + 2)], wf[n & 7], acc[t]);
                    SCHED();
                    if constexpr ((v & 3) == 2 && t == 2 && v < 60) bar();
                    if constexpr (n + 6 < 256) wf[(n + 6) & 7] = ws_frag(n + 6);
                    if constexpr (t == 1 && p2_dma(v)) {
                        if constexpr (v <= 39) dma_p2((v >> 2) + 6, v & 3);
                        else dma_p3(0, v - 43, ((v - 43) & 1) ? gg1 : gg0, lw, g_sb);
                    }
                    if constexpr (t == 3 && v + P2_LA < 64) load_act(v + P2_LA);
                    SCHED();
                });
            });
            const unsigned pgrp = WS_PART + layer * 8 * MB + (T2 * 4 + E2) * 131072;
            const int lx = lane_id(), i16x = lx & 15, tup = (lx >> 4) & 1, hx = lx >> 5;
#pragma unroll
            for (int a = 0; a < 2; ++a) {
                const int owner = 2 * a + (w >> 1);
                if (owner != j2) {
                    const unsigned po = pgrp + owner * 32768 + j2 * 8192 + (w & 1) * 1024 + tup * 2048 + (i16x + 16 * hx) * 16;
#pragma unroll
                    for (int u = 0; u < 2; ++u)
#pragma unroll
                        for (int qh = 0; qh < 2; ++qh) {
                            const f32x16& c = acc[2 * a + u];
                            store16((u32x4){pk(c[4 * qh], c[4 * qh + 1]), pk(c[4 * qh + 2], c[4 * qh + 3]),
                                            pk(c[8 + 4 * qh], c[8 + 4 * qh + 1]), pk(c[8 + 4 * qh + 2], c[8 + 4 * qh + 3])}, rw, po + u * 4096 + qh * 512);
                        }
                }
            }
#pragma unroll
            for (int a = 0; a < 2; ++a) {
                if (2 * a + (w >> 1) == j2) {
#pragma unroll
                    for (int u = 0; u < 2; ++u)
#pragma unroll
                        for (int g = 0; g < 4; ++g) {
                            const f32x16& c = acc[2 * a + u];
                            *(LAS f32x4*)(L + LDS_M + (2 * u + tup) * 8192 + (2 * (w & 1) + (g >> 1)) * 1024 + (i16x + 16 * (2 * (g & 1) + hx)) * 16) =
                                (f32x4){c[4 * g], c[4 * g + 1], c[4 * g + 2], c[4 * g + 3]};
                        }
                }
            }
            wait_vm<0>();
            wait_lds();
            bar();
            publish(rw, 6 * layer + 1, blk, tag + 1, w);
            const unsigned gu0 = p3_voff(lx, 1, 0), gu1 = p3_voff(lx, 1, 1), vx = lx * 16;
#pragma unroll
            for (int n = 0; n < 8; ++n) dma_p3(1, n, (n & 1) ? gg1 : gg0, lw, g_sb);
#pragma unroll
            for (int n = 0; n < 3; ++n) dma_p3(2, n, (n & 1) ? gu1 : gu0, lw, g_sb);
            if (layer == 0) {
                u32x2 res0[4];
                const rsrc_t rx = mk(x, S * D * 2);
                const unsigned vr = (64 * rt + 16 * w + i16x) * 1024 + (64 * ct + 4 * (lx >> 4)) * 2;
#pragma unroll
                for (int b = 0; b < 4; ++b) aload2(res0[b], vr + b * 32, rx, 0u);
                asm volatile("s_waitcnt vmcnt(0)" : "+v"(res0[0]), "+v"(res0[1]), "+v"(res0[2]), "+v"(res0[3]) :: "memory");
                *(LAS u32x4*)(L + LDS_M + w * 8192 + T_RES + vx) = (u32x4){res0[0][0], res0[0][1], res0[1][0], res0[1][1]};
                *(LAS u32x4*)(L + LDS_M + w * 8192 + T_RES + 1024 + vx) = (u32x4){res0[2][0], res0[2][1], res0[3][0], res0[3][1]};
            }
            poll8(rw, 6 * layer + 1, (blk & ~3) | (lx & 3), tag + 1, dead);
            bf16x8 pv[3][2];
#pragma unroll
            for (int d = 1; d < 4; ++d)
#pragma unroll
                for (int h = 0; h < 2; ++h) aload(pv[d - 1][h], vx, rw, pgrp + j2 * 32768 + ((j2 + d) & 3) * 8192 + w * 2048, h * 1024);
            f32x4 v[4];
#pragma unroll
            for (int b = 0; b < 4; ++b) v[b] = *(LAS f32x4*)(L + LDS_M + w * 8192 + b * 1024 + vx);
            asm volatile("s_waitcnt vmcnt(0)" : "+v"(pv[0][0]), "+v"(pv[0][1]), "+v"(pv[1][0]), "+v"(pv[1][1]), "+v"(pv[2][0]), "+v"(pv[2][1]) :: "memory");
#pragma unroll
            for (int n = 3; n < 8; ++n) dma_p3(2, n, (n & 1) ? gu1 : gu0, lw, g_sb);
#pragma unroll
            for (int d = 0; d < 3; ++d)
#pragma unroll
                for (int h = 0; h < 2; ++h) {
                    const u32x4 p = __builtin_bit_cast(u32x4, pv[d][h]);
                    v[2 * h] += (f32x4){lo16(p[0]), hi16(p[0]), lo16(p[1]), hi16(p[1])};
                    v[2 * h + 1] += (f32x4){lo16(p[2]), hi16(p[2]), lo16(p[3]), hi16(p[3])};
                }
            finish_strip(v, false, WS_X1 + layer * 2 * MB, WS_ST + layer * ST_BYTES);
            wait_vm<0>();
            bar();
            publish(rw, 6 * layer + 2, blk, tag + 2, w);
#pragma unroll
            for (int n = 0; n < 8; ++n) dma_p3(3, n, (n & 1) ? gu1 : gu0, lw, g_sb);
        }

        {
            const int w = opq_s(wid), lane = lane_id(), i16 = lane & 15, q = lane >> 4;
            poll8(rw, 6 * layer + 2, blk_p24(4 * RT + w, lane & 7), tag + 2, dead);
            if (layer == 0 && blk == 0 && w == 0) {
                u64 e2 = epoch; asm volatile("" : "+s"(e2)); e2 += 64;
                __builtin_amdgcn_raw_buffer_store_b64((u32x2){(unsigned)e2, (unsigned)(e2 >> 32)}, rw, 0, 0, 16);
                wait_vm<0>();
            }
            const int sb0 = 16 * RT + 4 * w;
            u32x2 sa[4], sb_[4];
            bf16x8 act[P3_LA + 1][4];
            const unsigned vo = lane * 16, xo = WS_X1 + layer * 2 * MB + sb0 * 16384;
            auto load_x = [&](int g, int t) { aload(act[g % (P3_LA + 1)][t], vo, rw, xo, t * 16384 + g * 1024); };
#pragma unroll
            for (int g = 0; g < P3_LA; ++g)
#pragma unroll
                for (int t = 0; t < 4; ++t) load_x(g, t);
            bar();
            const int qq = (lane >> 2) & 3, p4 = lane & 3;
            unsigned tb_[7];
            {
                const unsigned f = 2 * (qq >> 1) + 4 * (q & 1), rowb = (8 * q + qq) * 128 + 8 * (p4 & 1);
#pragma unroll
                for (int b = 0; b < 6; ++b) tb_[b] = (b & 1) * 65536 + rowb + (((2 * (b >> 1) + (p4 >> 1)) ^ f ^ (b & 1)) * 16);
                tb_[6] = (p4 >> 1) * 65536 + rowb + ((6 ^ f ^ (p4 >> 1)) * 16);
            }
            if (layer == 0) {
                f32x4 ac[7];
#pragma unroll
                for (int b = 0; b < 7; ++b) ac[b] = (f32x4){0.f, 0.f, 0.f, 0.f};
#pragma unroll
                for (int k = 0; k < 4; ++k)
#pragma unroll
                    for (int b = 0; b < 7; ++b) ac[b] = mfma(ldsT(L, tb_[b] + w * 16384 + k * 4096), ones, ac[b]);
#pragma unroll
                for (int b = 0; b < 7; ++b) *(LAS f32x4*)(L + T_CS3P + ((w * 7 + b) * 4 + q) * 16) = ac[b];
                wait_lds();
                bar();
                if (w == 0) {
                    const int l = lane < 28 ? lane : 27;
                    f32x4 s = *(LAS f32x4*)(L + T_CS3P + l * 16);
#pragma unroll
                    for (int k = 1; k < 4; ++k) s += *(LAS f32x4*)(L + T_CS3P + (k * 28 + l) * 16);
                    *(LAS f32x4*)(L + T_CS3 + l * 16) = s;
                }
            }
            auto w_lo = [&](int ks, int b) { return __builtin_amdgcn_ds_read_tr16_b64_v4i16((LAS s16x4*)(L + tb_[b] + ks * 4096)); };
            auto w_hi = [&](int ks, int b) { return __builtin_amdgcn_ds_read_tr16_b64_v4i16((LAS s16x4*)(L + tb_[b] + ks * 4096 + 512)); };
            const unsigned sto = WS_ST + layer * ST_BYTES + sb0 * 1024, sv0 = (2 * q * 16 + i16) * 8;
            f32x4 a3[7][4];
#pragma unroll
            for (int b = 0; b < 7; ++b)
#pragma unroll
                for (int t = 0; t < 4; ++t) a3[b][t] = (f32x4){0.f, 0.f, 0.f, 0.f};
            bf16x8 wf[7];
#pragma unroll
            for (int b = 0; b < 6; ++b) wf[b] = ldsT(L, tb_[b]);
            sfor<16>([&](auto KS) {
                constexpr int ks = CI(KS), sl = ks % (P3_LA + 1);
                wait4<p3_wait(ks)>(act[sl][0], act[sl][1], act[sl][2], act[sl][3]);
                sfor<7>([&](auto B) {
                    constexpr int b = CI(B), pb = b == 0 ? 6 : b - 1, pks = b == 0 ? ks : ks + 1;
                    s16x4 lo, hi;
                    a3[b][0] = mfma(wf[b], act[sl][0], a3[b][0]);
                    SCHED();
                    if constexpr (pks < 16) lo = w_lo(pks, pb);
                    SCHED();
                    a3[b][1] = mfma(wf[b], act[sl][1], a3[b][1]);
                    SCHED();
                    if constexpr (pks < 16) { hi = w_hi(pks, pb); wf[pb] = __builtin_shufflevector(lo, hi, 0, 1, 2, 3, 4, 5, 6, 7); }
                    SCHED();
                    a3[b][2] = mfma(wf[b], act[sl][2], a3[b][2]);
                    SCHED();
                    if constexpr (b >= 1 && b <= 4 && ks + P3_LA < 16) load_x(ks + P3_LA, b - 1);
                    SCHED();
                    a3[b][3] = mfma(wf[b], act[sl][3], a3[b][3]);
                    SCHED();
                    if constexpr (ks == 8 && b == 6) {
#pragma unroll
                        for (int t = 0; t < 4; ++t) { aload2(sa[t], sv0, rw, sto + t * 1024); aload2(sb_[t], sv0 + 128, rw, sto + t * 1024); }
                        SCHED();
                    }
                });
            });
            wait_lds();
            bar();

            const int l2 = lane_id();
            const unsigned vE = (l2 >> 3) * 1024 + (((l2 & 7) ^ ((l2 >> 3) & 2)) * 16), vO = vE ^ 64;
            const int ws_ = opq_s(wid);
            const unsigned lw = ldsb + ws_ * 8192, dsb = 448 * ws_ * 1024;
            const rsrc_t rwd = mk(wdn + 128 * ct, F * D * 2 - 128 * ct);
            auto dma_p4 = [&](int c, int p) {
                dma(lw, ((c + 1) & 3) * REG + p * 1024, (p & 1) ? vO : vE, rwd, dsb, p4_row(2 * c + (p >> 2), p & 3) * 1024);
            };
            float mean[4], sig[4], kk_[4];
            asm volatile("" : "+v"(sa[0]), "+v"(sa[1]), "+v"(sa[2]), "+v"(sa[3]), "+v"(sb_[0]), "+v"(sb_[1]), "+v"(sb_[2]), "+v"(sb_[3]));
            {
                float v[4];
                combine_stats<4>(sa, sb_, mean, v);
                float l2e = 1.44269504088896f;
                asm volatile("" : "+s"(l2e));
#pragma unroll
                for (int t = 0; t < 4; ++t) {
                    const float vv = v[t] + EPS, r = __builtin_amdgcn_rsqf(vv);
                    sig[t] = vv * r; kk_[t] = l2e * r;
                }
            }
            auto glu = [&](float G, float U, float cg, float cu, float m, float sg, float kk) {
                const float e = __builtin_amdgcn_exp2f(kk * (m * cg - G));
                return (U - m * cu) * __builtin_amdgcn_rcpf(sg * e + sg);
            };
            const unsigned ho = WS_H + layer * 7 * MB, hw = 14 * (CT >> 3);
            {
                u32x4 o[4];
                const f32x4 cg0 = *(LAS f32x4*)(L + T_CS3 + (0 * 4 + q) * 16), cu0 = *(LAS f32x4*)(L + T_CS3 + (1 * 4 + q) * 16);
                const f32x4 cg1 = *(LAS f32x4*)(L + T_CS3 + (2 * 4 + q) * 16), cu1 = *(LAS f32x4*)(L + T_CS3 + (3 * 4 + q) * 16);
                sfor<4>([&](auto T) {
                    constexpr int t = CI(T);
                    float h0[4], h1[4];
                    sfor<4>([&](auto R) {
                        constexpr int r = CI(R);
                        h0[r] = glu(a3[0][t][r], a3[1][t][r], cg0[r], cu0[r], mean[t], sig[t], kk_[t]);
                        h1[r] = glu(a3[2][t][r], a3[3][t][r], cg1[r], cu1[r], mean[t], sig[t], kk_[t]);
                        asm volatile("" : "+v"(h0[r]), "+v"(h1[r]));
                        dma_p4(t >> 1, 4 * (t & 1) + r);
                        SCHED();
                    });
                    o[t] = gather8(pk(h0[0], h0[1]), pk(h0[2], h0[3]), pk(h1[0], h1[1]), pk(h1[2], h1[3]));
                });
#pragma unroll
                for (int t = 0; t < 4; ++t) store16(o[t], rw, ho + ((sb0 + t) * 56 + hw + (CT & 7)) * 1024 + vo);
                SCHED();
            }
            {
                const f32x4 cg = *(LAS f32x4*)(L + T_CS3 + (4 * 4 + q) * 16), cu = *(LAS f32x4*)(L + T_CS3 + (5 * 4 + q) * 16);
                const f32x4 mg = *(LAS f32x4*)(L + T_CS3 + (6 * 4 + (q & 1)) * 16), mu = *(LAS f32x4*)(L + T_CS3 + (6 * 4 + 2 + (q & 1)) * 16);
                unsigned H[4][2], M[2][2];
                sfor<4>([&](auto T) {
                    constexpr int t = CI(T);
                    float h[4];
                    if constexpr (t == 2) {
                        wait_vm<8>();
                        bar();
                        publish(rw, 6 * layer + 3, blk, tag + 3, w);
                        SCHED();
                    }
#pragma unroll
                    for (int r = 0; r < 4; ++r) h[r] = glu(a3[4][t][r], a3[5][t][r], cg[r], cu[r], mean[t], sig[t], kk_[t]);
                    H[t][0] = pk(h[0], h[1]); H[t][1] = pk(h[2], h[3]);
                    asm volatile("" : "+v"(H[t][0]), "+v"(H[t][1]));
#pragma unroll
                    for (int r = 0; r < 4; ++r) dma_p4(2 + (t >> 1), 4 * (t & 1) + r);
                    SCHED();
                });
#pragma unroll
                for (int p = 0; p < 2; ++p) {
                    const float m = q < 2 ? mean[2 * p] : mean[2 * p + 1], sg = q < 2 ? sig[2 * p] : sig[2 * p + 1], kk = q < 2 ? kk_[2 * p] : kk_[2 * p + 1];
                    float h[4];
#pragma unroll
                    for (int r = 0; r < 4; ++r) {
                        auto sw = __builtin_amdgcn_permlane32_swap(__float_as_uint(a3[6][2 * p][r]), __float_as_uint(a3[6][2 * p + 1][r]), false, false);
                        h[r] = glu(__uint_as_float(sw[0]), __uint_as_float(sw[1]), mg[r], mu[r], m, sg, kk);
                    }
                    M[p][0] = pk(h[0], h[1]); M[p][1] = pk(h[2], h[3]);
                }
                const unsigned be = 3 * (CT & 7) + (q & 1), bm = 3 * (CT & 7) + 2;
                const unsigned hb = ho + ((sb0 + (q >> 1)) * 56 + hw + 8 + (be >> 2)) * 1024 + ((be & 3) * 16 + i16) * 16;
                store16(gather8(H[0][0], H[0][1], H[1][0], H[1][1]), rw, hb);
                store16(gather8(H[2][0], H[2][1], H[3][0], H[3][1]), rw, hb + 2 * 56 * 1024);
                store16(gather8(M[0][0], M[0][1], M[1][0], M[1][1]), rw, ho + ((sb0 + q) * 56 + hw + 8 + (bm >> 2)) * 1024 + ((bm & 3) * 16 + i16) * 16);
                SCHED();
            }

            poll8(rw, 6 * layer + 3, blk_p3(rt >> 2, 8 * w + (lane_id() & 7)), tag + 3, dead);
            bar();
            publish(rw, 6 * layer + 4, blk, tag + 4, w);
            unsigned so[4];
#pragma unroll
            for (int d = 0; d < 4; ++d) so[d] = ho + ((4 * rt + ((ws_ + d) & 3)) * 56 + 14 * ws_) * 1024;
            bf16x8 hact[6][4];
            auto load_h = [&](int u, int d) { aload(hact[u % 6][d], vo, rw, so[d], u * 1024); };
#pragma unroll
            for (int u = 0; u < 5; ++u)
#pragma unroll
                for (int d = 0; d < 4; ++d) load_h(u, d);
            f32x4 acc[4][4];
#pragma unroll
            for (int b = 0; b < 4; ++b)
#pragma unroll
                for (int d = 0; d < 4; ++d) acc[b][d] = (f32x4){0.f, 0.f, 0.f, 0.f};
            unsigned wb_[2][4];
            const int l3 = lane_id(), q3 = l3 >> 4, qq3 = (l3 >> 2) & 3, p3 = l3 & 3;
#pragma unroll
            for (int b = 0; b < 4; ++b) {
                wb_[0][b] = w * 8192 + (8 * q3 + qq3) * 128 + (((2 * b + (p3 >> 1)) ^ ((qq3 & 2) + 4 * (q3 & 1))) * 16) + 8 * (p3 & 1);
                wb_[1][b] = wb_[0][b] + 2 * REG;
            }
            auto wd_off = [&](int u, int b) { return wb_[(((u >> 1) + 1) & 3) >> 1][b] + (((u >> 1) + 1) & 1) * REG + (u & 1) * 4096; };
            bf16x8 wf4[2][4];
#pragma unroll
            for (int b = 0; b < 4; ++b) wf4[0][b] = ldsT(L, wd_off(0, b));
            u32x2 flb;
            sfor<14>([&](auto U) {
                constexpr int u = CI(U);
                wait4<p4_wait(u)>(hact[u % 6][0], hact[u % 6][1], hact[u % 6][2], hact[u % 6][3]);
                if constexpr (u == 1) {
                    aload2(flb, (unsigned)blk_p3(rt >> 2, 8 * w + (lane_id() & 7)) * 256u, rw, WS_FLAGS + (6 * layer + 4) * 65536u);
                    SCHED();
                }
                if constexpr (u == 3) {
                    asm volatile("" : "+v"(flb));
                    if (!__all((((u64)flb[1] << 32) | flb[0]) == tag + 4)) poll8(rw, 6 * layer + 4, blk_p3(rt >> 2, 8 * w + (lane_id() & 7)), tag + 4, dead);
                    SCHED();
                }
                sfor<8>([&](auto J) {
                    constexpr int j = CI(J), b = j >> 1, d0 = 2 * (j & 1);
                    acc[b][d0] = mfma(wf4[u & 1][b], hact[u % 6][d0], acc[b][d0]);
                    acc[b][d0 + 1] = mfma(wf4[u & 1][b], hact[u % 6][d0 + 1], acc[b][d0 + 1]);
                    SCHED();
                    if constexpr (j < 4 && u < 13) wf4[(u + 1) & 1][j] = ldsT(L, wd_off(u + 1, j));
                    if constexpr ((j & 1) == 0) {
                        constexpr int p = 4 * ((u + 1) & 1) + (j >> 1);
                        if constexpr (u >= 1 && u <= 6) dma_p4(((u - 1) >> 1) + 4, p);
                    } else if constexpr (u <= 8) load_h(u + 5, j >> 1);
                    SCHED();
                });
            });
            if (layer == 0) dma_p1_all();
            LAS char* x3 = L + w * 8192 + lane * 16 + 3 * REG;
            LAS char* xm = L + w * 8192 + lane * 16 + LDS_M;
            asm volatile("" : "+v"(x3), "+v"(xm));
#pragma unroll
            for (int d = 1; d < 4; ++d)
#pragma unroll
                for (int b = 0; b < 4; ++b)
                    *(LAS f32x4*)((d < 3 ? x3 : xm) + (d < 3 ? (d - 1) * 4096 : 0) + b * 1024) = acc[b][d];
            wait_lds();
            bar();
            if (layer == 0) dma_p2n(4, 6);
            f32x4 v[4], t[3][4];
#pragma unroll
            for (int d = 1; d < 4; ++d) {
                LAS char* src = L + ((w - d) & 3) * 8192 + lane * 16 + (d < 3 ? 3 * REG + (d - 1) * 4096 : LDS_M);
                asm volatile("" : "+v"(src));
#pragma unroll
                for (int b = 0; b < 4; ++b) t[d - 1][b] = *(LAS f32x4*)(src + b * 1024);
            }
            SCHED();
#pragma unroll
            for (int b = 0; b < 4; ++b) v[b] = ((acc[b][0] + t[0][b]) + t[1][b]) + t[2][b];
            if (layer == 0) {
                finish_strip(v, false, WS_X2, WS_ST + 2 * ST_BYTES);
                wait_vm<0>();
                bar();
                publish(rw, 5, blk, tag + 5, w);
                dma_p2n(6, 8);
            } else {
                finish_strip(v, true, 0, 0);
            }
        }
        if (layer == 1) break;
        phase1(std::false_type{});
    }
    asm volatile("fnet_glu_code_end:");
}

}

extern "C" void kernel_launch(void* const* d_in, const int* in_sizes, int n_in,
                              void* d_out, int out_size, void* d_ws, size_t ws_size,
                              hipStream_t stream) {
    (void)in_sizes; (void)n_in; (void)out_size; (void)ws_size;
    (void)hipFuncSetAttribute((const void*)fn::fnet_glu_kernel, hipFuncAttributeMaxDynamicSharedMemorySize, fn::LDS_BYTES);
    fn::fnet_glu_kernel<<<256, 256, fn::LDS_BYTES, stream>>>((const char*)d_in[0], (const char*)d_in[1], (const char*)d_in[2],
        (const char*)d_in[3], (const char*)d_in[4], (const char*)d_in[6], (const char*)d_in[7], (const char*)d_in[8],
        (char*)d_out, (char*)d_ws);
}
```
